```python
import torch

def _softmax(x, axis=-1):
    return torch.softmax(x, dim=axis)
def _gelu(x):
    return torch.nn.functional.gelu(x, approximate="tanh")
def _silu(x):
    return torch.nn.functional.silu(x)
def _sigmoid(x):
    return torch.sigmoid(x)
def _layernorm(x, axis=-1, eps=1e-5):
    m = x.mean(dim=axis, keepdim=True)
    v = x.var(dim=axis, keepdim=True, unbiased=False)
    return (x - m) / torch.sqrt(v + eps)
def _scatter_add(tgt, idx, src, axis=0):
    return tgt.clone().index_add_(axis, idx, src)
def _segment_sum(src, seg, n):
    out = torch.zeros((n,) + src.shape[1:], dtype=src.dtype)
    return out.index_add_(0, seg, src)
def _pad(x, pw):
    flat = tuple(v for pair in reversed(pw) for v in pair)
    return torch.nn.functional.pad(x, flat)

kernel_name = 'recurrent_scan__gqa_block_cached__mixer_channel_mlp__decode'

def setup_inputs():

    _w = (lambda a: (lambda t: t.to(torch.bfloat16) if t.is_floating_point() else t)(torch.as_tensor(a)))
    def _A_setup():
        B, Sq, D = 2, 1, 512
        Hq, Hkv, Sk, Dh = 32, 8, 8192, 128
        assert Hq % Hkv == 0, (Hq, Hkv)
        return {
            "x": _w((lambda *s: torch.randn(*s, dtype=torch.bfloat16) * 0.1)(B, Sq, D)),
            "k_cache": _w((lambda *s: torch.randn(*s, dtype=torch.bfloat16) * 0.8)(B, Hkv, Sk, Dh)),
            "v_cache": _w((lambda *s: torch.randn(*s, dtype=torch.bfloat16) * 3.2)(B, Hkv, Sk, Dh)),
            "wq": _w((lambda *s: torch.randn(*s, dtype=torch.bfloat16) * 0.8)(D, Hq * Dh) * (D ** -0.5)),
            "wo": _w((lambda *s: torch.randn(*s, dtype=torch.bfloat16) * 3.2)(Hq * Dh, D) * ((Hq * Dh) ** -0.5)),
        }
    _A_in = _A_setup()

    def _B_setup():
        B, S, D, F = 2, 1, 512, 1792
        return {
            "x": _w((lambda *s: torch.randn(*s, dtype=torch.bfloat16) * 0.1)(B, S, D)),
            "w1": _w((lambda *s: torch.randn(*s, dtype=torch.bfloat16) * 3.2)(D, F) * (D ** -0.5)),
            "b1": _w((lambda *s: torch.randn(*s, dtype=torch.bfloat16) * 3.2)(F)),
            "w2": _w((lambda *s: torch.randn(*s, dtype=torch.bfloat16) * 3.2)(F, D) * (F ** -0.5)),
            "b2": _w((lambda *s: torch.randn(*s, dtype=torch.bfloat16) * 3.2)(D)),
        }
    _B_in = _B_setup()

    return {
        "A_x": _A_in["x"],
        "A_k_cache": _A_in["k_cache"],
        "A_v_cache": _A_in["v_cache"],
        "A_wq": _A_in["wq"],
        "A_wo": _A_in["wo"],
        "B_x": _B_in["x"],
        "B_w1": _B_in["w1"],
        "B_b1": _B_in["b1"],
        "B_w2": _B_in["w2"],
        "B_b2": _B_in["b2"],
    }

def reference(A_x, A_k_cache, A_v_cache, A_wq, A_wo, B_x, B_w1, B_b1, B_w2, B_b2):
    def _A_ref(x, k_cache, v_cache, wq, wo):
        B, Sq, D = x.shape
        Hq, Dh = 32, 128
        Hkv = k_cache.shape[1]
        rep = Hq // Hkv
        q = (lambda x, *d: x.permute(*d))((x @ wq).reshape(B, Sq, Hq, Dh), 0, 2, 1, 3)
        k = (lambda x, n, axis: torch.repeat_interleave(x, n, dim=axis))(k_cache, rep, axis=1)
        v = (lambda x, n, axis: torch.repeat_interleave(x, n, dim=axis))(v_cache, rep, axis=1)
        scores = torch.einsum("bhqd,bhkd->bhqk", q, k) * (Dh ** -0.5)
        p = _softmax(scores, axis=-1)
        o = torch.einsum("bhqk,bhkd->bhqd", p, v)
        return (lambda x, *d: x.permute(*d))(o, 0, 2, 1, 3).reshape(B, Sq, Hq * Dh) @ wo

    def _B_ref(x, w1, b1, w2, b2):
        y = _layernorm(x, axis=-1)
        h = _gelu(torch.einsum("bsd,df->bsf", y, w1) + b1)
        return torch.einsum("bsf,fd->bsd", h, w2) + b2

    x = A_x
    xn = _layernorm(x, axis=-1)
    s = _A_ref(xn, A_k_cache, A_v_cache, A_wq, A_wo)
    g = _silu(_B_ref(xn, B_w1, B_b1, B_w2, B_b2))
    return x + s * g
```

```cpp
#include <hip/hip_runtime.h>
#include <hip/hip_bf16.h>
#include <stdint.h>

typedef unsigned short u16;
__device__ __forceinline__ float bf2f(u16 b) { return __builtin_bit_cast(float, (unsigned)b << 16); }
__device__ __forceinline__ float rbf(float f) { return __bfloat162float(__float2bfloat16(f)); }

constexpr int B = 2, D = 512, HQ = 32, HKV = 8, SK = 8192, DH = 128, F = 1792, NS = 16;

__device__ void ln_row(const float* in, float* out, float* red) {
  const int t = threadIdx.x;
  float s = 0.f;
  for (int i = t; i < D; i += blockDim.x) s += in[i];
  red[t] = s; __syncthreads();
  for (int o = blockDim.x / 2; o > 0; o >>= 1) { if (t < o) red[t] += red[t + o]; __syncthreads(); }
  const float mf = red[0] / D; __syncthreads();
  float v = 0.f;
  for (int i = t; i < D; i += blockDim.x) { float d = in[i] - mf; v += d * d; }
  red[t] = v; __syncthreads();
  for (int o = blockDim.x / 2; o > 0; o >>= 1) { if (t < o) red[t] += red[t + o]; __syncthreads(); }
  const float var = rbf(red[0] / D); __syncthreads();
  const float m = rbf(mf);
  const float sd = rbf(sqrtf(rbf(var + 1e-5f)));
  for (int i = t; i < D; i += blockDim.x) out[i] = rbf(rbf(in[i] - m) / sd);
  __syncthreads();
}

__global__ void k_pre(const u16* x, const u16* wq, const u16* w1, const u16* b1, float* q, float* h) {
  __shared__ float xs[B][D], xn[B][D], y[B][D], red[256];
  for (int i = threadIdx.x; i < B * D; i += 256) xs[i / D][i % D] = bf2f(x[i]);
  __syncthreads();
  for (int b = 0; b < B; ++b) { ln_row(xs[b], xn[b], red); ln_row(xn[b], y[b], red); }
  const int n = blockIdx.x * 256 + threadIdx.x;
  if (n < HQ * DH) {
    float a0 = 0.f, a1 = 0.f;
    for (int k = 0; k < D; ++k) { float w = bf2f(wq[(size_t)k * HQ * DH + n]); a0 += xn[0][k] * w; a1 += xn[1][k] * w; }
    q[n] = rbf(a0); q[HQ * DH + n] = rbf(a1);
  } else if (n < HQ * DH + F) {
    const int f = n - HQ * DH;
    float a0 = 0.f, a1 = 0.f;
    for (int k = 0; k < D; ++k) { float w = bf2f(w1[(size_t)k * F + f]); a0 += y[0][k] * w; a1 += y[1][k] * w; }
    float bb = bf2f(b1[f]);
    float v[2] = {rbf(rbf(a0) + bb), rbf(rbf(a1) + bb)};
    for (int b = 0; b < 2; ++b) { float u = v[b]; float t = tanhf(0.7978845608028654f * (u + 0.044715f * u * u * u)); h[b * F + f] = rbf(0.5f * u * (1.f + t)); }
  }
}

__global__ void k_attn(const float* q, const u16* k, const u16* v, float* pm, float* pl, float* po) {
  const int s = blockIdx.x, kvh = blockIdx.y, b = blockIdx.z, t = threadIdx.x;
  constexpr int KS = SK / NS;
  __shared__ float qs[4][DH], sc[4][KS], red[4];
  for (int i = t; i < 4 * DH; i += 256) qs[i / DH][i % DH] = q[b * HQ * DH + kvh * 4 * DH + i];
  __syncthreads();
  const u16* kb = k + ((size_t)(b * HKV + kvh) * SK + s * KS) * DH;
  const u16* vb = v + ((size_t)(b * HKV + kvh) * SK + s * KS) * DH;
  for (int j = t; j < KS; j += 256) {
    float a[4] = {0, 0, 0, 0};
    for (int d = 0; d < DH; ++d) { float kk = bf2f(kb[(size_t)j * DH + d]); for (int g = 0; g < 4; ++g) a[g] += qs[g][d] * kk; }
    for (int g = 0; g < 4; ++g) sc[g][j] = a[g] * 0.08838834764831845f;
  }
  __syncthreads();
  if (t < 4) { float m = -INFINITY; for (int j = 0; j < KS; ++j) m = fmaxf(m, sc[t][j]); float l = 0.f;
    for (int j = 0; j < KS; ++j) { float p = __expf(sc[t][j] - m); sc[t][j] = p; l += p; }
    const int row = (b * HQ + kvh * 4 + t) * NS + s; pm[row] = m; pl[row] = l; }
  __syncthreads();
  for (int i = t; i < 4 * DH; i += 256) { const int g = i / DH, d = i % DH; float a = 0.f;
    for (int j = 0; j < KS; ++j) a += sc[g][j] * bf2f(vb[(size_t)j * DH + d]);
    po[((size_t)(b * HQ + kvh * 4 + g) * NS + s) * DH + d] = a; }
}

__global__ void k_comb(const float* pm, const float* pl, const float* po, float* o) {
  const int row = blockIdx.x, d = threadIdx.x;
  float M = -INFINITY; for (int s = 0; s < NS; ++s) M = fmaxf(M, pm[row * NS + s]);
  float L = 0.f, a = 0.f;
  for (int s = 0; s < NS; ++s) { float w = __expf(pm[row * NS + s] - M); L += w * pl[row * NS + s]; a += w * po[((size_t)row * NS + s) * DH + d]; }
  o[row * DH + d] = rbf(a / L);
}

__global__ void k_out(const u16* x, const float* o, const float* h, const u16* wo, const u16* w2, const u16* b2, __hip_bfloat16* out) {
  const int n = blockIdx.x, b = blockIdx.y, t = threadIdx.x;
  __shared__ float r0[256], r1[256];
  float s = 0.f, g = 0.f;
  for (int k = t; k < HQ * DH; k += 256) s += o[b * HQ * DH + k] * bf2f(wo[(size_t)k * D + n]);
  for (int k = t; k < F; k += 256) g += h[b * F + k] * bf2f(w2[(size_t)k * D + n]);
  r0[t] = s; r1[t] = g; __syncthreads();
  for (int of = 128; of > 0; of >>= 1) { if (t < of) { r0[t] += r0[t + of]; r1[t] += r1[t + of]; } __syncthreads(); }
  if (t == 0) { float sv = rbf(r0[0]); float m = rbf(rbf(r1[0]) + bf2f(b2[n])); float gg = rbf(m / (1.f + __expf(-m)));
    out[b * D + n] = __float2bfloat16(bf2f(x[b * D + n]) + rbf(sv * gg)); }
}

extern "C" void kernel_launch(void* const* d_in, const int* in_sizes, int n_in, void* d_out, int out_size, void* d_ws, size_t ws_size, hipStream_t stream) {
  const u16* x = (const u16*)d_in[0]; const u16* k = (const u16*)d_in[1]; const u16* v = (const u16*)d_in[2];
  const u16* wq = (const u16*)d_in[3]; const u16* wo = (const u16*)d_in[4];
  const u16* w1 = (const u16*)d_in[6]; const u16* b1 = (const u16*)d_in[7]; const u16* w2 = (const u16*)d_in[8]; const u16* b2 = (const u16*)d_in[9];
  float* ws = (float*)d_ws;
  float* q = ws; float* h = q + B * HQ * DH; float* pm = h + B * F; float* pl = pm + B * HQ * NS; float* po = pl + B * HQ * NS; float* o = po + B * HQ * NS * DH;
  k_pre<<<(HQ * DH + F + 255) / 256, 256, 0, stream>>>(x, wq, w1, b1, q, h);
  k_attn<<<dim3(NS, HKV, B), 256, 0, stream>>>(q, k, v, pm, pl, po);
  k_comb<<<B * HQ, DH, 0, stream>>>(pm, pl, po, o);
  k_out<<<dim3(D, B), 256, 0, stream>>>(x, o, h, wo, w2, b2, (__hip_bfloat16*)d_out);
}
```

```cpp
#include <hip/hip_runtime.h>
#include <hip/hip_bf16.h>
#include <stdint.h>

#define WIN 8
#define QPOS 24
#define KFRONT 4

namespace fused {
typedef unsigned v4u __attribute__((ext_vector_type(4)));
typedef unsigned v2u __attribute__((ext_vector_type(2)));
typedef float v4f __attribute__((ext_vector_type(4)));
typedef float v2f __attribute__((ext_vector_type(2)));
typedef short s16x8 __attribute__((ext_vector_type(8)));
typedef __bf16 bf16x2 __attribute__((ext_vector_type(2)));
typedef __bf16 bf16x8 __attribute__((ext_vector_type(8)));
typedef __amdgpu_buffer_rsrc_t rsrc_t;
typedef unsigned long long u64;

constexpr int D = 512, HQ = 32, HKV = 8, SK = 8192, DH = 128, F = 1792;
constexpr int AUX_NT = 2, AUX_SC1 = 16;
constexpr int KV_BYTES = 2 * HKV * SK * DH * 2;
constexpr int BLK = 1024;
constexpr int WS_E = 0, WS_QP = 4096, WS_HP = WS_QP + 4 * 64 * BLK, WS_R1 = WS_HP + 4 * 28 * BLK, WS_SP = WS_R1 + 32 * 32 * BLK,
              WS_GP = WS_SP + 32 * 8 * BLK, WS_END = WS_GP + 14 * 8 * BLK;
constexpr unsigned SPIN_CAP = 1u << 18;
constexpr float LOG2E = 1.4426950408889634f;
constexpr float SC2 = 0.08838834764831845f * LOG2E;

__device__ __forceinline__ rsrc_t mk(const void* p, int bytes) { return __builtin_amdgcn_make_buffer_rsrc((void*)p, 0, bytes, 0x00020000); }
template <int AUX> __device__ __forceinline__ v4u ld16(rsrc_t r, int voff, int soff) { return __builtin_amdgcn_raw_buffer_load_b128(r, voff, soff, AUX); }
__device__ __forceinline__ unsigned ld4(rsrc_t r, int voff, int soff) { return __builtin_amdgcn_raw_buffer_load_b32(r, voff, soff, 0); }
__device__ __forceinline__ void st16(v4u v, rsrc_t r, int voff, int soff) { __builtin_amdgcn_raw_buffer_store_b128(v, r, voff, soff, AUX_SC1); }
__device__ __forceinline__ float asf(unsigned u) { return __builtin_bit_cast(float, u); }
__device__ __forceinline__ unsigned asu(float f) { return __builtin_bit_cast(unsigned, f); }
__device__ __forceinline__ float lo16(unsigned u) { return asf(u << 16); }
__device__ __forceinline__ float hi16(unsigned u) { return asf(u & 0xffff0000u); }
__device__ __forceinline__ unsigned pk(float a, float b) { return __builtin_bit_cast(unsigned, bf16x2{(__bf16)a, (__bf16)b}); }
__device__ __forceinline__ float rbf(float a) { return (float)(__bf16)a; }
template <int C> __device__ __forceinline__ unsigned dppu(unsigned x) { return (unsigned)__builtin_amdgcn_mov_dpp((int)x, C, 0xf, 0xf, true); }
template <int C> __device__ __forceinline__ float dppf(float x) { return asf(dppu<C>(asu(x))); }
template <int I> __device__ __forceinline__ float to_bank(float old, float src) {
  return asf((unsigned)__builtin_amdgcn_update_dpp((int)asu(old), (int)asu(src), 0x110 + 4 * I, 0xf, 1 << I, false)); }
constexpr int XOR1 = 0xB1, XOR2 = 0x4E, HMIR = 0x141, SHL4 = 0x104, SHL8 = 0x108, ROR4 = 0x124, ROR8 = 0x128, ROR12 = 0x12C;
__device__ __forceinline__ float sw32_add(float a, float b) {
  auto t = __builtin_amdgcn_permlane32_swap(asu(a), asu(b), false, false); return asf(t[0]) + asf(t[1]); }
__device__ __forceinline__ float sw16_add(float a, float b) {
  auto t = __builtin_amdgcn_permlane16_swap(asu(a), asu(b), false, false); return asf(t[0]) + asf(t[1]); }
__device__ __forceinline__ float sw32_max(float a) {
  auto t = __builtin_amdgcn_permlane32_swap(asu(a), asu(a), false, false); return fmaxf(asf(t[0]), asf(t[1])); }
__device__ __forceinline__ float sw16_max(float a) {
  auto t = __builtin_amdgcn_permlane16_swap(asu(a), asu(a), false, false); return fmaxf(asf(t[0]), asf(t[1])); }
__device__ __forceinline__ float wave_sum(float x) {
  x += dppf<XOR1>(x); x += dppf<XOR2>(x); x += dppf<HMIR>(x); x += dppf<ROR8>(x); x = sw16_add(x, x); return sw32_add(x, x); }
__device__ __forceinline__ float dot2(unsigned a, unsigned b) {
  return __builtin_amdgcn_fdot2_f32_bf16(__builtin_bit_cast(bf16x2, a), __builtin_bit_cast(bf16x2, b), 0.f, false); }
__device__ __forceinline__ v4f mfma(v4u a, v4u b, v4f c) {
  return __builtin_amdgcn_mfma_f32_16x16x32_bf16(__builtin_bit_cast(bf16x8, a), __builtin_bit_cast(bf16x8, b), c, 0, 0, 0); }
__device__ __forceinline__ float exp2f_(float x) { return __builtin_amdgcn_exp2f(x); }
__device__ __forceinline__ float rcpf_(float x) { return __builtin_amdgcn_rcpf(x); }
#define SB() __builtin_amdgcn_sched_barrier(0)
#define VMWAIT(n) __builtin_amdgcn_s_waitcnt(0x0F70 | ((n) & 15) | (((n) >> 4) << 14))
#define LDS_BARRIER() do { SB(); __builtin_amdgcn_s_waitcnt(0xC07F); __builtin_amdgcn_s_barrier(); SB(); asm volatile("" ::: "memory"); } while (0)

struct Tag { unsigned a, b; };
__device__ __forceinline__ bool fresh(v4u r, Tag t) { return (r.y == t.a) & (r.w == t.b); }
__device__ __forceinline__ v4u rec(unsigned p0, unsigned p1, Tag t) { return v4u{p0, t.a, p1, t.b}; }

template <int N> __device__ __forceinline__ bool all_fresh(const v4u (&r)[N], Tag t) {
  bool ok = true;
#pragma unroll
  for (int k = 0; k < N; ++k) ok = ok & fresh(r[k], t);
  return ok;
}
template <int N> __device__ __forceinline__ void await(v4u (&r)[N], const int (&off)[N], rsrc_t ws, Tag t) {
  bool ok = all_fresh<N>(r, t);
  if (!__all(ok)) {
    unsigned spin = 0;
#pragma nounroll
    do {
      asm volatile("" ::: "memory");
      if (!ok) {
#pragma unroll
        for (int k = 0; k < N; ++k) r[k] = ld16<AUX_SC1>(ws, off[k], 0);
      }
      ok = all_fresh<N>(r, t);
    } while (!__all(ok) && ++spin < SPIN_CAP);
  }
  asm volatile("" ::: "memory");
}

__device__ __forceinline__ void ln_stats(const float (&v)[8], float& m, float& rs) {
  float s = ((v[0] + v[1]) + (v[2] + v[3])) + ((v[4] + v[5]) + (v[6] + v[7]));
  const float mf = wave_sum(s) * (1.f / D);
  float q = 0.f;
#pragma unroll
  for (int j = 0; j < 8; ++j) { const float d = v[j] - mf; q = fmaf(d, d, q); }
  const float var = rbf(wave_sum(q) * (1.f / D));
  m = rbf(mf);
  rs = 1.f / rbf(sqrtf(rbf(var + 1e-5f)));
}
__device__ __forceinline__ float ln_apply(float x, float m, float rs) { return rbf(rbf(x - m) * rs); }

struct WPair { unsigned p[8]; };
__device__ __forceinline__ WPair pair_rows(v4u w0, v4u w1) {
  WPair o;
#pragma unroll
  for (int t = 0; t < 4; ++t) { o.p[2 * t] = __builtin_amdgcn_perm(w1[t], w0[t], 0x05040100u); o.p[2 * t + 1] = __builtin_amdgcn_perm(w1[t], w0[t], 0x07060302u); }
  return o;
}
__device__ __forceinline__ v2f block_product(const WPair& w, unsigned in0, unsigned in1, bool b3) {
  float a[8];
#pragma unroll
  for (int j = 0; j < 8; ++j) a[j] = sw32_add(dot2(w.p[j], in0), dot2(w.p[j], in1));
  float c[4];
#pragma unroll
  for (int j = 0; j < 4; ++j) c[j] = sw16_add(a[j], a[j + 4]);
  v2f o;
#pragma unroll
  for (int j = 0; j < 2; ++j) { const float x = c[j] + dppf<ROR8>(c[j]), y = c[j + 2] + dppf<ROR8>(c[j + 2]); o[j] = b3 ? y : x; }
  return o;
}
__device__ __forceinline__ v2f sum8(const v2f* p) {
  v2f a[8];
#pragma unroll
  for (int w = 0; w < 8; ++w) a[w] = p[w * 64];
  return ((a[0] + a[1]) + (a[2] + a[3])) + ((a[4] + a[5]) + (a[6] + a[7]));
}

#define ST(i)

__global__ void __launch_bounds__(512, 2)
k_fused(const void* __restrict__ x, const void* __restrict__ kc, const void* __restrict__ vc, const void* __restrict__ wq, const void* __restrict__ wo,
        const void* __restrict__ w1, const void* __restrict__ b1, const void* __restrict__ w2, const void* __restrict__ b2, void* out, void* wsp) {
  __shared__ __attribute__((aligned(16))) float lds[4096 + 1024 + 1024 + 256 + 64];
  float* const l_o = lds;
  v2f* const l_r0 = (v2f*)(lds + 4096);
  v2f* const l_r1 = (v2f*)(lds + 5120);
  unsigned* const l_q = (unsigned*)(lds + 6144);
  v2f* const l_ml = (v2f*)(lds + 6400);

  const int tid = threadIdx.x, lane = tid & 63, w = __builtin_amdgcn_readfirstlane(tid >> 6);
  const int blk = blockIdx.x, x8 = blk & 7, i32 = blk >> 3, rg = i32 >> 3, k8 = i32 & 7, bat = i32 >> 4;
  const int e = x8 + 8 * k8;
  const bool is1 = e < 28, is2 = e >= 28 && e < 56;
  const int id2 = rg * 28 + e - 28, rb = id2 >> 3, c2 = id2 & 7;
  const int r = lane >> 3, ch = lane & 7;
  const bool b3 = (lane & 8) != 0;
  ST(0);

  const rsrc_t rx = mk(x, 2 * D * 2), rk = mk(kc, KV_BYTES), rv = mk(vc, KV_BYTES), rws = mk(wsp, WS_END);
  const rsrc_t rq = mk(wq, D * HQ * DH * 2), ro = mk(wo, D * HQ * DH * 2);
  const rsrc_t r1 = mk(w1, is1 ? D * F * 2 : 0), r2 = mk(w2, is2 ? D * F * 2 : 0), rb1 = mk(b1, is2 ? F * 2 : 0), rb2 = mk(b2, D * 2);

  const v2u ep = __builtin_amdgcn_raw_buffer_load_b64(rws, 0, WS_E, AUX_SC1);
  const v4u xa0 = ld16<0>(rx, lane * 16, 0), xa1 = ld16<0>(rx, lane * 16, D * 2);
  const unsigned xr0 = ld4(rx, r * 4, (rg * 64 + w * 8) * 4), xr1 = ld4(rx, r * 4, (rg * 64 + w * 8) * 4 + D * 2);
  const int row0 = rg * 128 + w * 16;
  const v4u wq0 = ld16<AUX_NT>(rq, r * 2 * 8192 + ch * 16, row0 * 8192 + (x8 * 8 + k8) * 128);
  const v4u wq1 = ld16<AUX_NT>(rq, r * 2 * 8192 + ch * 16, row0 * 8192 + (x8 * 8 + k8) * 128 + 8192);
  const v4u wa0 = ld16<AUX_NT>(r1, r * 2 * 3584 + ch * 16, row0 * 3584 + e * 128);
  const v4u wa1 = ld16<AUX_NT>(r1, r * 2 * 3584 + ch * 16, row0 * 3584 + e * 128 + 3584);
  SB();
  const int kvbase = ((bat * HKV + x8) * SK + (i32 & 15) * 512) * 256 + w * 1024;
  const int koff = ((lane >> 2) & 3) * 256 + (lane >> 4) * 64 + (lane & 3) * 16, voff = lane * 16;
  v4u K[16], V[16];
#pragma unroll
  for (int n = 0; n < KFRONT; ++n) { K[n] = ld16<AUX_NT>(rk, koff, kvbase + n * 8192); SB(); }

  Tag tag;
  tag.a = __builtin_amdgcn_readfirstlane(ep.x) + 1u; tag.b = __builtin_amdgcn_readfirstlane(ep.y) + 0x9E3779B9u;
  {
    float v0[8], v1[8];
#pragma unroll
    for (int t = 0; t < 4; ++t) { v0[2 * t] = lo16(xa0[t]); v0[2 * t + 1] = hi16(xa0[t]); v1[2 * t] = lo16(xa1[t]); v1[2 * t + 1] = hi16(xa1[t]); }
    float m0, s0, m1, s1;
    ln_stats(v0, m0, s0); ln_stats(v1, m1, s1);
    const float n00 = ln_apply(lo16(xr0), m0, s0), n01 = ln_apply(hi16(xr0), m0, s0);
    const float n10 = ln_apply(lo16(xr1), m1, s1), n11 = ln_apply(hi16(xr1), m1, s1);
    l_r0[w * 64 + lane] = block_product(pair_rows(wq0, wq1), pk(n00, n01), pk(n10, n11), b3);
    if (is1) {
#pragma unroll
      for (int j = 0; j < 8; ++j) { v0[j] = ln_apply(v0[j], m0, s0); v1[j] = ln_apply(v1[j], m1, s1); }
      ln_stats(v0, m0, s0); ln_stats(v1, m1, s1);
      l_r1[w * 64 + lane] = block_product(pair_rows(wa0, wa1), pk(ln_apply(n00, m0, s0), ln_apply(n01, m0, s0)),
                                          pk(ln_apply(n10, m1, s1), ln_apply(n11, m1, s1)), b3);
    }
  }
  asm volatile("" :: "v"(wa0), "v"(wa1));
  LDS_BARRIER();
  ST(1);
  const int recoff = ((lane >> 5) * 32 + ch * 4 + (r & 3)) * 16;
  if (w == 0) { const v2f s = sum8(l_r0 + lane); st16(rec(asu(s.x), asu(s.y), tag), rws, recoff, WS_QP + (rg * 64 + x8 * 8 + k8) * BLK); }
  if (w == 1 && is1) { const v2f s = sum8(l_r1 + lane); st16(rec(asu(s.x), asu(s.y), tag), rws, recoff, WS_HP + (rg * 28 + e) * BLK); }
  SB();

  v4u qr[2];
  int qo[2];
#pragma unroll
  for (int k = 0; k < 2; ++k) qo[k] = WS_QP + ((2 * k + (lane >> 5)) * 64 + x8 * 8 + w) * BLK + (bat * 32 + (lane & 31)) * 16;
#pragma unroll
  for (int n = KFRONT; n < QPOS; ++n) {
    VMWAIT(WIN - 1);
    if (n < 16) K[n] = ld16<AUX_NT>(rk, koff, kvbase + n * 8192);
    else V[n - 16] = ld16<AUX_NT>(rv, voff, kvbase + (n - 16) * 8192);
    if (n == QPOS - WIN) { qr[0] = ld16<AUX_SC1>(rws, qo[0], 0); qr[1] = ld16<AUX_SC1>(rws, qo[1], 0); }
    SB();
  }
  ST(2);
  await<2>(qr, qo, rws, tag);
  {
    const float q0 = asf(qr[0].x) + asf(qr[1].x), q1 = asf(qr[0].z) + asf(qr[1].z);
    const unsigned qp = pk(sw32_add(q0, q0), sw32_add(q1, q1));
    if (lane < 32) l_q[w * 32 + lane] = qp;
  }
  LDS_BARRIER();
  ST(3);
  const int bank = (lane >> 2) & 3, hh = lane & 3;
  const v4u qf = *(const v4u*)(l_q + hh * 64 + (lane >> 4) * 16 + bank * 4);
  float S[4];
#pragma unroll
  for (int n = 0; n < 16; ++n) {
    const v4f d = mfma(K[n], qf, v4f{0.f, 0.f, 0.f, 0.f});
    const float t = d[0] + dppf<SHL4>(d[1]), u = d[2] + dppf<SHL4>(d[3]);
    const float s = t + dppf<SHL8>(u);
    if ((n & 3) == 0) S[n >> 2] = s;
    else if ((n & 3) == 1) S[n >> 2] = to_bank<1>(S[n >> 2], s);
    else if ((n & 3) == 2) S[n >> 2] = to_bank<2>(S[n >> 2], s);
    else S[n >> 2] = to_bank<3>(S[n >> 2], s);
  }
  float mw = fmaxf(fmaxf(S[0], S[1]), fmaxf(S[2], S[3]));
  mw = fmaxf(mw, dppf<ROR4>(mw)); mw = fmaxf(mw, dppf<ROR8>(mw)); mw = sw32_max(sw16_max(mw));
  mw *= SC2;
  float lw = 0.f;
#pragma unroll
  for (int z = 0; z < 4; ++z) { S[z] = exp2f_(fmaf(S[z], SC2, -mw)); lw += S[z]; }
  lw += dppf<ROR4>(lw); lw += dppf<ROR8>(lw); lw = sw16_add(lw, lw); lw = sw32_add(lw, lw);
  if (lane < 4) l_ml[lane * 8 + w] = v2f{mw, lw};
  SB();
#pragma unroll
  for (int n = QPOS; n < QPOS + 2; ++n) { V[n - 16] = ld16<AUX_NT>(rv, voff, kvbase + (n - 16) * 8192); SB(); }
  LDS_BARRIER();
  float lse;
  unsigned P[4];
  {
    v4f ml[4];
#pragma unroll
    for (int t = 0; t < 4; ++t) ml[t] = *(const v4f*)(l_ml + hh * 8 + 2 * t);
    float M = fmaxf(fmaxf(fmaxf(ml[0][0], ml[0][2]), fmaxf(ml[1][0], ml[1][2])), fmaxf(fmaxf(ml[2][0], ml[2][2]), fmaxf(ml[3][0], ml[3][2])));
    float L = 0.f;
#pragma unroll
    for (int t = 0; t < 4; ++t) L += exp2f_(ml[t][0] - M) * ml[t][1] + exp2f_(ml[t][2] - M) * ml[t][3];
    const float f = exp2f_(mw - M) * rcpf_(L);
    lse = M + __builtin_amdgcn_logf(L);
#pragma unroll
    for (int z = 0; z < 4; ++z) { const float p = S[z] * f; P[z] = pk(p, dppf<SHL4>(p)); }
  }
  unsigned bm[4];
#pragma unroll
  for (int u = 0; u < 4; ++u) bm[u] = bank == u ? 0xffffffffu : 0u;
  v4u Bf[2][4];
#pragma unroll
  for (int be = 0; be < 2; ++be) {
    const unsigned p0 = P[2 * be], p1 = P[2 * be + 1];
    Bf[be][0] = v4u{p0 & bm[0], dppu<ROR8>(p0) & bm[0], p1 & bm[0], dppu<ROR8>(p1) & bm[0]};
    Bf[be][1] = v4u{dppu<ROR4>(p0) & bm[1], dppu<ROR12>(p0) & bm[1], dppu<ROR4>(p1) & bm[1], dppu<ROR12>(p1) & bm[1]};
    Bf[be][2] = v4u{dppu<ROR8>(p0) & bm[2], p0 & bm[2], dppu<ROR8>(p1) & bm[2], p1 & bm[2]};
    Bf[be][3] = v4u{dppu<ROR12>(p0) & bm[3], dppu<ROR4>(p0) & bm[3], dppu<ROR12>(p1) & bm[3], dppu<ROR4>(p1) & bm[3]};
  }
  SB();
  ST(4);
#pragma unroll
  for (int n = QPOS + 2; n < 32; ++n) { VMWAIT(WIN - 1); V[n - 16] = ld16<AUX_NT>(rv, voff, kvbase + (n - 16) * 8192); SB(); }

  v4u hr[1];
  const int ho[1] = {WS_HP + ((ch & 3) * 28 + 2 * rb + (w >> 2)) * BLK + ((ch >> 2) * 32 + (w & 3) * 8 + r) * 16};
  const int hg = x8 * 4 + rg;
  VMWAIT(WIN - 1);
  const v4u wo0 = ld16<AUX_NT>(ro, r * 2048 + ch * 16, (hg * 128 + w * 16) * 1024 + k8 * 128);
  SB(); VMWAIT(WIN - 1);
  const v4u wo1 = ld16<AUX_NT>(ro, r * 2048 + ch * 16, (hg * 128 + w * 16) * 1024 + k8 * 128 + 1024);
  SB();
  const v4u wb0 = ld16<AUX_NT>(r2, r * 2048 + ch * 16, (rb * 128 + w * 16) * 1024 + c2 * 128);
  const v4u wb1 = ld16<AUX_NT>(r2, r * 2048 + ch * 16, (rb * 128 + w * 16) * 1024 + c2 * 128 + 1024);
  hr[0] = ld16<AUX_SC1>(is2 ? rws : r2, ho[0], 0);
  const unsigned bias1 = ld4(rb1, r * 4, (rb * 64 + w * 8) * 4);
  const int ocol = x8 * 64 + (i32 & 15) * 4;
  const unsigned xo = ld4(rx, (lane & 1) * 4, (bat * D + ocol) * 2), bias2 = ld4(rb2, (lane & 1) * 4, ocol * 2);
  SB();
  ST(5);

  v4f acc[2] = {v4f{0.f, 0.f, 0.f, 0.f}, v4f{0.f, 0.f, 0.f, 0.f}};
#pragma unroll
  for (int be = 0; be < 2; ++be) {
    v4u A[8];
#pragma unroll
    for (int t = 0; t < 4; ++t)
#pragma unroll
      for (int u = 0; u < 4; ++u) {
        A[2 * u][t] = __builtin_amdgcn_perm(V[8 * be + 2 * t + 1][u], V[8 * be + 2 * t][u], 0x05040100u);
        A[2 * u + 1][t] = __builtin_amdgcn_perm(V[8 * be + 2 * t + 1][u], V[8 * be + 2 * t][u], 0x07060302u);
      }
#pragma unroll
    for (int u = 0; u < 4; ++u) { acc[0] = mfma(A[2 * u], Bf[be][u], acc[0]); acc[1] = mfma(A[2 * u + 1], Bf[be][u], acc[1]); }
  }
  *(v4f*)(l_o + w * 512 + lane * 4) = v4f{acc[0][0], acc[1][0], acc[0][1], acc[1][1]};
  *(v4f*)(l_o + w * 512 + 256 + lane * 4) = v4f{acc[0][2], acc[1][2], acc[0][3], acc[1][3]};
  const float lse_m = asf(dppu<0x50>(asu(lse))), lse_n = asf(dppu<0xFA>(asu(lse)));
  LDS_BARRIER();
  ST(6);
  if (w < 4) {
    v2f a[8];
#pragma unroll
    for (int ww = 0; ww < 8; ++ww) a[ww] = *(const v2f*)(l_o + ww * 512 + (lane >> 5) * 256 + w * 64 + (lane & 31) * 2);
    const v2f s = ((a[0] + a[1]) + (a[2] + a[3])) + ((a[4] + a[5]) + (a[6] + a[7]));
    const int mh = (lane >> 1) & 3, mu = (lane >> 3) & 3, mq = (lane >> 5) * 2 + (lane & 1);
    const float ls = (lane & 4) ? lse_n : lse_m;
    st16(rec(pk(s.x, s.y), asu(ls), tag), rws, mh * 32 * BLK + (w * 16 + mq * 4 + mu) * 16, WS_R1 + (x8 * 4 * 32 + i32) * BLK);
  }
  SB();
  ST(7);
  v4u pr[4];
  int po[4];
#pragma unroll
  for (int k = 0; k < 4; ++k) { po[k] = WS_R1 + (hg * 32 + k * 8 + ch) * BLK + (w * 8 + r) * 16; pr[k] = ld16<AUX_SC1>(rws, po[k], 0); }
  SB();
  if (is2) {
    await<1>(hr, ho, rws, tag);
    float h0 = asf(hr[0].x), h1 = asf(hr[0].z);
    h0 += dppf<XOR1>(h0); h1 += dppf<XOR1>(h1); h0 += dppf<XOR2>(h0); h1 += dppf<XOR2>(h1);
    h0 = rbf(rbf(h0) + lo16(bias1)); h1 = rbf(rbf(h1) + hi16(bias1));
    constexpr float G0 = -2.f * 0.7978845608028654f * LOG2E, G1 = G0 * 0.044715f;
    h0 = h0 * rcpf_(1.f + exp2f_(h0 * fmaf(G1, h0 * h0, G0))); h1 = h1 * rcpf_(1.f + exp2f_(h1 * fmaf(G1, h1 * h1, G0)));
    const unsigned hp = pk(h0, h1), hx = dppu<HMIR>(hp);
    l_r1[w * 64 + lane] = block_product(pair_rows(wb0, wb1), (ch & 4) ? hx : hp, (ch & 4) ? hp : hx, b3);
  }
  asm volatile("" :: "v"(wb0), "v"(wb1), "v"(hr[0]), "v"(bias1), "v"(xo), "v"(bias2));
  const WPair wop = pair_rows(wo0, wo1);
  SB();
  ST(8);
  await<4>(pr, po, rws, tag);
  ST(9);
  unsigned on[2];
#pragma unroll
  for (int b = 0; b < 2; ++b) {
    const float l0 = asf(pr[2 * b].z), l1 = asf(pr[2 * b + 1].z);
    float M = fmaxf(l0, l1);
    M = fmaxf(M, dppf<XOR1>(M)); M = fmaxf(M, dppf<XOR2>(M)); M = fmaxf(M, dppf<HMIR>(M));
    const float e0 = exp2f_(l0 - M), e1 = exp2f_(l1 - M);
    float z = e0 + e1, o0 = fmaf(e0, lo16(pr[2 * b].x), e1 * lo16(pr[2 * b + 1].x)), o1 = fmaf(e0, hi16(pr[2 * b].x), e1 * hi16(pr[2 * b + 1].x));
    z += dppf<XOR1>(z); o0 += dppf<XOR1>(o0); o1 += dppf<XOR1>(o1);
    z += dppf<XOR2>(z); o0 += dppf<XOR2>(o0); o1 += dppf<XOR2>(o1);
    z += dppf<HMIR>(z); o0 += dppf<HMIR>(o0); o1 += dppf<HMIR>(o1);
    const float iz = rcpf_(z);
    on[b] = pk(o0 * iz, o1 * iz);
  }
  l_r0[w * 64 + lane] = block_product(wop, on[0], on[1], b3);
  LDS_BARRIER();
  ST(10);
  if (w == 0) { const v2f s = sum8(l_r0 + lane); st16(rec(asu(s.x), asu(s.y), tag), rws, recoff, WS_SP + (hg * 8 + k8) * BLK); }
  if (w == 1 && is2) { const v2f s = sum8(l_r1 + lane); st16(rec(asu(s.x), asu(s.y), tag), rws, recoff, WS_GP + (rb * 8 + c2) * BLK); }
  if (w == 2) {
    const int src = lane >> 1, irec = (bat * 32 + (i32 & 15) * 2 + (lane & 1)) * 16;
    v4u fs[1], fg[1];
    const int so[1] = {WS_SP + (src * 8 + x8) * BLK + irec}, go[1] = {WS_GP + ((src < 14 ? src : 0) * 8 + x8) * BLK + irec};
    fs[0] = ld16<AUX_SC1>(rws, so[0], 0); fg[0] = ld16<AUX_SC1>(rws, go[0], 0);
    await<1>(fg, go, rws, tag);
    await<1>(fs, so, rws, tag);
    ST(11);
    float s0 = asf(fs[0].x), s1 = asf(fs[0].z), g0 = src < 14 ? asf(fg[0].x) : 0.f, g1 = src < 14 ? asf(fg[0].z) : 0.f;
    float sa = sw32_add(s0, g0), sb = sw32_add(s1, g1);
    float sc = sw16_add(sa, sb);
    sc += dppf<ROR8>(sc); sc += dppf<ROR4>(sc); sc += dppf<XOR2>(sc);
    auto t = __builtin_amdgcn_permlane32_swap(asu(sc), asu(sc), false, false);
    const float sv = rbf(asf(t[0]));
    const bool odd = (lane & 16) != 0;
    const float mv = rbf(rbf(asf(t[1])) + (odd ? hi16(bias2) : lo16(bias2)));
    const float gv = rbf(mv * rcpf_(1.f + exp2f_(-LOG2E * mv)));
    const float ov = (odd ? hi16(xo) : lo16(xo)) + rbf(sv * gv);
    auto u = __builtin_amdgcn_permlane16_swap(asu(ov), asu(ov), false, false);
    if (lane < 2) ((unsigned*)out)[(bat * D + ocol) / 2 + lane] = pk(asf(u[0]), asf(u[1]));
    if (blk == 0 && lane == 0) __builtin_amdgcn_raw_buffer_store_b64(v2u{tag.a, tag.b}, rws, 0, WS_E, AUX_SC1);
    ST(12);
  }
}
}

extern "C" void kernel_launch(void* const* d_in, const int* in_sizes, int n_in, void* d_out, int out_size, void* d_ws, size_t ws_size,
                              hipStream_t stream) {
  fused::k_fused<<<256, 512, 0, stream>>>(d_in[0], d_in[1], d_in[2], d_in[3], d_in[4], d_in[6], d_in[7], d_in[8], d_in[9], d_out, d_ws);
}
```

```cpp
#define HOLD_AT 1
#define HOLD_US 10
#include <hip/hip_runtime.h>
#include <hip/hip_bf16.h>
#include <stdint.h>

#define WIN 6
#define QPOS 20
#define KFRONT 4

namespace fused {
typedef unsigned v4u __attribute__((ext_vector_type(4)));
typedef unsigned v2u __attribute__((ext_vector_type(2)));
typedef float v4f __attribute__((ext_vector_type(4)));
typedef float v2f __attribute__((ext_vector_type(2)));
typedef short s16x8 __attribute__((ext_vector_type(8)));
typedef __bf16 bf16x2 __attribute__((ext_vector_type(2)));
typedef __bf16 bf16x8 __attribute__((ext_vector_type(8)));
typedef __amdgpu_buffer_rsrc_t rsrc_t;
typedef unsigned long long u64;

constexpr int D = 512, HQ = 32, HKV = 8, SK = 8192, DH = 128, F = 1792;
constexpr int AUX_NT = 2, AUX_SC1 = 16;
constexpr int KV_BYTES = 2 * HKV * SK * DH * 2;
constexpr int BLK = 1024;
constexpr int WS_E = 0, WS_QP = 4096, WS_HP = WS_QP + 4 * 64 * BLK, WS_R1 = WS_HP + 4 * 28 * BLK, WS_SP = WS_R1 + 32 * 32 * BLK,
              WS_GP = WS_SP + 32 * 8 * BLK, WS_END = WS_GP + 14 * 8 * BLK;
constexpr unsigned SPIN_CAP = 1u << 18;
constexpr float LOG2E = 1.4426950408889634f;
constexpr float SC2 = 0.08838834764831845f * LOG2E;

__device__ __forceinline__ rsrc_t mk(const void* p, int bytes) { return __builtin_amdgcn_make_buffer_rsrc((void*)p, 0, bytes, 0x00020000); }
template <int AUX> __device__ __forceinline__ v4u ld16(rsrc_t r, int voff, int soff) { return __builtin_amdgcn_raw_buffer_load_b128(r, voff, soff, AUX); }
__device__ __forceinline__ unsigned ld4(rsrc_t r, int voff, int soff) { return __builtin_amdgcn_raw_buffer_load_b32(r, voff, soff, 0); }
__device__ __forceinline__ void st16(v4u v, rsrc_t r, int voff, int soff) { __builtin_amdgcn_raw_buffer_store_b128(v, r, voff, soff, AUX_SC1); }
__device__ __forceinline__ float asf(unsigned u) { return __builtin_bit_cast(float, u); }
__device__ __forceinline__ unsigned asu(float f) { return __builtin_bit_cast(unsigned, f); }
__device__ __forceinline__ float lo16(unsigned u) { return asf(u << 16); }
__device__ __forceinline__ float hi16(unsigned u) { return asf(u & 0xffff0000u); }
__device__ __forceinline__ unsigned pk(float a, float b) { return __builtin_bit_cast(unsigned, bf16x2{(__bf16)a, (__bf16)b}); }
__device__ __forceinline__ float rbf(float a) { return (float)(__bf16)a; }
template <int C> __device__ __forceinline__ unsigned dppu(unsigned x) { return (unsigned)__builtin_amdgcn_mov_dpp((int)x, C, 0xf, 0xf, true); }
template <int C> __device__ __forceinline__ float dppf(float x) { return asf(dppu<C>(asu(x))); }
template <int I> __device__ __forceinline__ float to_bank(float old, float src) {
  return asf((unsigned)__builtin_amdgcn_update_dpp((int)asu(old), (int)asu(src), 0x110 + 4 * I, 0xf, 1 << I, false)); }
constexpr int XOR1 = 0xB1, XOR2 = 0x4E, HMIR = 0x141, SHL4 = 0x104, SHL8 = 0x108, ROR4 = 0x124, ROR8 = 0x128, ROR12 = 0x12C;
__device__ __forceinline__ float sw32_add(float a, float b) {
  auto t = __builtin_amdgcn_permlane32_swap(asu(a), asu(b), false, false); return asf(t[0]) + asf(t[1]); }
__device__ __forceinline__ float sw16_add(float a, float b) {
  auto t = __builtin_amdgcn_permlane16_swap(asu(a), asu(b), false, false); return asf(t[0]) + asf(t[1]); }
__device__ __forceinline__ float sw32_max(float a) {
  auto t = __builtin_amdgcn_permlane32_swap(asu(a), asu(a), false, false); return fmaxf(asf(t[0]), asf(t[1])); }
__device__ __forceinline__ float sw16_max(float a) {
  auto t = __builtin_amdgcn_permlane16_swap(asu(a), asu(a), false, false); return fmaxf(asf(t[0]), asf(t[1])); }
__device__ __forceinline__ float wave_sum(float x) {
  x += dppf<XOR1>(x); x += dppf<XOR2>(x); x += dppf<HMIR>(x); x += dppf<ROR8>(x); x = sw16_add(x, x); return sw32_add(x, x); }
__device__ __forceinline__ float dot2(unsigned a, unsigned b) {
  return __builtin_amdgcn_fdot2_f32_bf16(__builtin_bit_cast(bf16x2, a), __builtin_bit_cast(bf16x2, b), 0.f, false); }
__device__ __forceinline__ v4f mfma(v4u a, v4u b, v4f c) {
  return __builtin_amdgcn_mfma_f32_16x16x32_bf16(__builtin_bit_cast(bf16x8, a), __builtin_bit_cast(bf16x8, b), c, 0, 0, 0); }
__device__ __forceinline__ float exp2f_(float x) { return __builtin_amdgcn_exp2f(x); }
__device__ __forceinline__ float rcpf_(float x) { return __builtin_amdgcn_rcpf(x); }
#define SB() __builtin_amdgcn_sched_barrier(0)
#define VMWAIT(n) __builtin_amdgcn_s_waitcnt(0x0F70 | ((n) & 15) | (((n) >> 4) << 14))
#define LDS_BARRIER() do { SB(); __builtin_amdgcn_s_waitcnt(0xC07F); __builtin_amdgcn_s_barrier(); SB(); asm volatile("" ::: "memory"); } while (0)

struct Tag { unsigned a, b; };
__device__ __forceinline__ bool fresh(v4u r, Tag t) { return (r.y == t.a) & (r.w == t.b); }
__device__ __forceinline__ v4u rec(unsigned p0, unsigned p1, Tag t) { return v4u{p0, t.a, p1, t.b}; }

template <int N> __device__ __forceinline__ bool all_fresh(const v4u (&r)[N], Tag t) {
  bool ok = true;
#pragma unroll
  for (int k = 0; k < N; ++k) ok = ok & fresh(r[k], t);
  return ok;
}
template <int N> __device__ __forceinline__ void await(v4u (&r)[N], const int (&off)[N], rsrc_t ws, Tag t) {
  bool ok = all_fresh<N>(r, t);
  if (!__all(ok)) {
    unsigned spin = 0;
#pragma nounroll
    do {
      asm volatile("" ::: "memory");
      if (!ok) {
#pragma unroll
        for (int k = 0; k < N; ++k) r[k] = ld16<AUX_SC1>(ws, off[k], 0);
      }
      ok = all_fresh<N>(r, t);
    } while (!__all(ok) && ++spin < SPIN_CAP);
  }
  asm volatile("" ::: "memory");
}

__device__ __forceinline__ void ln_stats(const float (&v)[8], float& m, float& rs) {
  float s = ((v[0] + v[1]) + (v[2] + v[3])) + ((v[4] + v[5]) + (v[6] + v[7]));
  const float mf = wave_sum(s) * (1.f / D);
  float q = 0.f;
#pragma unroll
  for (int j = 0; j < 8; ++j) { const float d = v[j] - mf; q = fmaf(d, d, q); }
  const float var = rbf(wave_sum(q) * (1.f / D));
  m = rbf(mf);
  rs = 1.f / rbf(sqrtf(rbf(var + 1e-5f)));
}
__device__ __forceinline__ float ln_apply(float x, float m, float rs) { return rbf(rbf(x - m) * rs); }

struct WPair { unsigned p[8]; };
__device__ __forceinline__ WPair pair_rows(v4u w0, v4u w1) {
  WPair o;
#pragma unroll
  for (int t = 0; t < 4; ++t) { o.p[2 * t] = __builtin_amdgcn_perm(w1[t], w0[t], 0x05040100u); o.p[2 * t + 1] = __builtin_amdgcn_perm(w1[t], w0[t], 0x07060302u); }
  return o;
}
__device__ __forceinline__ v2f block_product(const WPair& w, unsigned in0, unsigned in1, bool b3) {
  float a[8];
#pragma unroll
  for (int j = 0; j < 8; ++j) a[j] = sw32_add(dot2(w.p[j], in0), dot2(w.p[j], in1));
  float c[4];
#pragma unroll
  for (int j = 0; j < 4; ++j) c[j] = sw16_add(a[j], a[j + 4]);
  v2f o;
#pragma unroll
  for (int j = 0; j < 2; ++j) { const float x = c[j] + dppf<ROR8>(c[j]), y = c[j + 2] + dppf<ROR8>(c[j + 2]); o[j] = b3 ? y : x; }
  return o;
}
__device__ __forceinline__ v2f sum8(const v2f* p) {
  v2f a[8];
#pragma unroll
  for (int w = 0; w < 8; ++w) a[w] = p[w * 64];
  return ((a[0] + a[1]) + (a[2] + a[3])) + ((a[4] + a[5]) + (a[6] + a[7]));
}

#define ST(i) do { if ((i) == HOLD_AT) { SB(); while (__builtin_amdgcn_s_memrealtime() < t_start + HOLD_US * 100) __builtin_amdgcn_s_sleep(1); SB(); } } while (0)

__global__ void __launch_bounds__(512, 2)
k_fused(const void* __restrict__ x, const void* __restrict__ kc, const void* __restrict__ vc, const void* __restrict__ wq, const void* __restrict__ wo,
        const void* __restrict__ w1, const void* __restrict__ b1, const void* __restrict__ w2, const void* __restrict__ b2, void* out, void* wsp) {
  __shared__ __attribute__((aligned(16))) float lds[4096 + 1024 + 1024 + 256 + 64];
  float* const l_o = lds;
  v2f* const l_r0 = (v2f*)(lds + 4096);
  v2f* const l_r1 = (v2f*)(lds + 5120);
  unsigned* const l_q = (unsigned*)(lds + 6144);
  v2f* const l_ml = (v2f*)(lds + 6400);

  const int tid = threadIdx.x, lane = tid & 63, w = __builtin_amdgcn_readfirstlane(tid >> 6);
  const int blk = blockIdx.x, x8 = blk & 7, i32 = blk >> 3, rg = i32 >> 3, k8 = i32 & 7, bat = i32 >> 4;
  const int e = x8 + 8 * k8;
  const bool is1 = e < 28, is2 = e >= 28 && e < 56;
  const int id2 = rg * 28 + e - 28, rb = id2 >> 3, c2 = id2 & 7;
  const int r = lane >> 3, ch = lane & 7;
  const bool b3 = (lane & 8) != 0;
  const u64 t_start = __builtin_amdgcn_s_memrealtime();
  ST(0);

  const rsrc_t rx = mk(x, 2 * D * 2), rk = mk(kc, KV_BYTES), rv = mk(vc, KV_BYTES), rws = mk(wsp, WS_END);
  const rsrc_t rq = mk(wq, D * HQ * DH * 2), ro = mk(wo, D * HQ * DH * 2);
  const rsrc_t r1 = mk(w1, is1 ? D * F * 2 : 0), r2 = mk(w2, is2 ? D * F * 2 : 0), rb1 = mk(b1, is2 ? F * 2 : 0), rb2 = mk(b2, D * 2);

  const v2u ep = __builtin_amdgcn_raw_buffer_load_b64(rws, 0, WS_E, AUX_SC1);
  const v4u xa0 = ld16<0>(rx, lane * 16, 0), xa1 = ld16<0>(rx, lane * 16, D * 2);
  const unsigned xr0 = ld4(rx, r * 4, (rg * 64 + w * 8) * 4), xr1 = ld4(rx, r * 4, (rg * 64 + w * 8) * 4 + D * 2);
  const int row0 = rg * 128 + w * 16;
  const v4u wq0 = ld16<AUX_NT>(rq, r * 2 * 8192 + ch * 16, row0 * 8192 + (x8 * 8 + k8) * 128);
  const v4u wq1 = ld16<AUX_NT>(rq, r * 2 * 8192 + ch * 16, row0 * 8192 + (x8 * 8 + k8) * 128 + 8192);
  const v4u wa0 = ld16<AUX_NT>(r1, r * 2 * 3584 + ch * 16, row0 * 3584 + e * 128);
  const v4u wa1 = ld16<AUX_NT>(r1, r * 2 * 3584 + ch * 16, row0 * 3584 + e * 128 + 3584);
  SB();
  const int kvbase = ((bat * HKV + x8) * SK + (i32 & 15) * 512) * 256 + w * 1024;
  const int koff = ((lane >> 2) & 3) * 256 + (lane >> 4) * 64 + (lane & 3) * 16, voff = lane * 16;
  v4u K[16], V[16];
#pragma unroll
  for (int n = 0; n < KFRONT; ++n) { K[n] = ld16<AUX_NT>(rk, koff, kvbase + n * 8192); SB(); }

  Tag tag;
  tag.a = __builtin_amdgcn_readfirstlane(ep.x) + 1u; tag.b = __builtin_amdgcn_readfirstlane(ep.y) + 0x9E3779B9u;
  {
    float v0[8], v1[8];
#pragma unroll
    for (int t = 0; t < 4; ++t) { v0[2 * t] = lo16(xa0[t]); v0[2 * t + 1] = hi16(xa0[t]); v1[2 * t] = lo16(xa1[t]); v1[2 * t + 1] = hi16(xa1[t]); }
    float m0, s0, m1, s1;
    ln_stats(v0, m0, s0); ln_stats(v1, m1, s1);
    const float n00 = ln_apply(lo16(xr0), m0, s0), n01 = ln_apply(hi16(xr0), m0, s0);
    const float n10 = ln_apply(lo16(xr1), m1, s1), n11 = ln_apply(hi16(xr1), m1, s1);
    l_r0[w * 64 + lane] = block_product(pair_rows(wq0, wq1), pk(n00, n01), pk(n10, n11), b3);
    if (is1) {
#pragma unroll
      for (int j = 0; j < 8; ++j) { v0[j] = ln_apply(v0[j], m0, s0); v1[j] = ln_apply(v1[j], m1, s1); }
      ln_stats(v0, m0, s0); ln_stats(v1, m1, s1);
      l_r1[w * 64 + lane] = block_product(pair_rows(wa0, wa1), pk(ln_apply(n00, m0, s0), ln_apply(n01, m0, s0)),
                                          pk(ln_apply(n10, m1, s1), ln_apply(n11, m1, s1)), b3);
    }
  }
  asm volatile("" :: "v"(wa0), "v"(wa1));
  LDS_BARRIER();
  ST(1);
  const int recoff = ((lane >> 5) * 32 + ch * 4 + (r & 3)) * 16;
  if (w == 0) { const v2f s = sum8(l_r0 + lane); st16(rec(asu(s.x), asu(s.y), tag), rws, recoff, WS_QP + (rg * 64 + x8 * 8 + k8) * BLK); }
  if (w == 1 && is1) { const v2f s = sum8(l_r1 + lane); st16(rec(asu(s.x), asu(s.y), tag), rws, recoff, WS_HP + (rg * 28 + e) * BLK); }
  SB();

  v4u qr[2];
  int qo[2];
#pragma unroll
  for (int k = 0; k < 2; ++k) qo[k] = WS_QP + ((2 * k + (lane >> 5)) * 64 + x8 * 8 + w) * BLK + (bat * 32 + (lane & 31)) * 16;
#pragma unroll
  for (int n = KFRONT; n < QPOS; ++n) {
    VMWAIT(WIN - 1);
    if (n < 16) K[n] = ld16<AUX_NT>(rk, koff, kvbase + n * 8192);
    else V[n - 16] = ld16<AUX_NT>(rv, voff, kvbase + (n - 16) * 8192);
    if (n == QPOS - WIN) { qr[0] = ld16<AUX_SC1>(rws, qo[0], 0); qr[1] = ld16<AUX_SC1>(rws, qo[1], 0); }
    SB();
  }
  ST(2);
  await<2>(qr, qo, rws, tag);
  {
    const float q0 = asf(qr[0].x) + asf(qr[1].x), q1 = asf(qr[0].z) + asf(qr[1].z);
    const unsigned qp = pk(sw32_add(q0, q0), sw32_add(q1, q1));
    if (lane < 32) l_q[w * 32 + lane] = qp;
  }
  LDS_BARRIER();
  ST(3);
  const int bank = (lane >> 2) & 3, hh = lane & 3;
  const v4u qf = *(const v4u*)(l_q + hh * 64 + (lane >> 4) * 16 + bank * 4);
  float S[4];
#pragma unroll
  for (int n = 0; n < 16; ++n) {
    const v4f d = mfma(K[n], qf, v4f{0.f, 0.f, 0.f, 0.f});
    const float t = d[0] + dppf<SHL4>(d[1]), u = d[2] + dppf<SHL4>(d[3]);
    const float s = t + dppf<SHL8>(u);
    if ((n & 3) == 0) S[n >> 2] = s;
    else if ((n & 3) == 1) S[n >> 2] = to_bank<1>(S[n >> 2], s);
    else if ((n & 3) == 2) S[n >> 2] = to_bank<2>(S[n >> 2], s);
    else S[n >> 2] = to_bank<3>(S[n >> 2], s);
  }
  float mw = fmaxf(fmaxf(S[0], S[1]), fmaxf(S[2], S[3]));
  mw = fmaxf(mw, dppf<ROR4>(mw)); mw = fmaxf(mw, dppf<ROR8>(mw)); mw = sw32_max(sw16_max(mw));
  mw *= SC2;
  float lw = 0.f;
#pragma unroll
  for (int z = 0; z < 4; ++z) { S[z] = exp2f_(fmaf(S[z], SC2, -mw)); lw += S[z]; }
  lw += dppf<ROR4>(lw); lw += dppf<ROR8>(lw); lw = sw16_add(lw, lw); lw = sw32_add(lw, lw);
  if (lane < 4) l_ml[lane * 8 + w] = v2f{mw, lw};
  SB();
#pragma unroll
  for (int n = QPOS; n < QPOS + 2; ++n) { V[n - 16] = ld16<AUX_NT>(rv, voff, kvbase + (n - 16) * 8192); SB(); }
  LDS_BARRIER();
  float lse;
  unsigned P[4];
  {
    v4f ml[4];
#pragma unroll
    for (int t = 0; t < 4; ++t) ml[t] = *(const v4f*)(l_ml + hh * 8 + 2 * t);
    float M = fmaxf(fmaxf(fmaxf(ml[0][0], ml[0][2]), fmaxf(ml[1][0], ml[1][2])), fmaxf(fmaxf(ml[2][0], ml[2][2]), fmaxf(ml[3][0], ml[3][2])));
    float L = 0.f;
#pragma unroll
    for (int t = 0; t < 4; ++t) L += exp2f_(ml[t][0] - M) * ml[t][1] + exp2f_(ml[t][2] - M) * ml[t][3];
    const float f = exp2f_(mw - M) * rcpf_(L);
    lse = M + __builtin_amdgcn_logf(L);
#pragma unroll
    for (int z = 0; z < 4; ++z) { const float p = S[z] * f; P[z] = pk(p, dppf<SHL4>(p)); }
  }
  unsigned bm[4];
#pragma unroll
  for (int u = 0; u < 4; ++u) bm[u] = bank == u ? 0xffffffffu : 0u;
  v4u Bf[2][4];
#pragma unroll
  for (int be = 0; be < 2; ++be) {
    const unsigned p0 = P[2 * be], p1 = P[2 * be + 1];
    Bf[be][0] = v4u{p0 & bm[0], dppu<ROR8>(p0) & bm[0], p1 & bm[0], dppu<ROR8>(p1) & bm[0]};
    Bf[be][1] = v4u{dppu<ROR4>(p0) & bm[1], dppu<ROR12>(p0) & bm[1], dppu<ROR4>(p1) & bm[1], dppu<ROR12>(p1) & bm[1]};
    Bf[be][2] = v4u{dppu<ROR8>(p0) & bm[2], p0 & bm[2], dppu<ROR8>(p1) & bm[2], p1 & bm[2]};
    Bf[be][3] = v4u{dppu<ROR12>(p0) & bm[3], dppu<ROR4>(p0) & bm[3], dppu<ROR12>(p1) & bm[3], dppu<ROR4>(p1) & bm[3]};
  }
  SB();
  ST(4);
#pragma unroll
  for (int n = QPOS + 2; n < 32; ++n) { VMWAIT(WIN - 1); V[n - 16] = ld16<AUX_NT>(rv, voff, kvbase + (n - 16) * 8192); SB(); }

  v4u hr[1];
  const int ho[1] = {WS_HP + ((ch & 3) * 28 + 2 * rb + (w >> 2)) * BLK + ((ch >> 2) * 32 + (w & 3) * 8 + r) * 16};
  const int hg = x8 * 4 + rg;
  VMWAIT(WIN - 1);
  const v4u wo0 = ld16<AUX_NT>(ro, r * 2048 + ch * 16, (hg * 128 + w * 16) * 1024 + k8 * 128);
  SB(); VMWAIT(WIN - 1);
  const v4u wo1 = ld16<AUX_NT>(ro, r * 2048 + ch * 16, (hg * 128 + w * 16) * 1024 + k8 * 128 + 1024);
  SB();
  const v4u wb0 = ld16<AUX_NT>(r2, r * 2048 + ch * 16, (rb * 128 + w * 16) * 1024 + c2 * 128);
  const v4u wb1 = ld16<AUX_NT>(r2, r * 2048 + ch * 16, (rb * 128 + w * 16) * 1024 + c2 * 128 + 1024);
  hr[0] = ld16<AUX_SC1>(is2 ? rws : r2, ho[0], 0);
  const unsigned bias1 = ld4(rb1, r * 4, (rb * 64 + w * 8) * 4);
  const int ocol = x8 * 64 + (i32 & 15) * 4;
  const unsigned xo = ld4(rx, (lane & 1) * 4, (bat * D + ocol) * 2), bias2 = ld4(rb2, (lane & 1) * 4, ocol * 2);
  SB();
  ST(5);

  v4f acc[2] = {v4f{0.f, 0.f, 0.f, 0.f}, v4f{0.f, 0.f, 0.f, 0.f}};
#pragma unroll
  for (int be = 0; be < 2; ++be) {
    v4u A[8];
#pragma unroll
    for (int t = 0; t < 4; ++t)
#pragma unroll
      for (int u = 0; u < 4; ++u) {
        A[2 * u][t] = __builtin_amdgcn_perm(V[8 * be + 2 * t + 1][u], V[8 * be + 2 * t][u], 0x05040100u);
        A[2 * u + 1][t] = __builtin_amdgcn_perm(V[8 * be + 2 * t + 1][u], V[8 * be + 2 * t][u], 0x07060302u);
      }
#pragma unroll
    for (int u = 0; u < 4; ++u) { acc[0] = mfma(A[2 * u], Bf[be][u], acc[0]); acc[1] = mfma(A[2 * u + 1], Bf[be][u], acc[1]); }
  }
  *(v4f*)(l_o + w * 512 + lane * 4) = v4f{acc[0][0], acc[1][0], acc[0][1], acc[1][1]};
  *(v4f*)(l_o + w * 512 + 256 + lane * 4) = v4f{acc[0][2], acc[1][2], acc[0][3], acc[1][3]};
  const float lse_m = asf(dppu<0x50>(asu(lse))), lse_n = asf(dppu<0xFA>(asu(lse)));
  LDS_BARRIER();
  ST(6);
  if (w < 4) {
    v2f a[8];
#pragma unroll
    for (int ww = 0; ww < 8; ++ww) a[ww] = *(const v2f*)(l_o + ww * 512 + (lane >> 5) * 256 + w * 64 + (lane & 31) * 2);
    const v2f s = ((a[0] + a[1]) + (a[2] + a[3])) + ((a[4] + a[5]) + (a[6] + a[7]));
    const int mh = (lane >> 1) & 3, mu = (lane >> 3) & 3, mq = (lane >> 5) * 2 + (lane & 1);
    const float ls = (lane & 4) ? lse_n : lse_m;
    st16(rec(pk(s.x, s.y), asu(ls), tag), rws, mh * 32 * BLK + (w * 16 + mq * 4 + mu) * 16, WS_R1 + (x8 * 4 * 32 + i32) * BLK);
  }
  SB();
  ST(7);
  v4u pr[4];
  int po[4];
#pragma unroll
  for (int k = 0; k < 4; ++k) { po[k] = WS_R1 + (hg * 32 + k * 8 + ch) * BLK + (w * 8 + r) * 16; pr[k] = ld16<AUX_SC1>(rws, po[k], 0); }
  SB();
  if (is2) {
    await<1>(hr, ho, rws, tag);
    float h0 = asf(hr[0].x), h1 = asf(hr[0].z);
    h0 += dppf<XOR1>(h0); h1 += dppf<XOR1>(h1); h0 += dppf<XOR2>(h0); h1 += dppf<XOR2>(h1);
    h0 = rbf(rbf(h0) + lo16(bias1)); h1 = rbf(rbf(h1) + hi16(bias1));
    constexpr float G0 = -2.f * 0.7978845608028654f * LOG2E, G1 = G0 * 0.044715f;
    h0 = h0 * rcpf_(1.f + exp2f_(h0 * fmaf(G1, h0 * h0, G0))); h1 = h1 * rcpf_(1.f + exp2f_(h1 * fmaf(G1, h1 * h1, G0)));
    const unsigned hp = pk(h0, h1), hx = dppu<HMIR>(hp);
    l_r1[w * 64 + lane] = block_product(pair_rows(wb0, wb1), (ch & 4) ? hx : hp, (ch & 4) ? hp : hx, b3);
  }
  asm volatile("" :: "v"(wb0), "v"(wb1), "v"(hr[0]), "v"(bias1), "v"(xo), "v"(bias2));
  const WPair wop = pair_rows(wo0, wo1);
  SB();
  ST(8);
  await<4>(pr, po, rws, tag);
  ST(9);
  unsigned on[2];
#pragma unroll
  for (int b = 0; b < 2; ++b) {
    const float l0 = asf(pr[2 * b].z), l1 = asf(pr[2 * b + 1].z);
    float M = fmaxf(l0, l1);
    M = fmaxf(M, dppf<XOR1>(M)); M = fmaxf(M, dppf<XOR2>(M)); M = fmaxf(M, dppf<HMIR>(M));
    const float e0 = exp2f_(l0 - M), e1 = exp2f_(l1 - M);
    float z = e0 + e1, o0 = fmaf(e0, lo16(pr[2 * b].x), e1 * lo16(pr[2 * b + 1].x)), o1 = fmaf(e0, hi16(pr[2 * b].x), e1 * hi16(pr[2 * b + 1].x));
    z += dppf<XOR1>(z); o0 += dppf<XOR1>(o0); o1 += dppf<XOR1>(o1);
    z += dppf<XOR2>(z); o0 += dppf<XOR2>(o0); o1 += dppf<XOR2>(o1);
    z += dppf<HMIR>(z); o0 += dppf<HMIR>(o0); o1 += dppf<HMIR>(o1);
    const float iz = rcpf_(z);
    on[b] = pk(o0 * iz, o1 * iz);
  }
  l_r0[w * 64 + lane] = block_product(wop, on[0], on[1], b3);
  LDS_BARRIER();
  ST(10);
  if (w == 0) { const v2f s = sum8(l_r0 + lane); st16(rec(asu(s.x), asu(s.y), tag), rws, recoff, WS_SP + (hg * 8 + k8) * BLK); }
  if (w == 1 && is2) { const v2f s = sum8(l_r1 + lane); st16(rec(asu(s.x), asu(s.y), tag), rws, recoff, WS_GP + (rb * 8 + c2) * BLK); }
  if (w == 2) {
    const int src = lane >> 1, irec = (bat * 32 + (i32 & 15) * 2 + (lane & 1)) * 16;
    v4u fs[1], fg[1];
    const int so[1] = {WS_SP + (src * 8 + x8) * BLK + irec}, go[1] = {WS_GP + ((src < 14 ? src : 0) * 8 + x8) * BLK + irec};
    fs[0] = ld16<AUX_SC1>(rws, so[0], 0); fg[0] = ld16<AUX_SC1>(rws, go[0], 0);
    await<1>(fg, go, rws, tag);
    await<1>(fs, so, rws, tag);
    ST(11);
    float s0 = asf(fs[0].x), s1 = asf(fs[0].z), g0 = src < 14 ? asf(fg[0].x) : 0.f, g1 = src < 14 ? asf(fg[0].z) : 0.f;
    float sa = sw32_add(s0, g0), sb = sw32_add(s1, g1);
    float sc = sw16_add(sa, sb);
    sc += dppf<ROR8>(sc); sc += dppf<ROR4>(sc); sc += dppf<XOR2>(sc);
    auto t = __builtin_amdgcn_permlane32_swap(asu(sc), asu(sc), false, false);
    const float sv = rbf(asf(t[0]));
    const bool odd = (lane & 16) != 0;
    const float mv = rbf(rbf(asf(t[1])) + (odd ? hi16(bias2) : lo16(bias2)));
    const float gv = rbf(mv * rcpf_(1.f + exp2f_(-LOG2E * mv)));
    const float ov = (odd ? hi16(xo) : lo16(xo)) + rbf(sv * gv);
    auto u = __builtin_amdgcn_permlane16_swap(asu(ov), asu(ov), false, false);
    if (lane < 2) ((unsigned*)out)[(bat * D + ocol) / 2 + lane] = pk(asf(u[0]), asf(u[1]));
    if (blk == 0 && lane == 0) __builtin_amdgcn_raw_buffer_store_b64(v2u{tag.a, tag.b}, rws, 0, WS_E, AUX_SC1);
    ST(12);
  }
}
}

extern "C" void kernel_launch(void* const* d_in, const int* in_sizes, int n_in, void* d_out, int out_size, void* d_ws, size_t ws_size,
                              hipStream_t stream) {
  fused::k_fused<<<256, 512, 0, stream>>>(d_in[0], d_in[1], d_in[2], d_in[3], d_in[4], d_in[6], d_in[7], d_in[8], d_in[9], d_out, d_ws);
}
```

```cpp
#define HOLD_AT 12
#define HOLD_US 40
#define HOLD_COND blk == 255
#include <hip/hip_runtime.h>
#include <hip/hip_bf16.h>
#include <stdint.h>

#define WIN 6
#define QPOS 20
#define KFRONT 4

namespace fused {
typedef unsigned v4u __attribute__((ext_vector_type(4)));
typedef unsigned v2u __attribute__((ext_vector_type(2)));
typedef float v4f __attribute__((ext_vector_type(4)));
typedef float v2f __attribute__((ext_vector_type(2)));
typedef short s16x8 __attribute__((ext_vector_type(8)));
typedef __bf16 bf16x2 __attribute__((ext_vector_type(2)));
typedef __bf16 bf16x8 __attribute__((ext_vector_type(8)));
typedef __amdgpu_buffer_rsrc_t rsrc_t;
typedef unsigned long long u64;

constexpr int D = 512, HQ = 32, HKV = 8, SK = 8192, DH = 128, F = 1792;
constexpr int AUX_NT = 2, AUX_SC1 = 16;
constexpr int KV_BYTES = 2 * HKV * SK * DH * 2;
constexpr int BLK = 1024;
constexpr int WS_E = 0, WS_QP = 4096, WS_HP = WS_QP + 4 * 64 * BLK, WS_R1 = WS_HP + 4 * 28 * BLK, WS_SP = WS_R1 + 32 * 32 * BLK,
              WS_GP = WS_SP + 32 * 8 * BLK, WS_END = WS_GP + 14 * 8 * BLK;
constexpr unsigned SPIN_CAP = 1u << 18;
constexpr float LOG2E = 1.4426950408889634f;
constexpr float SC2 = 0.08838834764831845f * LOG2E;

__device__ __forceinline__ rsrc_t mk(const void* p, int bytes) { return __builtin_amdgcn_make_buffer_rsrc((void*)p, 0, bytes, 0x00020000); }
template <int AUX> __device__ __forceinline__ v4u ld16(rsrc_t r, int voff, int soff) { return __builtin_amdgcn_raw_buffer_load_b128(r, voff, soff, AUX); }
__device__ __forceinline__ unsigned ld4(rsrc_t r, int voff, int soff) { return __builtin_amdgcn_raw_buffer_load_b32(r, voff, soff, 0); }
__device__ __forceinline__ void st16(v4u v, rsrc_t r, int voff, int soff) { __builtin_amdgcn_raw_buffer_store_b128(v, r, voff, soff, AUX_SC1); }
__device__ __forceinline__ float asf(unsigned u) { return __builtin_bit_cast(float, u); }
__device__ __forceinline__ unsigned asu(float f) { return __builtin_bit_cast(unsigned, f); }
__device__ __forceinline__ float lo16(unsigned u) { return asf(u << 16); }
__device__ __forceinline__ float hi16(unsigned u) { return asf(u & 0xffff0000u); }
__device__ __forceinline__ unsigned pk(float a, float b) { return __builtin_bit_cast(unsigned, bf16x2{(__bf16)a, (__bf16)b}); }
__device__ __forceinline__ float rbf(float a) { return (float)(__bf16)a; }
template <int C> __device__ __forceinline__ unsigned dppu(unsigned x) { return (unsigned)__builtin_amdgcn_mov_dpp((int)x, C, 0xf, 0xf, true); }
template <int C> __device__ __forceinline__ float dppf(float x) { return asf(dppu<C>(asu(x))); }
template <int I> __device__ __forceinline__ float to_bank(float old, float src) {
  return asf((unsigned)__builtin_amdgcn_update_dpp((int)asu(old), (int)asu(src), 0x110 + 4 * I, 0xf, 1 << I, false)); }
constexpr int XOR1 = 0xB1, XOR2 = 0x4E, HMIR = 0x141, SHL4 = 0x104, SHL8 = 0x108, ROR4 = 0x124, ROR8 = 0x128, ROR12 = 0x12C;
__device__ __forceinline__ float sw32_add(float a, float b) {
  auto t = __builtin_amdgcn_permlane32_swap(asu(a), asu(b), false, false); return asf(t[0]) + asf(t[1]); }
__device__ __forceinline__ float sw16_add(float a, float b) {
  auto t = __builtin_amdgcn_permlane16_swap(asu(a), asu(b), false, false); return asf(t[0]) + asf(t[1]); }
__device__ __forceinline__ float sw32_max(float a) {
  auto t = __builtin_amdgcn_permlane32_swap(asu(a), asu(a), false, false); return fmaxf(asf(t[0]), asf(t[1])); }
__device__ __forceinline__ float sw16_max(float a) {
  auto t = __builtin_amdgcn_permlane16_swap(asu(a), asu(a), false, false); return fmaxf(asf(t[0]), asf(t[1])); }
__device__ __forceinline__ float wave_sum(float x) {
  x += dppf<XOR1>(x); x += dppf<XOR2>(x); x += dppf<HMIR>(x); x += dppf<ROR8>(x); x = sw16_add(x, x); return sw32_add(x, x); }
__device__ __forceinline__ float dot2(unsigned a, unsigned b) {
  return __builtin_amdgcn_fdot2_f32_bf16(__builtin_bit_cast(bf16x2, a), __builtin_bit_cast(bf16x2, b), 0.f, false); }
__device__ __forceinline__ v4f mfma(v4u a, v4u b, v4f c) {
  return __builtin_amdgcn_mfma_f32_16x16x32_bf16(__builtin_bit_cast(bf16x8, a), __builtin_bit_cast(bf16x8, b), c, 0, 0, 0); }
__device__ __forceinline__ float exp2f_(float x) { return __builtin_amdgcn_exp2f(x); }
__device__ __forceinline__ float rcpf_(float x) { return __builtin_amdgcn_rcpf(x); }
#define SB() __builtin_amdgcn_sched_barrier(0)
#define VMWAIT(n) __builtin_amdgcn_s_waitcnt(0x0F70 | ((n) & 15) | (((n) >> 4) << 14))
#define LDS_BARRIER() do { SB(); __builtin_amdgcn_s_waitcnt(0xC07F); __builtin_amdgcn_s_barrier(); SB(); asm volatile("" ::: "memory"); } while (0)

struct Tag { unsigned a, b; };
__device__ __forceinline__ bool fresh(v4u r, Tag t) { return (r.y == t.a) & (r.w == t.b); }
__device__ __forceinline__ v4u rec(unsigned p0, unsigned p1, Tag t) { return v4u{p0, t.a, p1, t.b}; }

template <int N> __device__ __forceinline__ bool all_fresh(const v4u (&r)[N], Tag t) {
  bool ok = true;
#pragma unroll
  for (int k = 0; k < N; ++k) ok = ok & fresh(r[k], t);
  return ok;
}
template <int N> __device__ __forceinline__ void await(v4u (&r)[N], const int (&off)[N], rsrc_t ws, Tag t) {
  bool ok = all_fresh<N>(r, t);
  if (!__all(ok)) {
    unsigned spin = 0;
#pragma nounroll
    do {
      asm volatile("" ::: "memory");
      if (!ok) {
#pragma unroll
        for (int k = 0; k < N; ++k) r[k] = ld16<AUX_SC1>(ws, off[k], 0);
      }
      ok = all_fresh<N>(r, t);
    } while (!__all(ok) && ++spin < SPIN_CAP);
  }
  asm volatile("" ::: "memory");
}

__device__ __forceinline__ void ln_stats(const float (&v)[8], float& m, float& rs) {
  float s = ((v[0] + v[1]) + (v[2] + v[3])) + ((v[4] + v[5]) + (v[6] + v[7]));
  const float mf = wave_sum(s) * (1.f / D);
  float q = 0.f;
#pragma unroll
  for (int j = 0; j < 8; ++j) { const float d = v[j] - mf; q = fmaf(d, d, q); }
  const float var = rbf(wave_sum(q) * (1.f / D));
  m = rbf(mf);
  rs = 1.f / rbf(sqrtf(rbf(var + 1e-5f)));
}
__device__ __forceinline__ float ln_apply(float x, float m, float rs) { return rbf(rbf(x - m) * rs); }

struct WPair { unsigned p[8]; };
__device__ __forceinline__ WPair pair_rows(v4u w0, v4u w1) {
  WPair o;
#pragma unroll
  for (int t = 0; t < 4; ++t) { o.p[2 * t] = __builtin_amdgcn_perm(w1[t], w0[t], 0x05040100u); o.p[2 * t + 1] = __builtin_amdgcn_perm(w1[t], w0[t], 0x07060302u); }
  return o;
}
__device__ __forceinline__ v2f block_product(const WPair& w, unsigned in0, unsigned in1, bool b3) {
  float a[8];
#pragma unroll
  for (int j = 0; j < 8; ++j) a[j] = sw32_add(dot2(w.p[j], in0), dot2(w.p[j], in1));
  float c[4];
#pragma unroll
  for (int j = 0; j < 4; ++j) c[j] = sw16_add(a[j], a[j + 4]);
  v2f o;
#pragma unroll
  for (int j = 0; j < 2; ++j) { const float x = c[j] + dppf<ROR8>(c[j]), y = c[j + 2] + dppf<ROR8>(c[j + 2]); o[j] = b3 ? y : x; }
  return o;
}
__device__ __forceinline__ v2f sum8(const v2f* p) {
  v2f a[8];
#pragma unroll
  for (int w = 0; w < 8; ++w) a[w] = p[w * 64];
  return ((a[0] + a[1]) + (a[2] + a[3])) + ((a[4] + a[5]) + (a[6] + a[7]));
}

#define ST(i) do { if ((i) == HOLD_AT && (HOLD_COND)) { SB(); while (__builtin_amdgcn_s_memrealtime() < t_start + HOLD_US * 100) __builtin_amdgcn_s_sleep(1); SB(); } } while (0)

__global__ void __launch_bounds__(512, 2)
k_fused(const void* __restrict__ x, const void* __restrict__ kc, const void* __restrict__ vc, const void* __restrict__ wq, const void* __restrict__ wo,
        const void* __restrict__ w1, const void* __restrict__ b1, const void* __restrict__ w2, const void* __restrict__ b2, void* out, void* wsp) {
  __shared__ __attribute__((aligned(16))) float lds[4096 + 1024 + 1024 + 256 + 64];
  float* const l_o = lds;
  v2f* const l_r0 = (v2f*)(lds + 4096);
  v2f* const l_r1 = (v2f*)(lds + 5120);
  unsigned* const l_q = (unsigned*)(lds + 6144);
  v2f* const l_ml = (v2f*)(lds + 6400);

  const int tid = threadIdx.x, lane = tid & 63, w = __builtin_amdgcn_readfirstlane(tid >> 6);
  const int blk = blockIdx.x, x8 = blk & 7, i32 = blk >> 3, rg = i32 >> 3, k8 = i32 & 7, bat = i32 >> 4;
  const int e = x8 + 8 * k8;
  const bool is1 = e < 28, is2 = e >= 28 && e < 56;
  const int id2 = rg * 28 + e - 28, rb = id2 >> 3, c2 = id2 & 7;
  const int r = lane >> 3, ch = lane & 7;
  const bool b3 = (lane & 8) != 0;
  const u64 t_start = __builtin_amdgcn_s_memrealtime();
  ST(0);

  const rsrc_t rx = mk(x, 2 * D * 2), rk = mk(kc, KV_BYTES), rv = mk(vc, KV_BYTES), rws = mk(wsp, WS_END);
  const rsrc_t rq = mk(wq, D * HQ * DH * 2), ro = mk(wo, D * HQ * DH * 2);
  const rsrc_t r1 = mk(w1, is1 ? D * F * 2 : 0), r2 = mk(w2, is2 ? D * F * 2 : 0), rb1 = mk(b1, is2 ? F * 2 : 0), rb2 = mk(b2, D * 2);

  const v2u ep = __builtin_amdgcn_raw_buffer_load_b64(rws, 0, WS_E, AUX_SC1);
  const v4u xa0 = ld16<0>(rx, lane * 16, 0), xa1 = ld16<0>(rx, lane * 16, D * 2);
  const unsigned xr0 = ld4(rx, r * 4, (rg * 64 + w * 8) * 4), xr1 = ld4(rx, r * 4, (rg * 64 + w * 8) * 4 + D * 2);
  const int row0 = rg * 128 + w * 16;
  const v4u wq0 = ld16<AUX_NT>(rq, r * 2 * 8192 + ch * 16, row0 * 8192 + (x8 * 8 + k8) * 128);
  const v4u wq1 = ld16<AUX_NT>(rq, r * 2 * 8192 + ch * 16, row0 * 8192 + (x8 * 8 + k8) * 128 + 8192);
  const v4u wa0 = ld16<AUX_NT>(r1, r * 2 * 3584 + ch * 16, row0 * 3584 + e * 128);
  const v4u wa1 = ld16<AUX_NT>(r1, r * 2 * 3584 + ch * 16, row0 * 3584 + e * 128 + 3584);
  SB();
  const int kvbase = ((bat * HKV + x8) * SK + (i32 & 15) * 512) * 256 + w * 1024;
  const int koff = ((lane >> 2) & 3) * 256 + (lane >> 4) * 64 + (lane & 3) * 16, voff = lane * 16;
  v4u K[16], V[16];
#pragma unroll
  for (int n = 0; n < KFRONT; ++n) { K[n] = ld16<AUX_NT>(rk, koff, kvbase + n * 8192); SB(); }

  Tag tag;
  tag.a = __builtin_amdgcn_readfirstlane(ep.x) + 1u; tag.b = __builtin_amdgcn_readfirstlane(ep.y) + 0x9E3779B9u;
  {
    float v0[8], v1[8];
#pragma unroll
    for (int t = 0; t < 4; ++t) { v0[2 * t] = lo16(xa0[t]); v0[2 * t + 1] = hi16(xa0[t]); v1[2 * t] = lo16(xa1[t]); v1[2 * t + 1] = hi16(xa1[t]); }
    float m0, s0, m1, s1;
    ln_stats(v0, m0, s0); ln_stats(v1, m1, s1);
    const float n00 = ln_apply(lo16(xr0), m0, s0), n01 = ln_apply(hi16(xr0), m0, s0);
    const float n10 = ln_apply(lo16(xr1), m1, s1), n11 = ln_apply(hi16(xr1), m1, s1);
    l_r0[w * 64 + lane] = block_product(pair_rows(wq0, wq1), pk(n00, n01), pk(n10, n11), b3);
    if (is1) {
#pragma unroll
      for (int j = 0; j < 8; ++j) { v0[j] = ln_apply(v0[j], m0, s0); v1[j] = ln_apply(v1[j], m1, s1); }
      ln_stats(v0, m0, s0); ln_stats(v1, m1, s1);
      l_r1[w * 64 + lane] = block_product(pair_rows(wa0, wa1), pk(ln_apply(n00, m0, s0), ln_apply(n01, m0, s0)),
                                          pk(ln_apply(n10, m1, s1), ln_apply(n11, m1, s1)), b3);
    }
  }
  asm volatile("" :: "v"(wa0), "v"(wa1));
  LDS_BARRIER();
  ST(1);
  const int recoff = ((lane >> 5) * 32 + ch * 4 + (r & 3)) * 16;
  if (w == 0) { const v2f s = sum8(l_r0 + lane); st16(rec(asu(s.x), asu(s.y), tag), rws, recoff, WS_QP + (rg * 64 + x8 * 8 + k8) * BLK); }
  if (w == 1 && is1) { const v2f s = sum8(l_r1 + lane); st16(rec(asu(s.x), asu(s.y), tag), rws, recoff, WS_HP + (rg * 28 + e) * BLK); }
  SB();

  v4u qr[2];
  int qo[2];
#pragma unroll
  for (int k = 0; k < 2; ++k) qo[k] = WS_QP + ((2 * k + (lane >> 5)) * 64 + x8 * 8 + w) * BLK + (bat * 32 + (lane & 31)) * 16;
#pragma unroll
  for (int n = KFRONT; n < QPOS; ++n) {
    VMWAIT(WIN - 1);
    if (n < 16) K[n] = ld16<AUX_NT>(rk, koff, kvbase + n * 8192);
    else V[n - 16] = ld16<AUX_NT>(rv, voff, kvbase + (n - 16) * 8192);
    if (n == QPOS - WIN) { qr[0] = ld16<AUX_SC1>(rws, qo[0], 0); qr[1] = ld16<AUX_SC1>(rws, qo[1], 0); }
    SB();
  }
  ST(2);
  await<2>(qr, qo, rws, tag);
  {
    const float q0 = asf(qr[0].x) + asf(qr[1].x), q1 = asf(qr[0].z) + asf(qr[1].z);
    const unsigned qp = pk(sw32_add(q0, q0), sw32_add(q1, q1));
    if (lane < 32) l_q[w * 32 + lane] = qp;
  }
  LDS_BARRIER();
  ST(3);
  const int bank = (lane >> 2) & 3, hh = lane & 3;
  const v4u qf = *(const v4u*)(l_q + hh * 64 + (lane >> 4) * 16 + bank * 4);
  float S[4];
#pragma unroll
  for (int n = 0; n < 16; ++n) {
    const v4f d = mfma(K[n], qf, v4f{0.f, 0.f, 0.f, 0.f});
    const float t = d[0] + dppf<SHL4>(d[1]), u = d[2] + dppf<SHL4>(d[3]);
    const float s = t + dppf<SHL8>(u);
    if ((n & 3) == 0) S[n >> 2] = s;
    else if ((n & 3) == 1) S[n >> 2] = to_bank<1>(S[n >> 2], s);
    else if ((n & 3) == 2) S[n >> 2] = to_bank<2>(S[n >> 2], s);
    else S[n >> 2] = to_bank<3>(S[n >> 2], s);
  }
  float mw = fmaxf(fmaxf(S[0], S[1]), fmaxf(S[2], S[3]));
  mw = fmaxf(mw, dppf<ROR4>(mw)); mw = fmaxf(mw, dppf<ROR8>(mw)); mw = sw32_max(sw16_max(mw));
  mw *= SC2;
  float lw = 0.f;
#pragma unroll
  for (int z = 0; z < 4; ++z) { S[z] = exp2f_(fmaf(S[z], SC2, -mw)); lw += S[z]; }
  lw += dppf<ROR4>(lw); lw += dppf<ROR8>(lw); lw = sw16_add(lw, lw); lw = sw32_add(lw, lw);
  if (lane < 4) l_ml[lane * 8 + w] = v2f{mw, lw};
  SB();
#pragma unroll
  for (int n = QPOS; n < QPOS + 2; ++n) { V[n - 16] = ld16<AUX_NT>(rv, voff, kvbase + (n - 16) * 8192); SB(); }
  LDS_BARRIER();
  float lse;
  unsigned P[4];
  {
    v4f ml[4];
#pragma unroll
    for (int t = 0; t < 4; ++t) ml[t] = *(const v4f*)(l_ml + hh * 8 + 2 * t);
    float M = fmaxf(fmaxf(fmaxf(ml[0][0], ml[0][2]), fmaxf(ml[1][0], ml[1][2])), fmaxf(fmaxf(ml[2][0], ml[2][2]), fmaxf(ml[3][0], ml[3][2])));
    float L = 0.f;
#pragma unroll
    for (int t = 0; t < 4; ++t) L += exp2f_(ml[t][0] - M) * ml[t][1] + exp2f_(ml[t][2] - M) * ml[t][3];
    const float f = exp2f_(mw - M) * rcpf_(L);
    lse = M + __builtin_amdgcn_logf(L);
#pragma unroll
    for (int z = 0; z < 4; ++z) { const float p = S[z] * f; P[z] = pk(p, dppf<SHL4>(p)); }
  }
  unsigned bm[4];
#pragma unroll
  for (int u = 0; u < 4; ++u) bm[u] = bank == u ? 0xffffffffu : 0u;
  v4u Bf[2][4];
#pragma unroll
  for (int be = 0; be < 2; ++be) {
    const unsigned p0 = P[2 * be], p1 = P[2 * be + 1];
    Bf[be][0] = v4u{p0 & bm[0], dppu<ROR8>(p0) & bm[0], p1 & bm[0], dppu<ROR8>(p1) & bm[0]};
    Bf[be][1] = v4u{dppu<ROR4>(p0) & bm[1], dppu<ROR12>(p0) & bm[1], dppu<ROR4>(p1) & bm[1], dppu<ROR12>(p1) & bm[1]};
    Bf[be][2] = v4u{dppu<ROR8>(p0) & bm[2], p0 & bm[2], dppu<ROR8>(p1) & bm[2], p1 & bm[2]};
    Bf[be][3] = v4u{dppu<ROR12>(p0) & bm[3], dppu<ROR4>(p0) & bm[3], dppu<ROR12>(p1) & bm[3], dppu<ROR4>(p1) & bm[3]};
  }
  SB();
  ST(4);
#pragma unroll
  for (int n = QPOS + 2; n < 32; ++n) { VMWAIT(WIN - 1); V[n - 16] = ld16<AUX_NT>(rv, voff, kvbase + (n - 16) * 8192); SB(); }

  v4u hr[1];
  const int ho[1] = {WS_HP + ((ch & 3) * 28 + 2 * rb + (w >> 2)) * BLK + ((ch >> 2) * 32 + (w & 3) * 8 + r) * 16};
  const int hg = x8 * 4 + rg;
  VMWAIT(WIN - 1);
  const v4u wo0 = ld16<AUX_NT>(ro, r * 2048 + ch * 16, (hg * 128 + w * 16) * 1024 + k8 * 128);
  SB(); VMWAIT(WIN - 1);
  const v4u wo1 = ld16<AUX_NT>(ro, r * 2048 + ch * 16, (hg * 128 + w * 16) * 1024 + k8 * 128 + 1024);
  SB();
  const v4u wb0 = ld16<AUX_NT>(r2, r * 2048 + ch * 16, (rb * 128 + w * 16) * 1024 + c2 * 128);
  const v4u wb1 = ld16<AUX_NT>(r2, r * 2048 + ch * 16, (rb * 128 + w * 16) * 1024 + c2 * 128 + 1024);
  hr[0] = ld16<AUX_SC1>(is2 ? rws : r2, ho[0], 0);
  const unsigned bias1 = ld4(rb1, r * 4, (rb * 64 + w * 8) * 4);
  const int ocol = x8 * 64 + (i32 & 15) * 4;
  const unsigned xo = ld4(rx, (lane & 1) * 4, (bat * D + ocol) * 2), bias2 = ld4(rb2, (lane & 1) * 4, ocol * 2);
  SB();
  ST(5);

  v4f acc[2] = {v4f{0.f, 0.f, 0.f, 0.f}, v4f{0.f, 0.f, 0.f, 0.f}};
#pragma unroll
  for (int be = 0; be < 2; ++be) {
    v4u A[8];
#pragma unroll
    for (int t = 0; t < 4; ++t)
#pragma unroll
      for (int u = 0; u < 4; ++u) {
        A[2 * u][t] = __builtin_amdgcn_perm(V[8 * be + 2 * t + 1][u], V[8 * be + 2 * t][u], 0x05040100u);
        A[2 * u + 1][t] = __builtin_amdgcn_perm(V[8 * be + 2 * t + 1][u], V[8 * be + 2 * t][u], 0x07060302u);
      }
#pragma unroll
    for (int u = 0; u < 4; ++u) { acc[0] = mfma(A[2 * u], Bf[be][u], acc[0]); acc[1] = mfma(A[2 * u + 1], Bf[be][u], acc[1]); }
  }
  *(v4f*)(l_o + w * 512 + lane * 4) = v4f{acc[0][0], acc[1][0], acc[0][1], acc[1][1]};
  *(v4f*)(l_o + w * 512 + 256 + lane * 4) = v4f{acc[0][2], acc[1][2], acc[0][3], acc[1][3]};
  const float lse_m = asf(dppu<0x50>(asu(lse))), lse_n = asf(dppu<0xFA>(asu(lse)));
  LDS_BARRIER();
  ST(6);
  if (w < 4) {
    v2f a[8];
#pragma unroll
    for (int ww = 0; ww < 8; ++ww) a[ww] = *(const v2f*)(l_o + ww * 512 + (lane >> 5) * 256 + w * 64 + (lane & 31) * 2);
    const v2f s = ((a[0] + a[1]) + (a[2] + a[3])) + ((a[4] + a[5]) + (a[6] + a[7]));
    const int mh = (lane >> 1) & 3, mu = (lane >> 3) & 3, mq = (lane >> 5) * 2 + (lane & 1);
    const float ls = (lane & 4) ? lse_n : lse_m;
    st16(rec(pk(s.x, s.y), asu(ls), tag), rws, mh * 32 * BLK + (w * 16 + mq * 4 + mu) * 16, WS_R1 + (x8 * 4 * 32 + i32) * BLK);
  }
  SB();
  ST(7);
  v4u pr[4];
  int po[4];
#pragma unroll
  for (int k = 0; k < 4; ++k) { po[k] = WS_R1 + (hg * 32 + k * 8 + ch) * BLK + (w * 8 + r) * 16; pr[k] = ld16<AUX_SC1>(rws, po[k], 0); }
  SB();
  if (is2) {
    await<1>(hr, ho, rws, tag);
    float h0 = asf(hr[0].x), h1 = asf(hr[0].z);
    h0 += dppf<XOR1>(h0); h1 += dppf<XOR1>(h1); h0 += dppf<XOR2>(h0); h1 += dppf<XOR2>(h1);
    h0 = rbf(rbf(h0) + lo16(bias1)); h1 = rbf(rbf(h1) + hi16(bias1));
    constexpr float G0 = -2.f * 0.7978845608028654f * LOG2E, G1 = G0 * 0.044715f;
    h0 = h0 * rcpf_(1.f + exp2f_(h0 * fmaf(G1, h0 * h0, G0))); h1 = h1 * rcpf_(1.f + exp2f_(h1 * fmaf(G1, h1 * h1, G0)));
    const unsigned hp = pk(h0, h1), hx = dppu<HMIR>(hp);
    l_r1[w * 64 + lane] = block_product(pair_rows(wb0, wb1), (ch & 4) ? hx : hp, (ch & 4) ? hp : hx, b3);
  }
  asm volatile("" :: "v"(wb0), "v"(wb1), "v"(hr[0]), "v"(bias1), "v"(xo), "v"(bias2));
  const WPair wop = pair_rows(wo0, wo1);
  SB();
  ST(8);
  await<4>(pr, po, rws, tag);
  ST(9);
  unsigned on[2];
#pragma unroll
  for (int b = 0; b < 2; ++b) {
    const float l0 = asf(pr[2 * b].z), l1 = asf(pr[2 * b + 1].z);
    float M = fmaxf(l0, l1);
    M = fmaxf(M, dppf<XOR1>(M)); M = fmaxf(M, dppf<XOR2>(M)); M = fmaxf(M, dppf<HMIR>(M));
    const float e0 = exp2f_(l0 - M), e1 = exp2f_(l1 - M);
    float z = e0 + e1, o0 = fmaf(e0, lo16(pr[2 * b].x), e1 * lo16(pr[2 * b + 1].x)), o1 = fmaf(e0, hi16(pr[2 * b].x), e1 * hi16(pr[2 * b + 1].x));
    z += dppf<XOR1>(z); o0 += dppf<XOR1>(o0); o1 += dppf<XOR1>(o1);
    z += dppf<XOR2>(z); o0 += dppf<XOR2>(o0); o1 += dppf<XOR2>(o1);
    z += dppf<HMIR>(z); o0 += dppf<HMIR>(o0); o1 += dppf<HMIR>(o1);
    const float iz = rcpf_(z);
    on[b] = pk(o0 * iz, o1 * iz);
  }
  l_r0[w * 64 + lane] = block_product(wop, on[0], on[1], b3);
  LDS_BARRIER();
  ST(10);
  if (w == 0) { const v2f s = sum8(l_r0 + lane); st16(rec(asu(s.x), asu(s.y), tag), rws, recoff, WS_SP + (hg * 8 + k8) * BLK); }
  if (w == 1 && is2) { const v2f s = sum8(l_r1 + lane); st16(rec(asu(s.x), asu(s.y), tag), rws, recoff, WS_GP + (rb * 8 + c2) * BLK); }
  if (w == 2) {
    const int src = lane >> 1, irec = (bat * 32 + (i32 & 15) * 2 + (lane & 1)) * 16;
    v4u fs[1], fg[1];
    const int so[1] = {WS_SP + (src * 8 + x8) * BLK + irec}, go[1] = {WS_GP + ((src < 14 ? src : 0) * 8 + x8) * BLK + irec};
    fs[0] = ld16<AUX_SC1>(rws, so[0], 0); fg[0] = ld16<AUX_SC1>(rws, go[0], 0);
    await<1>(fg, go, rws, tag);
    await<1>(fs, so, rws, tag);
    ST(11);
    float s0 = asf(fs[0].x), s1 = asf(fs[0].z), g0 = src < 14 ? asf(fg[0].x) : 0.f, g1 = src < 14 ? asf(fg[0].z) : 0.f;
    float sa = sw32_add(s0, g0), sb = sw32_add(s1, g1);
    float sc = sw16_add(sa, sb);
    sc += dppf<ROR8>(sc); sc += dppf<ROR4>(sc); sc += dppf<XOR2>(sc);
    auto t = __builtin_amdgcn_permlane32_swap(asu(sc), asu(sc), false, false);
    const float sv = rbf(asf(t[0]));
    const bool odd = (lane & 16) != 0;
    const float mv = rbf(rbf(asf(t[1])) + (odd ? hi16(bias2) : lo16(bias2)));
    const float gv = rbf(mv * rcpf_(1.f + exp2f_(-LOG2E * mv)));
    const float ov = (odd ? hi16(xo) : lo16(xo)) + rbf(sv * gv);
    auto u = __builtin_amdgcn_permlane16_swap(asu(ov), asu(ov), false, false);
    if (lane < 2) ((unsigned*)out)[(bat * D + ocol) / 2 + lane] = pk(asf(u[0]), asf(u[1]));
    if (blk == 0 && lane == 0) __builtin_amdgcn_raw_buffer_store_b64(v2u{tag.a, tag.b}, rws, 0, WS_E, AUX_SC1);
    ST(12);
  }
}
}

extern "C" void kernel_launch(void* const* d_in, const int* in_sizes, int n_in, void* d_out, int out_size, void* d_ws, size_t ws_size,
                              hipStream_t stream) {
  fused::k_fused<<<256, 512, 0, stream>>>(d_in[0], d_in[1], d_in[2], d_in[3], d_in[4], d_in[6], d_in[7], d_in[8], d_in[9], d_out, d_ws);
}
```

```cpp
#include <hip/hip_runtime.h>
#include <hip/hip_bf16.h>
#include <stdint.h>

#define WIN 6
#define QPOS 20
#define KFRONT 4

namespace fused {
typedef unsigned v4u __attribute__((ext_vector_type(4)));
typedef unsigned v2u __attribute__((ext_vector_type(2)));
typedef float v4f __attribute__((ext_vector_type(4)));
typedef float v2f __attribute__((ext_vector_type(2)));
typedef short s16x8 __attribute__((ext_vector_type(8)));
typedef __bf16 bf16x2 __attribute__((ext_vector_type(2)));
typedef __bf16 bf16x8 __attribute__((ext_vector_type(8)));
typedef __amdgpu_buffer_rsrc_t rsrc_t;
typedef unsigned long long u64;

constexpr int D = 512, HQ = 32, HKV = 8, SK = 8192, DH = 128, F = 1792;
constexpr int AUX_NT = 2, AUX_SC1 = 16;
constexpr int KV_BYTES = 2 * HKV * SK * DH * 2;
constexpr int BLK = 1024;
constexpr int WS_E = 0, WS_QP = 4096, WS_HP = WS_QP + 4 * 64 * BLK, WS_R1 = WS_HP + 4 * 28 * BLK, WS_SP = WS_R1 + 32 * 32 * BLK,
              WS_GP = WS_SP + 32 * 8 * BLK, WS_END = WS_GP + 14 * 8 * BLK;
constexpr unsigned SPIN_CAP = 1u << 18;
constexpr float LOG2E = 1.4426950408889634f;
constexpr float SC2 = 0.08838834764831845f * LOG2E;

__device__ __forceinline__ rsrc_t mk(const void* p, int bytes) { return __builtin_amdgcn_make_buffer_rsrc((void*)p, 0, bytes, 0x00020000); }
template <int AUX> __device__ __forceinline__ v4u ld16(rsrc_t r, int voff, int soff) { return __builtin_amdgcn_raw_buffer_load_b128(r, voff, soff, AUX); }
__device__ __forceinline__ unsigned ld4(rsrc_t r, int voff, int soff) { return __builtin_amdgcn_raw_buffer_load_b32(r, voff, soff, 0); }
__device__ __forceinline__ void st16(v4u v, rsrc_t r, int voff, int soff) { __builtin_amdgcn_raw_buffer_store_b128(v, r, voff, soff, AUX_SC1); }
__device__ __forceinline__ float asf(unsigned u) { return __builtin_bit_cast(float, u); }
__device__ __forceinline__ unsigned asu(float f) { return __builtin_bit_cast(unsigned, f); }
__device__ __forceinline__ float lo16(unsigned u) { return asf(u << 16); }
__device__ __forceinline__ float hi16(unsigned u) { return asf(u & 0xffff0000u); }
__device__ __forceinline__ unsigned pk(float a, float b) { return __builtin_bit_cast(unsigned, bf16x2{(__bf16)a, (__bf16)b}); }
__device__ __forceinline__ float rbf(float a) { return (float)(__bf16)a; }
template <int C> __device__ __forceinline__ unsigned dppu(unsigned x) { return (unsigned)__builtin_amdgcn_mov_dpp((int)x, C, 0xf, 0xf, true); }
template <int C> __device__ __forceinline__ float dppf(float x) { return asf(dppu<C>(asu(x))); }
template <int I> __device__ __forceinline__ float to_bank(float old, float src) {
  return asf((unsigned)__builtin_amdgcn_update_dpp((int)asu(old), (int)asu(src), 0x110 + 4 * I, 0xf, 1 << I, false)); }
constexpr int XOR1 = 0xB1, XOR2 = 0x4E, HMIR = 0x141, SHL4 = 0x104, SHL8 = 0x108, ROR4 = 0x124, ROR8 = 0x128, ROR12 = 0x12C;
__device__ __forceinline__ float sw32_add(float a, float b) {
  auto t = __builtin_amdgcn_permlane32_swap(asu(a), asu(b), false, false); return asf(t[0]) + asf(t[1]); }
__device__ __forceinline__ float sw16_add(float a, float b) {
  auto t = __builtin_amdgcn_permlane16_swap(asu(a), asu(b), false, false); return asf(t[0]) + asf(t[1]); }
__device__ __forceinline__ float sw32_max(float a) {
  auto t = __builtin_amdgcn_permlane32_swap(asu(a), asu(a), false, false); return fmaxf(asf(t[0]), asf(t[1])); }
__device__ __forceinline__ float sw16_max(float a) {
  auto t = __builtin_amdgcn_permlane16_swap(asu(a), asu(a), false, false); return fmaxf(asf(t[0]), asf(t[1])); }
__device__ __forceinline__ float wave_sum(float x) {
  x += dppf<XOR1>(x); x += dppf<XOR2>(x); x += dppf<HMIR>(x); x += dppf<ROR8>(x); x = sw16_add(x, x); return sw32_add(x, x); }
__device__ __forceinline__ float dot2(unsigned a, unsigned b) {
  return __builtin_amdgcn_fdot2_f32_bf16(__builtin_bit_cast(bf16x2, a), __builtin_bit_cast(bf16x2, b), 0.f, false); }
__device__ __forceinline__ v4f mfma(v4u a, v4u b, v4f c) {
  return __builtin_amdgcn_mfma_f32_16x16x32_bf16(__builtin_bit_cast(bf16x8, a), __builtin_bit_cast(bf16x8, b), c, 0, 0, 0); }
__device__ __forceinline__ float exp2f_(float x) { return __builtin_amdgcn_exp2f(x); }
__device__ __forceinline__ float rcpf_(float x) { return __builtin_amdgcn_rcpf(x); }
#define SB() __builtin_amdgcn_sched_barrier(0)
#define VMWAIT(n) __builtin_amdgcn_s_waitcnt(0x0F70 | ((n) & 15) | (((n) >> 4) << 14))
#define LDS_BARRIER() do { SB(); __builtin_amdgcn_s_waitcnt(0xC07F); __builtin_amdgcn_s_barrier(); SB(); asm volatile("" ::: "memory"); } while (0)

struct Tag { unsigned a, b; };
__device__ __forceinline__ bool fresh(v4u r, Tag t) { return (r.y == t.a) & (r.w == t.b); }
__device__ __forceinline__ v4u rec(unsigned p0, unsigned p1, Tag t) { return v4u{p0, t.a, p1, t.b}; }

template <int N> __device__ __forceinline__ bool all_fresh(const v4u (&r)[N], Tag t) {
  bool ok = true;
#pragma unroll
  for (int k = 0; k < N; ++k) ok = ok & fresh(r[k], t);
  return ok;
}
template <int N> __device__ __forceinline__ void await(v4u (&r)[N], const int (&off)[N], rsrc_t ws, Tag t) {
  bool ok = all_fresh<N>(r, t);
  if (!__all(ok)) {
    unsigned spin = 0;
#pragma nounroll
    do {
      asm volatile("" ::: "memory");
      if (!ok) {
#pragma unroll
        for (int k = 0; k < N; ++k) r[k] = ld16<AUX_SC1>(ws, off[k], 0);
      }
      ok = all_fresh<N>(r, t);
    } while (!__all(ok) && ++spin < SPIN_CAP);
  }
  asm volatile("" ::: "memory");
}

__device__ __forceinline__ void ln_stats(const float (&v)[8], float& m, float& rs) {
  float s = ((v[0] + v[1]) + (v[2] + v[3])) + ((v[4] + v[5]) + (v[6] + v[7]));
  const float mf = wave_sum(s) * (1.f / D);
  float q = 0.f;
#pragma unroll
  for (int j = 0; j < 8; ++j) { const float d = v[j] - mf; q = fmaf(d, d, q); }
  const float var = rbf(wave_sum(q) * (1.f / D));
  m = rbf(mf);
  rs = 1.f / rbf(sqrtf(rbf(var + 1e-5f)));
}
__device__ __forceinline__ float ln_apply(float x, float m, float rs) { return rbf(rbf(x - m) * rs); }

struct WPair { unsigned p[8]; };
__device__ __forceinline__ WPair pair_rows(v4u w0, v4u w1) {
  WPair o;
#pragma unroll
  for (int t = 0; t < 4; ++t) { o.p[2 * t] = __builtin_amdgcn_perm(w1[t], w0[t], 0x05040100u); o.p[2 * t + 1] = __builtin_amdgcn_perm(w1[t], w0[t], 0x07060302u); }
  return o;
}
__device__ __forceinline__ v2f block_product(const WPair& w, unsigned in0, unsigned in1, bool b3) {
  float a[8];
#pragma unroll
  for (int j = 0; j < 8; ++j) a[j] = sw32_add(dot2(w.p[j], in0), dot2(w.p[j], in1));
  float c[4];
#pragma unroll
  for (int j = 0; j < 4; ++j) c[j] = sw16_add(a[j], a[j + 4]);
  v2f o;
#pragma unroll
  for (int j = 0; j < 2; ++j) { const float x = c[j] + dppf<ROR8>(c[j]), y = c[j + 2] + dppf<ROR8>(c[j + 2]); o[j] = b3 ? y : x; }
  return o;
}
__device__ __forceinline__ v2f sum8(const v2f* p) {
  v2f a[8];
#pragma unroll
  for (int w = 0; w < 8; ++w) a[w] = p[w * 64];
  return ((a[0] + a[1]) + (a[2] + a[3])) + ((a[4] + a[5]) + (a[6] + a[7]));
}

#define ST(i)

__global__ void __launch_bounds__(512, 2)
k_fused(const void* __restrict__ x, const void* __restrict__ kc, const void* __restrict__ vc, const void* __restrict__ wq, const void* __restrict__ wo,
        const void* __restrict__ w1, const void* __restrict__ b1, const void* __restrict__ w2, const void* __restrict__ b2, void* out, void* wsp) {
  __shared__ __attribute__((aligned(16))) float lds[4096 + 1024 + 1024 + 256 + 64];
  float* const l_o = lds;
  v2f* const l_r0 = (v2f*)(lds + 4096);
  v2f* const l_r1 = (v2f*)(lds + 5120);
  unsigned* const l_q = (unsigned*)(lds + 6144);
  v2f* const l_ml = (v2f*)(lds + 6400);

  const int tid = threadIdx.x, lane = tid & 63, w = __builtin_amdgcn_readfirstlane(tid >> 6);
  const int blk = blockIdx.x, x8 = blk & 7, i32 = blk >> 3, rg = i32 >> 3, k8 = i32 & 7, bat = i32 >> 4;
  const int e = x8 + 8 * k8;
  const bool is1 = e < 28, is2 = e >= 28 && e < 56;
  const int id2 = rg * 28 + e - 28, rb = id2 >> 3, c2 = id2 & 7;
  const int r = lane >> 3, ch = lane & 7;
  const bool b3 = (lane & 8) != 0;
  ST(0);

  const rsrc_t rx = mk(x, 2 * D * 2), rk = mk(kc, KV_BYTES), rv = mk(vc, KV_BYTES), rws = mk(wsp, WS_END);
  const rsrc_t rq = mk(wq, D * HQ * DH * 2), ro = mk(wo, D * HQ * DH * 2);
  const rsrc_t r1 = mk(w1, is1 ? D * F * 2 : 0), r2 = mk(w2, is2 ? D * F * 2 : 0), rb1 = mk(b1, is2 ? F * 2 : 0), rb2 = mk(b2, D * 2);

  const v2u ep = __builtin_amdgcn_raw_buffer_load_b64(rws, 0, WS_E, AUX_SC1);
  const v4u xa0 = ld16<0>(rx, lane * 16, 0), xa1 = ld16<0>(rx, lane * 16, D * 2);
  const unsigned xr0 = ld4(rx, r * 4, (rg * 64 + w * 8) * 4), xr1 = ld4(rx, r * 4, (rg * 64 + w * 8) * 4 + D * 2);
  const int row0 = rg * 128 + w * 16;
  const v4u wq0 = ld16<AUX_NT>(rq, r * 2 * 8192 + ch * 16, row0 * 8192 + (x8 * 8 + k8) * 128);
  const v4u wq1 = ld16<AUX_NT>(rq, r * 2 * 8192 + ch * 16, row0 * 8192 + (x8 * 8 + k8) * 128 + 8192);
  const v4u wa0 = ld16<AUX_NT>(r1, r * 2 * 3584 + ch * 16, row0 * 3584 + e * 128);
  const v4u wa1 = ld16<AUX_NT>(r1, r * 2 * 3584 + ch * 16, row0 * 3584 + e * 128 + 3584);
  SB();
  const int kvbase = ((bat * HKV + x8) * SK + (i32 & 15) * 512) * 256 + w * 1024;
  const int koff = ((lane >> 2) & 3) * 256 + (lane >> 4) * 64 + (lane & 3) * 16, voff = lane * 16;
  v4u K[16], V[16];
#pragma unroll
  for (int n = 0; n < KFRONT; ++n) { K[n] = ld16<AUX_NT>(rk, koff, kvbase + n * 8192); SB(); }

  Tag tag;
  tag.a = __builtin_amdgcn_readfirstlane(ep.x) + 1u; tag.b = __builtin_amdgcn_readfirstlane(ep.y) + 0x9E3779B9u;
  {
    float v0[8], v1[8];
#pragma unroll
    for (int t = 0; t < 4; ++t) { v0[2 * t] = lo16(xa0[t]); v0[2 * t + 1] = hi16(xa0[t]); v1[2 * t] = lo16(xa1[t]); v1[2 * t + 1] = hi16(xa1[t]); }
    float m0, s0, m1, s1;
    ln_stats(v0, m0, s0); ln_stats(v1, m1, s1);
    const float n00 = ln_apply(lo16(xr0), m0, s0), n01 = ln_apply(hi16(xr0), m0, s0);
    const float n10 = ln_apply(lo16(xr1), m1, s1), n11 = ln_apply(hi16(xr1), m1, s1);
    l_r0[w * 64 + lane] = block_product(pair_rows(wq0, wq1), pk(n00, n01), pk(n10, n11), b3);
    if (is1) {
#pragma unroll
      for (int j = 0; j < 8; ++j) { v0[j] = ln_apply(v0[j], m0, s0); v1[j] = ln_apply(v1[j], m1, s1); }
      ln_stats(v0, m0, s0); ln_stats(v1, m1, s1);
      l_r1[w * 64 + lane] = block_product(pair_rows(wa0, wa1), pk(ln_apply(n00, m0, s0), ln_apply(n01, m0, s0)),
                                          pk(ln_apply(n10, m1, s1), ln_apply(n11, m1, s1)), b3);
    }
  }
  asm volatile("" :: "v"(wa0), "v"(wa1));
  LDS_BARRIER();
  ST(1);
  const int recoff = ((lane >> 5) * 32 + ch * 4 + (r & 3)) * 16;
  if (w == 0) { const v2f s = sum8(l_r0 + lane); st16(rec(asu(s.x), asu(s.y), tag), rws, recoff, WS_QP + (rg * 64 + x8 * 8 + k8) * BLK); }
  if (w == 1 && is1) { const v2f s = sum8(l_r1 + lane); st16(rec(asu(s.x), asu(s.y), tag), rws, recoff, WS_HP + (rg * 28 + e) * BLK); }
  SB();

  v4u qr[2];
  int qo[2];
#pragma unroll
  for (int k = 0; k < 2; ++k) qo[k] = WS_QP + ((2 * k + (lane >> 5)) * 64 + x8 * 8 + w) * BLK + (bat * 32 + (lane & 31)) * 16;
#pragma unroll
  for (int n = KFRONT; n < QPOS; ++n) {
    VMWAIT(WIN - 1);
    if (n < 16) K[n] = ld16<AUX_NT>(rk, koff, kvbase + n * 8192);
    else V[n - 16] = ld16<AUX_NT>(rv, voff, kvbase + (n - 16) * 8192);
    if (n == QPOS - WIN) { qr[0] = ld16<AUX_SC1>(rws, qo[0], 0); qr[1] = ld16<AUX_SC1>(rws, qo[1], 0); }
    SB();
  }
  ST(2);
  await<2>(qr, qo, rws, tag);
  {
    const float q0 = asf(qr[0].x) + asf(qr[1].x), q1 = asf(qr[0].z) + asf(qr[1].z);
    const unsigned qp = pk(sw32_add(q0, q0), sw32_add(q1, q1));
    if (lane < 32) l_q[w * 32 + lane] = qp;
  }
  LDS_BARRIER();
  ST(3);
  const int bank = (lane >> 2) & 3, hh = lane & 3;
  const v4u qf = *(const v4u*)(l_q + hh * 64 + (lane >> 4) * 16 + bank * 4);
  float S[4];
#pragma unroll
  for (int n = 0; n < 16; ++n) {
    const v4f d = mfma(K[n], qf, v4f{0.f, 0.f, 0.f, 0.f});
    const float t = d[0] + dppf<SHL4>(d[1]), u = d[2] + dppf<SHL4>(d[3]);
    const float s = t + dppf<SHL8>(u);
    if ((n & 3) == 0) S[n >> 2] = s;
    else if ((n & 3) == 1) S[n >> 2] = to_bank<1>(S[n >> 2], s);
    else if ((n & 3) == 2) S[n >> 2] = to_bank<2>(S[n >> 2], s);
    else S[n >> 2] = to_bank<3>(S[n >> 2], s);
  }
  float mw = fmaxf(fmaxf(S[0], S[1]), fmaxf(S[2], S[3]));
  mw = fmaxf(mw, dppf<ROR4>(mw)); mw = fmaxf(mw, dppf<ROR8>(mw)); mw = sw32_max(sw16_max(mw));
  mw *= SC2;
  float lw = 0.f;
#pragma unroll
  for (int z = 0; z < 4; ++z) { S[z] = exp2f_(fmaf(S[z], SC2, -mw)); lw += S[z]; }
  lw += dppf<ROR4>(lw); lw += dppf<ROR8>(lw); lw = sw16_add(lw, lw); lw = sw32_add(lw, lw);
  if (lane < 4) l_ml[lane * 8 + w] = v2f{mw, lw};
  SB();
#pragma unroll
  for (int n = QPOS; n < QPOS + 2; ++n) { V[n - 16] = ld16<AUX_NT>(rv, voff, kvbase + (n - 16) * 8192); SB(); }
  LDS_BARRIER();
  float lse;
  unsigned P[4];
  {
    v4f ml[4];
#pragma unroll
    for (int t = 0; t < 4; ++t) ml[t] = *(const v4f*)(l_ml + hh * 8 + 2 * t);
    float M = fmaxf(fmaxf(fmaxf(ml[0][0], ml[0][2]), fmaxf(ml[1][0], ml[1][2])), fmaxf(fmaxf(ml[2][0], ml[2][2]), fmaxf(ml[3][0], ml[3][2])));
    float L = 0.f;
#pragma unroll
    for (int t = 0; t < 4; ++t) L += exp2f_(ml[t][0] - M) * ml[t][1] + exp2f_(ml[t][2] - M) * ml[t][3];
    const float f = exp2f_(mw - M) * rcpf_(L);
    lse = M + __builtin_amdgcn_logf(L);
#pragma unroll
    for (int z = 0; z < 4; ++z) { const float p = S[z] * f; P[z] = pk(p, dppf<SHL4>(p)); }
  }
  unsigned bm[4];
#pragma unroll
  for (int u = 0; u < 4; ++u) bm[u] = bank == u ? 0xffffffffu : 0u;
  v4u Bf[2][4];
#pragma unroll
  for (int be = 0; be < 2; ++be) {
    const unsigned p0 = P[2 * be], p1 = P[2 * be + 1];
    Bf[be][0] = v4u{p0 & bm[0], dppu<ROR8>(p0) & bm[0], p1 & bm[0], dppu<ROR8>(p1) & bm[0]};
    Bf[be][1] = v4u{dppu<ROR4>(p0) & bm[1], dppu<ROR12>(p0) & bm[1], dppu<ROR4>(p1) & bm[1], dppu<ROR12>(p1) & bm[1]};
    Bf[be][2] = v4u{dppu<ROR8>(p0) & bm[2], p0 & bm[2], dppu<ROR8>(p1) & bm[2], p1 & bm[2]};
    Bf[be][3] = v4u{dppu<ROR12>(p0) & bm[3], dppu<ROR4>(p0) & bm[3], dppu<ROR12>(p1) & bm[3], dppu<ROR4>(p1) & bm[3]};
  }
  SB();
  ST(4);
#pragma unroll
  for (int n = QPOS + 2; n < 32; ++n) { VMWAIT(WIN - 1); V[n - 16] = ld16<AUX_NT>(rv, voff, kvbase + (n - 16) * 8192); SB(); }

  v4u hr[1];
  const int ho[1] = {WS_HP + ((ch & 3) * 28 + 2 * rb + (w >> 2)) * BLK + ((ch >> 2) * 32 + (w & 3) * 8 + r) * 16};
  const int hg = x8 * 4 + rg;
  VMWAIT(WIN - 1);
  const v4u wo0 = ld16<AUX_NT>(ro, r * 2048 + ch * 16, (hg * 128 + w * 16) * 1024 + k8 * 128);
  SB(); VMWAIT(WIN - 1);
  const v4u wo1 = ld16<AUX_NT>(ro, r * 2048 + ch * 16, (hg * 128 + w * 16) * 1024 + k8 * 128 + 1024);
  SB();
  const v4u wb0 = ld16<AUX_NT>(r2, r * 2048 + ch * 16, (rb * 128 + w * 16) * 1024 + c2 * 128);
  const v4u wb1 = ld16<AUX_NT>(r2, r * 2048 + ch * 16, (rb * 128 + w * 16) * 1024 + c2 * 128 + 1024);
  hr[0] = ld16<AUX_SC1>(is2 ? rws : r2, ho[0], 0);
  const unsigned bias1 = ld4(rb1, r * 4, (rb * 64 + w * 8) * 4);
  const int ocol = x8 * 64 + (i32 & 15) * 4;
  const unsigned xo = ld4(rx, (lane & 1) * 4, (bat * D + ocol) * 2), bias2 = ld4(rb2, (lane & 1) * 4, ocol * 2);
  SB();
  ST(5);

  v4f acc[2] = {v4f{0.f, 0.f, 0.f, 0.f}, v4f{0.f, 0.f, 0.f, 0.f}};
#pragma unroll
  for (int be = 0; be < 2; ++be) {
    v4u A[8];
#pragma unroll
    for (int t = 0; t < 4; ++t)
#pragma unroll
      for (int u = 0; u < 4; ++u) {
        A[2 * u][t] = __builtin_amdgcn_perm(V[8 * be + 2 * t + 1][u], V[8 * be + 2 * t][u], 0x05040100u);
        A[2 * u + 1][t] = __builtin_amdgcn_perm(V[8 * be + 2 * t + 1][u], V[8 * be + 2 * t][u], 0x07060302u);
      }
#pragma unroll
    for (int u = 0; u < 4; ++u) { acc[0] = mfma(A[2 * u], Bf[be][u], acc[0]); acc[1] = mfma(A[2 * u + 1], Bf[be][u], acc[1]); }
  }
  *(v4f*)(l_o + w * 512 + lane * 4) = v4f{acc[0][0], acc[1][0], acc[0][1], acc[1][1]};
  *(v4f*)(l_o + w * 512 + 256 + lane * 4) = v4f{acc[0][2], acc[1][2], acc[0][3], acc[1][3]};
  const float lse_m = asf(dppu<0x50>(asu(lse))), lse_n = asf(dppu<0xFA>(asu(lse)));
  LDS_BARRIER();
  ST(6);
  if (w < 4) {
    v2f a[8];
#pragma unroll
    for (int ww = 0; ww < 8; ++ww) a[ww] = *(const v2f*)(l_o + ww * 512 + (lane >> 5) * 256 + w * 64 + (lane & 31) * 2);
    const v2f s = ((a[0] + a[1]) + (a[2] + a[3])) + ((a[4] + a[5]) + (a[6] + a[7]));
    const int mh = (lane >> 1) & 3, mu = (lane >> 3) & 3, mq = (lane >> 5) * 2 + (lane & 1);
    const float ls = (lane & 4) ? lse_n : lse_m;
    st16(rec(pk(s.x, s.y), asu(ls), tag), rws, mh * 32 * BLK + (w * 16 + mq * 4 + mu) * 16, WS_R1 + (x8 * 4 * 32 + i32) * BLK);
  }
  SB();
  ST(7);
  v4u pr[4];
  int po[4];
#pragma unroll
  for (int k = 0; k < 4; ++k) { po[k] = WS_R1 + (hg * 32 + k * 8 + ch) * BLK + (w * 8 + r) * 16; pr[k] = ld16<AUX_SC1>(rws, po[k], 0); }
  SB();
  if (is2) {
    await<1>(hr, ho, rws, tag);
    float h0 = asf(hr[0].x), h1 = asf(hr[0].z);
    h0 += dppf<XOR1>(h0); h1 += dppf<XOR1>(h1); h0 += dppf<XOR2>(h0); h1 += dppf<XOR2>(h1);
    h0 = rbf(rbf(h0) + lo16(bias1)); h1 = rbf(rbf(h1) + hi16(bias1));
    constexpr float G0 = -2.f * 0.7978845608028654f * LOG2E, G1 = G0 * 0.044715f;
    h0 = h0 * rcpf_(1.f + exp2f_(h0 * fmaf(G1, h0 * h0, G0))); h1 = h1 * rcpf_(1.f + exp2f_(h1 * fmaf(G1, h1 * h1, G0)));
    const unsigned hp = pk(h0, h1), hx = dppu<HMIR>(hp);
    l_r1[w * 64 + lane] = block_product(pair_rows(wb0, wb1), (ch & 4) ? hx : hp, (ch & 4) ? hp : hx, b3);
  }
  asm volatile("" :: "v"(wb0), "v"(wb1), "v"(hr[0]), "v"(bias1), "v"(xo), "v"(bias2));
  const WPair wop = pair_rows(wo0, wo1);
  SB();
  ST(8);
  await<4>(pr, po, rws, tag);
  ST(9);
  unsigned on[2];
#pragma unroll
  for (int b = 0; b < 2; ++b) {
    const float l0 = asf(pr[2 * b].z), l1 = asf(pr[2 * b + 1].z);
    float M = fmaxf(l0, l1);
    M = fmaxf(M, dppf<XOR1>(M)); M = fmaxf(M, dppf<XOR2>(M)); M = fmaxf(M, dppf<HMIR>(M));
    const float e0 = exp2f_(l0 - M), e1 = exp2f_(l1 - M);
    float z = e0 + e1, o0 = fmaf(e0, lo16(pr[2 * b].x), e1 * lo16(pr[2 * b + 1].x)), o1 = fmaf(e0, hi16(pr[2 * b].x), e1 * hi16(pr[2 * b + 1].x));
    z += dppf<XOR1>(z); o0 += dppf<XOR1>(o0); o1 += dppf<XOR1>(o1);
    z += dppf<XOR2>(z); o0 += dppf<XOR2>(o0); o1 += dppf<XOR2>(o1);
    z += dppf<HMIR>(z); o0 += dppf<HMIR>(o0); o1 += dppf<HMIR>(o1);
    const float iz = rcpf_(z);
    on[b] = pk(o0 * iz, o1 * iz);
  }
  l_r0[w * 64 + lane] = block_product(wop, on[0], on[1], b3);
  LDS_BARRIER();
  ST(10);
  if (w == 0) { const v2f s = sum8(l_r0 + lane); st16(rec(asu(s.x), asu(s.y), tag), rws, recoff, WS_SP + (hg * 8 + k8) * BLK); }
  if (w == 1 && is2) { const v2f s = sum8(l_r1 + lane); st16(rec(asu(s.x), asu(s.y), tag), rws, recoff, WS_GP + (rb * 8 + c2) * BLK); }
  if (w == 2) {
    const int src = lane >> 1, irec = (bat * 32 + (i32 & 15) * 2 + (lane & 1)) * 16;
    v4u fs[1], fg[1];
    const int so[1] = {WS_SP + (src * 8 + x8) * BLK + irec}, go[1] = {WS_GP + ((src < 14 ? src : 0) * 8 + x8) * BLK + irec};
    fs[0] = ld16<AUX_SC1>(rws, so[0], 0); fg[0] = ld16<AUX_SC1>(rws, go[0], 0);
    await<1>(fg, go, rws, tag);
    await<1>(fs, so, rws, tag);
    ST(11);
    float s0 = asf(fs[0].x), s1 = asf(fs[0].z), g0 = src < 14 ? asf(fg[0].x) : 0.f, g1 = src < 14 ? asf(fg[0].z) : 0.f;
    float sa = sw32_add(s0, g0), sb = sw32_add(s1, g1);
    float sc = sw16_add(sa, sb);
    sc += dppf<ROR8>(sc); sc += dppf<ROR4>(sc); sc += dppf<XOR2>(sc);
    auto t = __builtin_amdgcn_permlane32_swap(asu(sc), asu(sc), false, false);
    const float sv = rbf(asf(t[0]));
    const bool odd = (lane & 16) != 0;
    const float mv = rbf(rbf(asf(t[1])) + (odd ? hi16(bias2) : lo16(bias2)));
    const float gv = rbf(mv * rcpf_(1.f + exp2f_(-LOG2E * mv)));
    const float ov = (odd ? hi16(xo) : lo16(xo)) + rbf(sv * gv);
    auto u = __builtin_amdgcn_permlane16_swap(asu(ov), asu(ov), false, false);
    if (lane < 2) ((unsigned*)out)[(bat * D + ocol) / 2 + lane] = pk(asf(u[0]), asf(u[1]));
    if (blk == 0 && lane == 0) __builtin_amdgcn_raw_buffer_store_b64(v2u{tag.a, tag.b}, rws, 0, WS_E, AUX_SC1);
    ST(12);
  }
}
}

extern "C" void kernel_launch(void* const* d_in, const int* in_sizes, int n_in, void* d_out, int out_size, void* d_ws, size_t ws_size,
                              hipStream_t stream) {
  fused::k_fused<<<256, 512, 0, stream>>>(d_in[0], d_in[1], d_in[2], d_in[3], d_in[4], d_in[6], d_in[7], d_in[8], d_in[9], d_out, d_ws);
}
```

```cpp
#include <hip/hip_runtime.h>
#include <hip/hip_bf16.h>
#include <stdint.h>

#define KFRONT 0
#define WIN 64
#define QWAIT 400
#define QPOS 24
#define GSLEEP 0
#define W2LAST 0
#define MISS_OK 2
#define QSLEEP 16
#define SSLEEP 2
#define CODE_PF 1

namespace fused {
typedef unsigned v4u __attribute__((ext_vector_type(4)));
typedef unsigned v2u __attribute__((ext_vector_type(2)));
typedef float v4f __attribute__((ext_vector_type(4)));
typedef float v2f __attribute__((ext_vector_type(2)));
typedef short s16x4 __attribute__((ext_vector_type(4)));
typedef __bf16 bf16x2 __attribute__((ext_vector_type(2)));
typedef __bf16 bf16x8 __attribute__((ext_vector_type(8)));
typedef __amdgpu_buffer_rsrc_t rsrc_t;
typedef unsigned long long u64;
#define LAS __attribute__((address_space(3)))

constexpr int D = 512, HQ = 32, HKV = 8, SK = 8192, DH = 128, F = 1792;
constexpr int AUX_NT = 2, AUX_SC1 = 16;
constexpr int KV_BYTES = 2 * HKV * SK * DH * 2;
constexpr int BLK = 1024;
constexpr int WS_E = 0, WS_QP = 4096, WS_HP = WS_QP + 4 * 64 * BLK, WS_LS = WS_HP + 4 * 28 * BLK, WS_R1 = WS_LS + 32 * 32 * 16, WS_SP = WS_R1 + 32 * 32 * 512,
              WS_GP = WS_SP + 32 * 8 * BLK, WS_END = WS_GP + 14 * 8 * BLK;
constexpr unsigned SPIN_CAP = 1u << 18;
constexpr float LOG2E = 1.4426950408889634f;
constexpr float SC2 = 0.08838834764831845f * LOG2E;
constexpr int PW = 192;
constexpr int L_O = 0, L_R0 = 4096, L_R1 = L_R0 + 8 * PW, L_R2 = L_R1 + 4 * PW, L_Q = L_R2 + 4 * PW, L_ML = L_Q + 256, L_CNT = L_ML + 64, L_END = L_CNT + 8;
constexpr int CODE_BYTES = 11008;

__device__ __forceinline__ rsrc_t mk(const void* p, int bytes) { return __builtin_amdgcn_make_buffer_rsrc((void*)p, 0, bytes, 0x00020000); }
template <int AUX> __device__ __forceinline__ v4u ld16(rsrc_t r, int voff, int soff) { return __builtin_amdgcn_raw_buffer_load_b128(r, voff, soff, AUX); }
template <int AUX> __device__ __forceinline__ v2u ld8(rsrc_t r, int voff, int soff) { return __builtin_amdgcn_raw_buffer_load_b64(r, voff, soff, AUX); }
__device__ __forceinline__ unsigned ld4(rsrc_t r, int voff, int soff) { return __builtin_amdgcn_raw_buffer_load_b32(r, voff, soff, 0); }
__device__ __forceinline__ void st16(v4u v, rsrc_t r, int voff, int soff) { __builtin_amdgcn_raw_buffer_store_b128(v, r, voff, soff, AUX_SC1); }
__device__ __forceinline__ float asf(unsigned u) { return __builtin_bit_cast(float, u); }
__device__ __forceinline__ unsigned asu(float f) { return __builtin_bit_cast(unsigned, f); }
__device__ __forceinline__ float lo16(unsigned u) { return asf(u << 16); }
__device__ __forceinline__ float hi16(unsigned u) { return asf(u & 0xffff0000u); }
__device__ __forceinline__ unsigned pk(float a, float b) { return __builtin_bit_cast(unsigned, bf16x2{(__bf16)a, (__bf16)b}); }
__device__ __forceinline__ float rbf(float a) { return (float)(__bf16)a; }
template <int C> __device__ __forceinline__ unsigned dppu(unsigned x) { return (unsigned)__builtin_amdgcn_mov_dpp((int)x, C, 0xf, 0xf, true); }
template <int C> __device__ __forceinline__ float dppf(float x) { return asf(dppu<C>(asu(x))); }
template <int I> __device__ __forceinline__ float to_bank(float old, float src) {
  return asf((unsigned)__builtin_amdgcn_update_dpp((int)asu(old), (int)asu(src), 0x110 + 4 * I, 0xf, 1 << I, false)); }
constexpr int XOR1 = 0xB1, XOR2 = 0x4E, HMIR = 0x141, SHL4 = 0x104, SHL8 = 0x108, ROR4 = 0x124, ROR8 = 0x128, ROR12 = 0x12C;
__device__ __forceinline__ float sw32_add(float a, float b) {
  auto t = __builtin_amdgcn_permlane32_swap(asu(a), asu(b), false, false); return asf(t[0]) + asf(t[1]); }
__device__ __forceinline__ float sw16_add(float a, float b) {
  auto t = __builtin_amdgcn_permlane16_swap(asu(a), asu(b), false, false); return asf(t[0]) + asf(t[1]); }
__device__ __forceinline__ float sw32_max(float a) {
  auto t = __builtin_amdgcn_permlane32_swap(asu(a), asu(a), false, false); return fmaxf(asf(t[0]), asf(t[1])); }
__device__ __forceinline__ float sw16_max(float a) {
  auto t = __builtin_amdgcn_permlane16_swap(asu(a), asu(a), false, false); return fmaxf(asf(t[0]), asf(t[1])); }
__device__ __forceinline__ v4f mfma32(v4u a, v4u b, v4f c) {
  return __builtin_amdgcn_mfma_f32_16x16x32_bf16(__builtin_bit_cast(bf16x8, a), __builtin_bit_cast(bf16x8, b), c, 0, 0, 0); }
__device__ __forceinline__ v4f mfma16(v2u a, v2u b, v4f c) {
  return __builtin_amdgcn_mfma_f32_16x16x16bf16_1k(__builtin_bit_cast(s16x4, a), __builtin_bit_cast(s16x4, b), c, 0, 0, 0); }
__device__ __forceinline__ unsigned perm_lo(unsigned even, unsigned odd) { return __builtin_amdgcn_perm(odd, even, 0x05040100u); }
__device__ __forceinline__ unsigned perm_hi(unsigned even, unsigned odd) { return __builtin_amdgcn_perm(odd, even, 0x07060302u); }
__device__ __forceinline__ float exp2f_(float x) { return __builtin_amdgcn_exp2f(x); }
__device__ __forceinline__ float rcpf_(float x) { return __builtin_amdgcn_rcpf(x); }
#define SB() __builtin_amdgcn_sched_barrier(0)
#define VMWAIT(n) __builtin_amdgcn_s_waitcnt(0x0F70 | ((n) & 15) | (((n) >> 4) << 14))
#define LDS_BARRIER() do { SB(); asm volatile("s_waitcnt lgkmcnt(0)\n\ts_barrier" ::: "memory"); SB(); } while (0)

struct Tag { unsigned a, b; };
__device__ __forceinline__ bool fresh(v4u r, Tag t) { return (r.y == t.a) & (r.w == t.b); }
__device__ __forceinline__ v4u rec(unsigned p0, unsigned p1, Tag t) { return v4u{p0, t.a, p1, t.b}; }

template <int N> __device__ __forceinline__ bool all_fresh(const v4u (&r)[N], Tag t) {
  bool ok = true;
#pragma unroll
  for (int k = 0; k < N; ++k) ok = ok & fresh(r[k], t);
  return ok;
}
template <int N, int SLEEP = 0> __device__ __forceinline__ unsigned gather(v4u (&r)[N], const int (&off)[N], rsrc_t ws, Tag t) {
  bool ok = false;
  unsigned spin = 0;
#pragma nounroll
  do {
    asm volatile("" ::: "memory");
    if (!ok) {
#pragma unroll
      for (int k = 0; k < N; ++k) r[k] = ld16<AUX_SC1>(ws, off[k], 0);
    }
    ok = all_fresh<N>(r, t);
    const u64 miss = __ballot(!ok);
    if (miss == 0 || ++spin >= SPIN_CAP) break;
    if (SLEEP && __builtin_popcountll(miss) > 16) __builtin_amdgcn_s_sleep(SLEEP);
  } while (true);
  asm volatile("" ::: "memory");
  return spin;
}

template <int MISS, int SLEEP> __device__ __forceinline__ unsigned sentinel(int off, bool on, rsrc_t ws, Tag t) {
  bool ok = !on;
  unsigned spin = 0;
#pragma nounroll
  while (true) {
    asm volatile("" ::: "memory");
    if (!ok) ok = fresh(ld16<AUX_SC1>(ws, off, 0), t);
    if (__builtin_popcountll(__ballot(!ok)) <= MISS || ++spin >= SPIN_CAP) break;
    __builtin_amdgcn_s_sleep(SLEEP);
  }
  return spin;
}
__device__ __forceinline__ void lds_arrive(LAS unsigned* c, int lane) {
  asm volatile("s_waitcnt lgkmcnt(0)" ::: "memory");
  if (lane == 0) __hip_atomic_fetch_add(c, 1u, __ATOMIC_RELAXED, __HIP_MEMORY_SCOPE_WORKGROUP);
}
__device__ __forceinline__ void lds_wait(LAS unsigned* c, unsigned n) {
#pragma nounroll
  for (unsigned spin = 0; spin < (SPIN_CAP << 4); ++spin) {
    if ((unsigned)__builtin_amdgcn_readfirstlane(__hip_atomic_load(c, __ATOMIC_RELAXED, __HIP_MEMORY_SCOPE_WORKGROUP)) >= n) break;
    __builtin_amdgcn_s_sleep(1);
  }
  asm volatile("" ::: "memory");
}

__device__ __forceinline__ float half_sum(float a, float b) {
  float x = sw32_add(a, b);
  x += dppf<XOR1>(x); x += dppf<XOR2>(x); x += dppf<HMIR>(x); x += dppf<ROR8>(x); return sw16_add(x, x); }
struct Stat { float m0, r0, m1, r1; };
__device__ __forceinline__ Stat ln_stats(const float (&u)[8], const float (&v)[8]) {
  const float su = ((u[0] + u[1]) + (u[2] + u[3])) + ((u[4] + u[5]) + (u[6] + u[7]));
  const float sv = ((v[0] + v[1]) + (v[2] + v[3])) + ((v[4] + v[5]) + (v[6] + v[7]));
  const float mf = half_sum(su, sv) * (1.f / D);
  const float mu = asf(__builtin_amdgcn_readlane(asu(mf), 0)), mv = asf(__builtin_amdgcn_readlane(asu(mf), 32));
  float qu = 0.f, qv = 0.f;
#pragma unroll
  for (int j = 0; j < 8; ++j) { const float a = u[j] - mu, b = v[j] - mv; qu = fmaf(a, a, qu); qv = fmaf(b, b, qv); }
  const float var = rbf(half_sum(qu, qv) * (1.f / D));
  const float rs = rcpf_(rbf(__builtin_amdgcn_sqrtf(rbf(var + 1e-5f))));
  return Stat{rbf(mu), asf(__builtin_amdgcn_readlane(asu(rs), 0)), rbf(mv), asf(__builtin_amdgcn_readlane(asu(rs), 32))};
}
__device__ __forceinline__ float ln_apply(float x, float m, float rs) { return rbf(rbf(x - m) * rs); }

__device__ __forceinline__ v4f product32(const v2u (&w)[8], v4u in, const unsigned (&mj)[4]) {
  v4f acc = {0.f, 0.f, 0.f, 0.f};
#pragma unroll
  for (int j = 0; j < 4; ++j) {
    v4u a;
#pragma unroll
    for (int t = 0; t < 4; ++t) a[t] = (j & 1) ? perm_hi(w[2 * t][j >> 1], w[2 * t + 1][j >> 1]) : perm_lo(w[2 * t][j >> 1], w[2 * t + 1][j >> 1]);
    acc = mfma32(a, v4u{in[0] & mj[j], in[1] & mj[j], in[2] & mj[j], in[3] & mj[j]}, acc);
  }
  return acc;
}
template <int N> __device__ __forceinline__ v2f sum_waves(const float* p, int lane) {
  const float* q = p + (lane >> 5) * 96 + (lane & 1) * 48 + ((lane & 31) >> 1);
  v2f a[N];
#pragma unroll
  for (int w = 0; w < N; ++w) a[w] = v2f{q[w * PW], q[w * PW + 24]};
  v2f s = (a[0] + a[1]) + (a[2] + a[3]);
  if (N == 8) s += (a[4 % N] + a[5 % N]) + (a[6 % N] + a[7 % N]);
  return s;
}

#define ST(i)
#define ST_FLUSH()
#define CNT(i, v) (void)(v)

__global__ void __launch_bounds__(768, 3)
k_fused(const void* __restrict__ x, const void* __restrict__ kc, const void* __restrict__ vc, const void* __restrict__ wq, const void* __restrict__ wo,
        const void* __restrict__ w1, const void* __restrict__ b1, const void* __restrict__ w2, const void* __restrict__ b2, void* out, void* wsp) {
  __shared__ __attribute__((aligned(16))) float lds[L_END];
  const int tid = threadIdx.x, lane = tid & 63, wv = __builtin_amdgcn_readfirstlane(tid >> 6);
  const int blk = blockIdx.x, x8 = blk & 7, i32 = blk >> 3, rg = i32 >> 3, k8 = i32 & 7, bat = i32 >> 4;
  const int g = lane >> 4, c16 = lane & 15, hg = x8 * 4 + rg;
  u64 pc;
  asm volatile("s_getpc_b64 %0" : "=s"(pc));
  ST(0);

  if (__builtin_expect(wv >= 4, 1)) {
    const int w = wv - 4;
    const rsrc_t rk = mk(kc, KV_BYTES), rv = mk(vc, KV_BYTES);
    const int kvbase = ((bat * HKV + x8) * SK + (i32 & 15) * 512) * 256 + w * 1024;
    const int koff = ((lane >> 2) & 3) * 256 + g * 64 + (lane & 3) * 16, voff = lane * 16;
    v4u K[16], V[16];
    if (KFRONT == 0) LDS_BARRIER();
#pragma unroll
    for (int n = 0; n < QPOS; ++n) {
      if (n >= WIN) VMWAIT(WIN - 1);
      if (n < 16) K[n] = ld16<AUX_NT>(rk, koff, kvbase + n * 8192);
      else V[n - 16] = ld16<AUX_NT>(rv, voff, kvbase + (n - 16) * 8192);
      SB();
      if (n == KFRONT - 1) LDS_BARRIER();
    }
    ST(2);
    LAS unsigned* cnt = (LAS unsigned*)(lds + L_CNT);
    lds_wait(cnt + 2, 4u);
    ST(3);
    const int bank = (lane >> 2) & 3, hh = lane & 3;
    const v4u qf = *(const v4u*)(lds + L_Q + hh * 64 + g * 16 + bank * 4);
    v4f d[16];
#pragma unroll
    for (int n = 0; n < 16; ++n) d[n] = mfma32(K[n], qf, v4f{0.f, 0.f, 0.f, 0.f});
    SB();
    float S[4], sn[16];
#pragma unroll
    for (int n = 0; n < 16; ++n) { d[n][0] += dppf<SHL4>(d[n][1]); d[n][2] += dppf<SHL4>(d[n][3]); }
    SB();
#pragma unroll
    for (int n = 0; n < 16; ++n) sn[n] = d[n][0] + dppf<SHL8>(d[n][2]);
    SB();
#pragma unroll
    for (int z = 0; z < 4; ++z) S[z] = to_bank<1>(sn[4 * z], sn[4 * z + 1]);
#pragma unroll
    for (int z = 0; z < 4; ++z) S[z] = to_bank<2>(S[z], sn[4 * z + 2]);
#pragma unroll
    for (int z = 0; z < 4; ++z) S[z] = to_bank<3>(S[z], sn[4 * z + 3]);
    SB();
    float mw = fmaxf(fmaxf(S[0], S[1]), fmaxf(S[2], S[3]));
    mw = fmaxf(mw, dppf<ROR4>(mw)); mw = fmaxf(mw, dppf<ROR8>(mw)); mw = sw32_max(sw16_max(mw));
    mw *= SC2;
    float lw = 0.f;
#pragma unroll
    for (int z = 0; z < 4; ++z) { S[z] = exp2f_(fmaf(S[z], SC2, -mw)); lw += S[z]; }
    lw += dppf<ROR4>(lw); lw += dppf<ROR8>(lw); lw = sw16_add(lw, lw); lw = sw32_add(lw, lw);
    if (lane < 4) *(v2f*)(lds + L_ML + lane * 16 + w * 2) = v2f{mw, lw};
    lds_arrive(cnt + 3, lane);
    ST(4);
    const v2u lt = *(const v2u*)(lds + L_CNT + 6);
    const Tag tag = {(unsigned)__builtin_amdgcn_readfirstlane(lt.x), (unsigned)__builtin_amdgcn_readfirstlane(lt.y)};
    unsigned P[4];
#pragma unroll
    for (int z = 0; z < 4; ++z) P[z] = pk(S[z], dppf<SHL4>(S[z]));
    unsigned bm[4];
#pragma unroll
    for (int u = 0; u < 4; ++u) bm[u] = bank == u ? 0xffffffffu : 0u;
    v4u Bf[2][4];
#pragma unroll
    for (int be = 0; be < 2; ++be) {
      const unsigned p0 = P[2 * be], p1 = P[2 * be + 1];
      Bf[be][0] = v4u{p0 & bm[0], dppu<ROR8>(p0) & bm[0], p1 & bm[0], dppu<ROR8>(p1) & bm[0]};
      Bf[be][1] = v4u{dppu<ROR4>(p0) & bm[1], dppu<ROR12>(p0) & bm[1], dppu<ROR4>(p1) & bm[1], dppu<ROR12>(p1) & bm[1]};
      Bf[be][2] = v4u{dppu<ROR8>(p0) & bm[2], p0 & bm[2], dppu<ROR8>(p1) & bm[2], p1 & bm[2]};
      Bf[be][3] = v4u{dppu<ROR12>(p0) & bm[3], dppu<ROR4>(p0) & bm[3], dppu<ROR12>(p1) & bm[3], dppu<ROR4>(p1) & bm[3]};
    }
    SB();
#pragma unroll
    for (int n = QPOS; n < 32; ++n) { V[n - 16] = ld16<AUX_NT>(rv, voff, kvbase + (n - 16) * 8192); SB(); }
    const rsrc_t ro = mk(wo, D * HQ * DH * 2);
    v2u wr[4];
#pragma unroll
    for (int i = 0; i < 4; ++i) wr[i] = ld8<AUX_NT>(ro, g * 4096 + c16 * 8 + i * 1024, (hg * 128 + w * 16) * 1024 + k8 * 128);
    SB();
    ST(5);
    v4f acc[2] = {v4f{0.f, 0.f, 0.f, 0.f}, v4f{0.f, 0.f, 0.f, 0.f}};
#pragma unroll
    for (int be = 0; be < 2; ++be) {
      v4u A[8];
#pragma unroll
      for (int t = 0; t < 4; ++t) {
#pragma unroll
        for (int u = 0; u < 4; ++u) {
          A[2 * u][t] = perm_lo(V[8 * be + 2 * t][u], V[8 * be + 2 * t + 1][u]);
          A[2 * u + 1][t] = perm_hi(V[8 * be + 2 * t][u], V[8 * be + 2 * t + 1][u]);
        }
        SB();
      }
#pragma unroll
      for (int u = 0; u < 4; ++u) { acc[0] = mfma32(A[2 * u], Bf[be][u], acc[0]); acc[1] = mfma32(A[2 * u + 1], Bf[be][u], acc[1]); }
      SB();
    }
    *(v4f*)(lds + L_O + w * 512 + lane * 4) = acc[0];
    *(v4f*)(lds + L_O + w * 512 + 256 + lane * 4) = acc[1];
    LDS_BARRIER();
    ST(6);
    const rsrc_t rws = mk(wsp, WS_END);
    unsigned mj[4];
#pragma unroll
    for (int j = 0; j < 4; ++j) mj[j] = (lane & 11) == j ? 0xffffffffu : 0u;
    const int pslot = ((lane >> 2) & 1) * 96 + (lane & 3) * 24 + g * 4;
    const int slot0 = hg * 32 + ((lane >> 2) & 1) * 16 + (lane & 3) + ((lane >> 3) & 1) * 4;
    v4u lr[2], pr[2];
    const int lo[2] = {WS_LS + slot0 * 16, WS_LS + slot0 * 16 + 128};
    const int po[2] = {WS_R1 + slot0 * 512 + (w * 4 + g) * 16, WS_R1 + slot0 * 512 + (w * 4 + g) * 16 + 4096};
    gather<2, GSLEEP>(lr, lo, rws, tag);
    float e0, e1;
    {
      float M = fmaxf(asf(lr[0].x), asf(lr[1].x));
      M = fmaxf(M, dppf<XOR1>(M)); M = fmaxf(M, dppf<XOR2>(M)); M = fmaxf(M, dppf<ROR8>(M));
      e0 = exp2f_(asf(lr[0].x) - M); e1 = exp2f_(asf(lr[1].x) - M);
      float z = e0 + e1;
      z += dppf<XOR1>(z); z += dppf<XOR2>(z); z += dppf<ROR8>(z);
      const float iz = rcpf_(z);
      e0 *= iz; e1 *= iz;
    }
    v2u wa[4];
#pragma unroll
    for (int j = 0; j < 4; ++j)
      wa[j] = (j & 1) ? v2u{perm_hi(wr[0][j >> 1], wr[1][j >> 1]), perm_hi(wr[2][j >> 1], wr[3][j >> 1])}
                      : v2u{perm_lo(wr[0][j >> 1], wr[1][j >> 1]), perm_lo(wr[2][j >> 1], wr[3][j >> 1])};
    asm volatile("" : "+v"(wa[0]), "+v"(wa[1]), "+v"(wa[2]), "+v"(wa[3]), "+v"(mj[0]), "+v"(mj[1]), "+v"(mj[2]), "+v"(mj[3]), "+v"(e0), "+v"(e1));
    SB();
    ST(8);
    CNT(14, (gather<2, GSLEEP>(pr, po, rws, tag)));
    ST(9);
    float o[4] = {fmaf(e0, lo16(pr[0].x), e1 * lo16(pr[1].x)), fmaf(e0, hi16(pr[0].x), e1 * hi16(pr[1].x)),
                  fmaf(e0, lo16(pr[0].z), e1 * lo16(pr[1].z)), fmaf(e0, hi16(pr[0].z), e1 * hi16(pr[1].z))};
#pragma unroll
    for (int t = 0; t < 4; ++t) o[t] += dppf<XOR1>(o[t]);
#pragma unroll
    for (int t = 0; t < 4; ++t) o[t] += dppf<XOR2>(o[t]);
#pragma unroll
    for (int t = 0; t < 4; ++t) o[t] += dppf<ROR8>(o[t]);
    const unsigned on0 = pk(o[0], o[1]), on1 = pk(o[2], o[3]);
    v4f sp = {0.f, 0.f, 0.f, 0.f};
#pragma unroll
    for (int j = 0; j < 4; ++j) sp = mfma16(wa[j], v2u{on0 & mj[j], on1 & mj[j]}, sp);
    if (c16 < 8) *(v4f*)(lds + L_R0 + w * PW + pslot) = sp;
    LDS_BARRIER();
    ST(10);
    ST_FLUSH();
    return;
  }

  const int a = wv;
  const unsigned t_in = (unsigned)__builtin_amdgcn_s_memrealtime();
  const rsrc_t rws = mk(wsp, WS_END);
  unsigned mj[4];
#pragma unroll
  for (int j = 0; j < 4; ++j) mj[j] = (lane & 11) == j ? 0xffffffffu : 0u;
  const int pslot = ((lane >> 2) & 1) * 96 + (lane & 3) * 24 + g * 4;
  const int e = x8 + 8 * k8;
  const bool is1 = e < 28, is2 = e >= 28 && e < 56;
  const int id2 = rg * 28 + e - 28, rb = id2 >> 3, c2 = id2 & 7;
  const int lb = (lane >> 2) & 1;
  const rsrc_t rx = mk(x, 2 * D * 2), rq = mk(wq, D * HQ * DH * 2);
  const rsrc_t r1 = mk(w1, is1 ? D * F * 2 : 0), r2 = mk(w2, is2 ? D * F * 2 : 0), rb1 = mk(b1, is2 ? F * 2 : 0), rb2 = mk(b2, D * 2);
  const v2u ep = ld8<AUX_SC1>(rws, 0, WS_E);
  const v4u xa0 = ld16<0>(rx, lane * 16, 0), xa1 = ld16<0>(rx, lane * 16, D * 2);
  const v4u xb = ld16<0>(rx, lb * D * 2 + g * 16, (rg * 128 + a * 32) * 2);
  v2u wA[8], wB[8];
#pragma unroll
  for (int i = 0; i < 8; ++i) wA[i] = ld8<AUX_NT>(rq, g * 8 * 8192 + c16 * 8, (rg * 128 + a * 32 + i) * 8192 + (x8 * 8 + k8) * 128);
#pragma unroll
  for (int i = 0; i < 8; ++i) wB[i] = ld8<AUX_NT>(r1, g * 8 * 3584 + c16 * 8, (rg * 128 + a * 32 + i) * 3584 + e * 128);
  const unsigned code_pf = ld4(mk((const void*)(pc & ~(u64)255), CODE_BYTES), lane * 128, (a & 1) * 8192);
  const int ocol = x8 * 64 + (i32 & 15) * 4;
  const unsigned xo = ld4(rx, (lane & 1) * 4, (bat * D + ocol) * 2), bias2 = ld4(rb2, (lane & 1) * 4, ocol * 2);
  const v2u bias1 = ld8<0>(rb1, g * 16 + ((lane >> 3) & 1) * 8, (rb * 128 + a * 32) * 2);
  const unsigned out_pf = ld4(mk(out, 2 * D * 2), 0, 0);
  LAS unsigned* cnt = (LAS unsigned*)(lds + L_CNT);
  if (tid < 6) cnt[tid] = 0u;
  LDS_BARRIER();

  Tag tag;
  tag.a = __builtin_amdgcn_readfirstlane(ep.x) + 1u; tag.b = __builtin_amdgcn_readfirstlane(ep.y) + 0x9E3779B9u;
  {
    float u[8], v[8], y[8];
#pragma unroll
    for (int t = 0; t < 4; ++t) { u[2 * t] = lo16(xa0[t]); u[2 * t + 1] = hi16(xa0[t]); v[2 * t] = lo16(xa1[t]); v[2 * t + 1] = hi16(xa1[t]);
                                  y[2 * t] = lo16(xb[t]); y[2 * t + 1] = hi16(xb[t]); }
    Stat s = ln_stats(u, v);
#pragma unroll
    for (int j = 0; j < 8; ++j) y[j] = ln_apply(y[j], lb ? s.m1 : s.m0, lb ? s.r1 : s.r0);
    const v4f p = product32(wA, v4u{pk(y[0], y[1]), pk(y[2], y[3]), pk(y[4], y[5]), pk(y[6], y[7])}, mj);
    if (c16 < 8) *(v4f*)(lds + L_R1 + a * PW + pslot) = p;
    asm volatile("s_waitcnt lgkmcnt(0)" ::: "memory");
    unsigned old = 0;
    if (lane == 0) old = __hip_atomic_fetch_add(cnt, 1u, __ATOMIC_RELAXED, __HIP_MEMORY_SCOPE_WORKGROUP);
    if (__builtin_amdgcn_readfirstlane(old) == 3u) {
      asm volatile("" ::: "memory");
      const v2f q = sum_waves<4>(lds + L_R1, lane);
      st16(rec(asu(q.x), asu(q.y), tag), rws, lane * 16, WS_QP + (rg * 64 + x8 * 8 + k8) * BLK);
    }
    ST(1);
    if (is1) {
#pragma unroll
      for (int j = 0; j < 8; ++j) { u[j] = ln_apply(u[j], s.m0, s.r0); v[j] = ln_apply(v[j], s.m1, s.r1); }
      s = ln_stats(u, v);
#pragma unroll
      for (int j = 0; j < 8; ++j) y[j] = ln_apply(y[j], lb ? s.m1 : s.m0, lb ? s.r1 : s.r0);
      const v4f ph = product32(wB, v4u{pk(y[0], y[1]), pk(y[2], y[3]), pk(y[4], y[5]), pk(y[6], y[7])}, mj);
      if (c16 < 8) *(v4f*)(lds + L_R2 + a * PW + pslot) = ph;
      asm volatile("s_waitcnt lgkmcnt(0)" ::: "memory");
      unsigned old1 = 0;
      if (lane == 0) old1 = __hip_atomic_fetch_add(cnt + 1, 1u, __ATOMIC_RELAXED, __HIP_MEMORY_SCOPE_WORKGROUP);
      if (__builtin_amdgcn_readfirstlane(old1) == 3u) {
        asm volatile("" ::: "memory");
        const v2f q = sum_waves<4>(lds + L_R2, lane);
        st16(rec(asu(q.x), asu(q.y), tag), rws, lane * 16, WS_HP + (rg * 28 + e) * BLK);
      }
    }
    asm volatile("" :: "v"(wB[0]), "v"(wB[1]), "v"(wB[2]), "v"(wB[3]), "v"(wB[4]), "v"(wB[5]), "v"(wB[6]), "v"(wB[7]));
    asm volatile("" :: "v"(code_pf));
    asm volatile("" :: "v"(xo), "v"(bias2), "v"(bias1), "v"(out_pf));
  }
  {
    v4u qr[4];
    int qo[4];
#pragma unroll
    for (int k = 0; k < 4; ++k) qo[k] = WS_QP + (k * 64 + x8 * 8 + a * 2 + (lane >> 5)) * BLK + (bat * 32 + (lane & 31)) * 16;
    while ((unsigned)__builtin_amdgcn_s_memrealtime() - t_in < QWAIT) __builtin_amdgcn_s_sleep(4);
    CNT(13, gather<4>(qr, qo, rws, tag));
    ((unsigned*)lds)[L_Q + a * 64 + lane] = pk((asf(qr[0].x) + asf(qr[1].x)) + (asf(qr[2].x) + asf(qr[3].x)), (asf(qr[0].z) + asf(qr[1].z)) + (asf(qr[2].z) + asf(qr[3].z)));
  }
  if (tid == 0) *(v2u*)(lds + L_CNT + 6) = v2u{tag.a, tag.b};
  lds_arrive(cnt + 2, lane);
  ST(2);
  v4u hin = {0u, 0u, 0u, 0u};
  if (is2) {
    v4u hr[2];
    int ho[2];
#pragma unroll
    for (int k = 0; k < 2; ++k) ho[k] = WS_HP + ((lane & 3) * 28 + 2 * rb + (a >> 1)) * BLK + (lb * 32 + (a & 1) * 16 + g * 4 + ((lane >> 3) & 1) * 2 + k) * 16;
    gather<2>(hr, ho, rws, tag);
    float h[4] = {asf(hr[0].x), asf(hr[0].z), asf(hr[1].x), asf(hr[1].z)};
    const float bb[4] = {lo16(bias1.x), hi16(bias1.x), lo16(bias1.y), hi16(bias1.y)};
    constexpr float G0 = -2.f * 0.7978845608028654f * LOG2E, G1 = G0 * 0.044715f;
#pragma unroll
    for (int j = 0; j < 4; ++j) {
      h[j] += dppf<XOR1>(h[j]); h[j] += dppf<XOR2>(h[j]);
      const float t = rbf(rbf(h[j]) + bb[j]);
      h[j] = t * rcpf_(1.f + exp2f_(t * fmaf(G1, t * t, G0)));
    }
    const unsigned h0 = pk(h[0], h[1]), h1 = pk(h[2], h[3]);
    hin = v4u{h0, h1, dppu<ROR8>(h0), dppu<ROR8>(h1)};
  }
  ST(3);
  lds_wait(cnt + 3, 8u);
  ST(4);
  const int mh = (lane >> 1) & 3;
  float lse, fw[8];
  {
    v4f ml[4];
#pragma unroll
    for (int t = 0; t < 4; ++t) ml[t] = *(const v4f*)(lds + L_ML + mh * 16 + 4 * t);
    const float M = fmaxf(fmaxf(fmaxf(ml[0][0], ml[0][2]), fmaxf(ml[1][0], ml[1][2])), fmaxf(fmaxf(ml[2][0], ml[2][2]), fmaxf(ml[3][0], ml[3][2])));
    float L = 0.f;
#pragma unroll
    for (int t = 0; t < 4; ++t) { fw[2 * t] = exp2f_(ml[t][0] - M); fw[2 * t + 1] = exp2f_(ml[t][2] - M); L = fmaf(fw[2 * t], ml[t][1], fmaf(fw[2 * t + 1], ml[t][3], L)); }
    const float il = rcpf_(L);
#pragma unroll
    for (int t = 0; t < 8; ++t) fw[t] *= il;
    lse = M + __builtin_amdgcn_logf(L);
    asm volatile("" : "+v"(fw[0]), "+v"(fw[1]), "+v"(fw[2]), "+v"(fw[3]), "+v"(fw[4]), "+v"(fw[5]), "+v"(fw[6]), "+v"(fw[7]));
    if (a == 0 && lane < 8 && !(lane & 1)) st16(rec(asu(lse), asu(lse), tag), rws, mh * 512, WS_LS + (x8 * 4 * 32 + i32) * 16);
  }
#pragma unroll
  for (int i = 0; i < 8; ++i) wB[i] = ld8<AUX_NT>(r2, g * 8 * 1024 + c16 * 8 + (i & 3) * 1024, (rb * 128 + a * 32 + (i & 4)) * 1024 + c2 * 128);
  SB();
  const float* mp = lds + L_O + (a * 16 + ((lane >> 3) & 3) * 4 + mh) * 4 + (lane >> 5) * 2 + (lane & 1);
  const int moff = (lane & 8) ? WS_END : mh * 32 * 512 + (a * 8 + ((lane >> 5) * 2 + (lane & 1)) * 2 + ((lane >> 4) & 1)) * 16;
  LDS_BARRIER();
  ST(6);
  {
    v2f t[8];
#pragma unroll
    for (int ww = 0; ww < 8; ++ww) t[ww] = v2f{mp[ww * 512], mp[ww * 512 + 256]};
    const v2f s = (fw[0] * t[0] + fw[1] * t[1] + fw[2] * t[2] + fw[3] * t[3]) + (fw[4] * t[4] + fw[5] * t[5] + fw[6] * t[6] + fw[7] * t[7]);
    const unsigned p = pk(s.x, s.y);
    st16(rec(p, dppu<ROR8>(p), tag), rws, moff, WS_R1 + (x8 * 4 * 32 + i32) * 512);
  }
  SB();
  ST(7);
  if (is2) {
    const v4f pg = product32(wB, hin, mj);
    if (c16 < 8) *(v4f*)(lds + L_R2 + a * PW + pslot) = pg;
    asm volatile("s_waitcnt lgkmcnt(0)" ::: "memory");
    unsigned old2 = 0;
    if (lane == 0) old2 = __hip_atomic_fetch_add(cnt + 1, 1u, __ATOMIC_RELAXED, __HIP_MEMORY_SCOPE_WORKGROUP);
    if (__builtin_amdgcn_readfirstlane(old2) == 3u) {
      asm volatile("" ::: "memory");
      const v2f q = sum_waves<4>(lds + L_R2, lane);
      st16(rec(asu(q.x), asu(q.y), tag), rws, lane * 16, WS_GP + (rb * 8 + c2) * BLK);
    }
  }
  asm volatile("" :: "v"(wB[0]), "v"(wB[1]), "v"(wB[2]), "v"(wB[3]), "v"(wB[4]), "v"(wB[5]), "v"(wB[6]), "v"(wB[7]));
  ST(8);
  LDS_BARRIER();
  ST(10);
  if (a == 0) { const v2f s = sum_waves<8>(lds + L_R0, lane); st16(rec(asu(s.x), asu(s.y), tag), rws, lane * 16, WS_SP + (hg * 8 + k8) * BLK);
  }
  if (a == 2) {
    const int src = lane >> 1, irec = (bat * 32 + (i32 & 15) * 2 + (lane & 1)) * 16;
    v4u fs[1], fg[1];
    const int so[1] = {WS_SP + (src * 8 + x8) * BLK + irec}, go[1] = {WS_GP + ((src < 14 ? src : 0) * 8 + x8) * BLK + irec};
    gather<1>(fg, go, rws, tag);
    CNT(15, gather<1>(fs, so, rws, tag));
    ST(11);
    const float g0 = src < 14 ? asf(fg[0].x) : 0.f, g1 = src < 14 ? asf(fg[0].z) : 0.f;
    float sc = sw16_add(sw32_add(asf(fs[0].x), g0), sw32_add(asf(fs[0].z), g1));
    sc += dppf<ROR8>(sc); sc += dppf<ROR4>(sc); sc += dppf<XOR2>(sc);
    auto t = __builtin_amdgcn_permlane32_swap(asu(sc), asu(sc), false, false);
    const float sv = rbf(asf(t[0]));
    const bool odd = (lane & 16) != 0;
    const float mv = rbf(rbf(asf(t[1])) + (odd ? hi16(bias2) : lo16(bias2)));
    const float gv = rbf(mv * rcpf_(1.f + exp2f_(-LOG2E * mv)));
    const float ov = (odd ? hi16(xo) : lo16(xo)) + rbf(sv * gv);
    auto u = __builtin_amdgcn_permlane16_swap(asu(ov), asu(ov), false, false);
    if (lane < 2) ((unsigned*)out)[(bat * D + ocol) / 2 + lane] = pk(asf(u[0]), asf(u[1]));
    if (blk == 0 && lane == 0) __builtin_amdgcn_raw_buffer_store_b64(v2u{tag.a, tag.b}, rws, 0, WS_E, AUX_SC1);
    ST(12);
  }
  ST_FLUSH();
}
}

extern "C" void kernel_launch(void* const* d_in, const int* in_sizes, int n_in, void* d_out, int out_size, void* d_ws, size_t ws_size,
                              hipStream_t stream) {
  fused::k_fused<<<256, 768, 0, stream>>>(d_in[0], d_in[1], d_in[2], d_in[3], d_in[4], d_in[6], d_in[7], d_in[8], d_in[9], d_out, d_ws);
}
```

```cpp
#define HOLDX 1
#include <hip/hip_runtime.h>
#include <hip/hip_bf16.h>
#include <stdint.h>

#define KFRONT 2
#define WIN 4
#define BURST 3
#define WIN2 64
#define QPOS 24
#define QWAIT 500
#define GSLEEP 0
#define DELTA 40
#define WOPOS 0
#define FINAL_AT 2u
#define FSLEEP 0
#define ONSLEEP 0
#define HWAIT 600
#define ROT 0
#define HEARLY 1
#define WINY 64
#define PRIO 1
#define TAU 335
#define TBASE 30
#define MID 0
#define FDELTA 35
#define PV0 28
#define TLAST 1080
#define TBIG 100
#define WGBASE 1
#define WOD0 45
#define WOD1 75
#define PSLEEP 0
#define GDELTA 22
#define WINB 5
#define SPREAD 1
#define TOUCH 0
#define WOPRE 8
#define GLOBAL 1
#define TWAIT 300
#define NSH 13
#define CODE_PF 1
#define ABL 0

namespace fused {
typedef unsigned v16u __attribute__((ext_vector_type(16)));
typedef unsigned v4u __attribute__((ext_vector_type(4)));
typedef unsigned v2u __attribute__((ext_vector_type(2)));
typedef float v4f __attribute__((ext_vector_type(4)));
typedef float v2f __attribute__((ext_vector_type(2)));
typedef short s16x4 __attribute__((ext_vector_type(4)));
typedef __bf16 bf16x2 __attribute__((ext_vector_type(2)));
typedef __bf16 bf16x8 __attribute__((ext_vector_type(8)));
typedef __amdgpu_buffer_rsrc_t rsrc_t;
typedef unsigned long long u64;
#define LAS __attribute__((address_space(3)))

constexpr int D = 512, HQ = 32, HKV = 8, SK = 8192, DH = 128, F = 1792;
constexpr int AUX_NT = 2, AUX_SC1 = 16;
constexpr int KV_BYTES = 2 * HKV * SK * DH * 2;
constexpr int BLK = 1024;
constexpr int WS_E = 0, WS_T0 = 128, R_QP = 256, R_HP = R_QP + 8 * 512, R_GP = R_HP + 4 * 512, R_LS = R_GP + 7 * BLK, R_R1 = R_LS + 16 * 128, R_SP = R_R1 + 64 * 512,
              WS_END = SPREAD ? 16 << 17 : 16 << 17;
template <int S> __device__ __forceinline__ int blk_at(int region, int j) {
  return SPREAD ? (((j + (j >> 4)) & 15) << 17) + region + (j >> 4) * S : region * 16 + j * S; }
constexpr unsigned SPIN_CAP = 1u << 18;
constexpr float LOG2E = 1.4426950408889634f;
constexpr float SC2 = 0.08838834764831845f * LOG2E;
constexpr int PW = 192;
constexpr int L_O = 0, L_X = 4096, L_R1 = L_X + 2048, L_R2 = L_R1 + 4 * PW, L_Q = L_R2 + 4 * PW, L_ML = L_Q + 256, L_ON = L_ML + 64, L_CNT = L_ON + 32, L_T0 = L_CNT + 6, L_TAG = L_CNT + 8, L_END = L_CNT + 12;
enum { C_AUX = 0, C_SP = 1, C_Q = 2, C_ML = 3, C_ISSUED = 4, C_G = 5 };

__device__ __forceinline__ rsrc_t mk(const void* p, int bytes) { return __builtin_amdgcn_make_buffer_rsrc((void*)p, 0, bytes, 0x00020000); }
template <int AUX> __device__ __forceinline__ v4u ld16(rsrc_t r, int voff, int soff) { return __builtin_amdgcn_raw_buffer_load_b128(r, voff, soff, AUX); }
template <int AUX> __device__ __forceinline__ v2u ld8(rsrc_t r, int voff, int soff) { return __builtin_amdgcn_raw_buffer_load_b64(r, voff, soff, AUX); }
__device__ __forceinline__ unsigned ld4(rsrc_t r, int voff, int soff) { return __builtin_amdgcn_raw_buffer_load_b32(r, voff, soff, 0); }
__device__ __forceinline__ void st16(v4u v, rsrc_t r, int voff, int soff) { __builtin_amdgcn_raw_buffer_store_b128(v, r, voff, soff, AUX_SC1); }
__device__ __forceinline__ float asf(unsigned u) { return __builtin_bit_cast(float, u); }
__device__ __forceinline__ unsigned asu(float f) { return __builtin_bit_cast(unsigned, f); }
__device__ __forceinline__ unsigned uni(unsigned u) { return (unsigned)__builtin_amdgcn_readfirstlane((int)u); }
__device__ __forceinline__ float lo16(unsigned u) { return asf(u << 16); }
__device__ __forceinline__ float hi16(unsigned u) { return asf(u & 0xffff0000u); }
__device__ __forceinline__ unsigned pk(float a, float b) { return __builtin_bit_cast(unsigned, bf16x2{(__bf16)a, (__bf16)b}); }
__device__ __forceinline__ float rbf(float a) { return (float)(__bf16)a; }
template <int C> __device__ __forceinline__ unsigned dppu(unsigned x) { return (unsigned)__builtin_amdgcn_mov_dpp((int)x, C, 0xf, 0xf, true); }
template <int C> __device__ __forceinline__ float dppf(float x) { return asf(dppu<C>(asu(x))); }
template <int I> __device__ __forceinline__ float to_bank(float old, float src) {
  return asf((unsigned)__builtin_amdgcn_update_dpp((int)asu(old), (int)asu(src), 0x110 + 4 * I, 0xf, 1 << I, false)); }
constexpr int XOR1 = 0xB1, XOR2 = 0x4E, HMIR = 0x141, SHL4 = 0x104, SHL8 = 0x108, ROR4 = 0x124, ROR8 = 0x128, ROR12 = 0x12C;
__device__ __forceinline__ float sw32_add(float a, float b) {
  auto t = __builtin_amdgcn_permlane32_swap(asu(a), asu(b), false, false); return asf(t[0]) + asf(t[1]); }
__device__ __forceinline__ float sw16_add(float a, float b) {
  auto t = __builtin_amdgcn_permlane16_swap(asu(a), asu(b), false, false); return asf(t[0]) + asf(t[1]); }
__device__ __forceinline__ float sw32_max(float a) {
  auto t = __builtin_amdgcn_permlane32_swap(asu(a), asu(a), false, false); return fmaxf(asf(t[0]), asf(t[1])); }
__device__ __forceinline__ float sw16_max(float a) {
  auto t = __builtin_amdgcn_permlane16_swap(asu(a), asu(a), false, false); return fmaxf(asf(t[0]), asf(t[1])); }
__device__ __forceinline__ v4f mfma32(v4u a, v4u b, v4f c) {
  return __builtin_amdgcn_mfma_f32_16x16x32_bf16(__builtin_bit_cast(bf16x8, a), __builtin_bit_cast(bf16x8, b), c, 0, 0, 0); }
__device__ __forceinline__ v4f mfma16(v2u a, v2u b, v4f c) {
  return __builtin_amdgcn_mfma_f32_16x16x16bf16_1k(__builtin_bit_cast(s16x4, a), __builtin_bit_cast(s16x4, b), c, 0, 0, 0); }
__device__ __forceinline__ unsigned perm_lo(unsigned even, unsigned odd) { return __builtin_amdgcn_perm(odd, even, 0x05040100u); }
__device__ __forceinline__ unsigned perm_hi(unsigned even, unsigned odd) { return __builtin_amdgcn_perm(odd, even, 0x07060302u); }
__device__ __forceinline__ float exp2f_(float x) { return __builtin_amdgcn_exp2f(x); }
__device__ __forceinline__ float rcpf_(float x) { return __builtin_amdgcn_rcpf(x); }
#define SB() __builtin_amdgcn_sched_barrier(0)
#define VMWAIT(n) __builtin_amdgcn_s_waitcnt(0x0F70 | ((n) & 15) | (((n) >> 4) << 14))
#define LDS_BARRIER() do { SB(); asm volatile("s_waitcnt lgkmcnt(0)\n\ts_barrier" ::: "memory"); SB(); } while (0)

struct Tag { unsigned a, b; };
__device__ __forceinline__ bool fresh(v4u r, Tag t) { return (r.y == t.a) & (r.w == t.b); }
__device__ __forceinline__ v4u rec(unsigned p0, unsigned p1, Tag t) { return v4u{p0, t.a, p1, t.b}; }
template <int N> __device__ __forceinline__ bool all_fresh(const v4u (&r)[N], Tag t) {
  bool ok = true;
#pragma unroll
  for (int k = 0; k < N; ++k) ok = ok & fresh(r[k], t);
  return ok;
}
template <int N> __device__ __forceinline__ void request(v4u (&r)[N], const int (&off)[N], rsrc_t ws) {
#pragma unroll
  for (int k = 0; k < N; ++k) r[k] = ld16<AUX_SC1>(ws, off[k], 0);
}
template <int N, int SLEEP = 0> __device__ __forceinline__ unsigned await(v4u (&r)[N], const int (&off)[N], rsrc_t ws, Tag t) {
  bool ok = all_fresh<N>(r, t);
  unsigned spin = 0;
  if (__builtin_expect(!__all(ok), 0)) {
#pragma nounroll
    do {
      if (SLEEP) __builtin_amdgcn_s_sleep(SLEEP);
      asm volatile("" ::: "memory");
      if (!ok) request<N>(r, off, ws);
      ok = all_fresh<N>(r, t);
      ++spin;
    } while (!__all(ok) && spin < SPIN_CAP);
  }
  asm volatile("" ::: "memory");
  return spin;
}
__device__ __forceinline__ void lds_arrive(LAS unsigned* c, int lane) {
  asm volatile("s_waitcnt lgkmcnt(0)" ::: "memory");
  if (lane == 0) __hip_atomic_fetch_add(c, 1u, __ATOMIC_RELAXED, __HIP_MEMORY_SCOPE_WORKGROUP);
}
template <int SLEEP = 1> __device__ __forceinline__ void lds_wait(LAS unsigned* c, unsigned n) {
#pragma nounroll
  for (unsigned spin = 0; spin < (SPIN_CAP << 4); ++spin) {
    if (uni(__hip_atomic_load(c, __ATOMIC_RELAXED, __HIP_MEMORY_SCOPE_WORKGROUP)) >= n) break;
    if (SLEEP) __builtin_amdgcn_s_sleep(SLEEP);
  }
  asm volatile("" ::: "memory");
}

__device__ __forceinline__ float half_sum(float a, float b) {
  float x = sw32_add(a, b);
  x += dppf<XOR1>(x); x += dppf<XOR2>(x); x += dppf<HMIR>(x); x += dppf<ROR8>(x); return sw16_add(x, x); }
struct Stat { float m0, r0, m1, r1; };
__device__ __forceinline__ Stat ln_stats(const float (&u)[8], const float (&v)[8]) {
  const float su = ((u[0] + u[1]) + (u[2] + u[3])) + ((u[4] + u[5]) + (u[6] + u[7]));
  const float sv = ((v[0] + v[1]) + (v[2] + v[3])) + ((v[4] + v[5]) + (v[6] + v[7]));
  const float mf = half_sum(su, sv) * (1.f / D);
  const float mu = asf(__builtin_amdgcn_readlane(asu(mf), 0)), mv = asf(__builtin_amdgcn_readlane(asu(mf), 32));
  float qu = 0.f, qv = 0.f;
#pragma unroll
  for (int j = 0; j < 8; ++j) { const float a = u[j] - mu, b = v[j] - mv; qu = fmaf(a, a, qu); qv = fmaf(b, b, qv); }
  const float var = rbf(half_sum(qu, qv) * (1.f / D));
  const float rs = rcpf_(rbf(__builtin_amdgcn_sqrtf(rbf(var + 1e-5f))));
  return Stat{rbf(mu), asf(__builtin_amdgcn_readlane(asu(rs), 0)), rbf(mv), asf(__builtin_amdgcn_readlane(asu(rs), 32))};
}
__device__ __forceinline__ float ln_apply(float x, float m, float rs) { return rbf(rbf(x - m) * rs); }

__device__ __forceinline__ v4f product32(const v2u (&w)[8], v4u in, const unsigned (&mj)[4]) {
  v4f acc = {0.f, 0.f, 0.f, 0.f};
#pragma unroll
  for (int j = 0; j < 4; ++j) {
    v4u a;
#pragma unroll
    for (int t = 0; t < 4; ++t) a[t] = (j & 1) ? perm_hi(w[2 * t][j >> 1], w[2 * t + 1][j >> 1]) : perm_lo(w[2 * t][j >> 1], w[2 * t + 1][j >> 1]);
    acc = mfma32(a, v4u{in[0] & mj[j], in[1] & mj[j], in[2] & mj[j], in[3] & mj[j]}, acc);
  }
  return acc;
}
__device__ __forceinline__ bool sum_products(v4f p, float* buf, int a, LAS unsigned* c, int lane, v2f& s) {
  if ((lane & 15) < 8) *(v4f*)(buf + a * PW + ((lane >> 2) & 1) * 96 + (lane & 3) * 24 + (lane >> 4) * 4) = p;
  asm volatile("s_waitcnt lgkmcnt(0)" ::: "memory");
  unsigned old = 0;
  if (lane == 0) old = __hip_atomic_fetch_add(c, 1u, __ATOMIC_RELAXED, __HIP_MEMORY_SCOPE_WORKGROUP);
  if (uni(old) != 3u) return false;
  asm volatile("" ::: "memory");
  const float* q = buf + (lane >> 5) * 96 + (lane & 1) * 48 + ((lane & 31) >> 1);
  s = (v2f{q[0], q[24]} + v2f{q[PW], q[PW + 24]}) + (v2f{q[2 * PW], q[2 * PW + 24]} + v2f{q[3 * PW], q[3 * PW + 24]});
  return true;
}

#define ST(i)
#define CNT(i, v) (void)(v)
#define ST_FLUSH()

__global__ void __launch_bounds__(768, 3)
k_fused(const void*, const void*, const void*, const void*, const void*, const void*, const void*, const void*, const void*, void*, void*) {
  v16u ka; v4u kb; v2u kc2; u64 t_in;
  asm volatile("s_memrealtime %3\n\ts_load_dwordx16 %0, %4, 0x0\n\ts_load_dwordx4 %1, %4, 0x40\n\ts_load_dwordx2 %2, %4, 0x50"
               : "=&s"(ka), "=&s"(kb), "=&s"(kc2), "=&s"(t_in) : "s"(__builtin_amdgcn_kernarg_segment_ptr()));
  SB();
  const int tid = threadIdx.x, lane = tid & 63, wv = uni(tid >> 6);
  const int blk = blockIdx.x, x8 = blk & 7, i32 = blk >> 3, rg = i32 >> 3, k8 = i32 & 7, bat = i32 >> 4;
  const int g = lane >> 4, c16 = lane & 15, hg = x8 * 4 + rg, w = wv - 4;
  int kvbase = (bat * HKV + x8) * SK * 256 + (ROT ? (i32 & 15) * 8192 : (i32 & 15) * 131072) + w * 1024;
#define UNTIL(t) do { while ((int)((unsigned)__builtin_amdgcn_s_memrealtime() - (unsigned)t_in) < (int)(t)) { if (PSLEEP) __builtin_amdgcn_s_sleep(1); } SB(); } while (0)
#define PACE(n) do { UNTIL((base + (n) * tau) >> 4); if (WINB < 16) VMWAIT(WINB - 1); if (WIN < WINB && !big) VMWAIT(WIN - 1); SB(); } while (0)
#define PIECE(n) (kvbase + (ROT ? ((((n) + i32) & 15) << 17) : (n) * 8192))
  int koff = ((lane >> 2) & 3) * 256 + g * 64 + (lane & 3) * 16;
  asm volatile("s_waitcnt lgkmcnt(0)" : "+s"(ka), "+s"(kb), "+s"(kc2), "+s"(t_in), "+s"(kvbase), "+v"(koff));
#define ARG(v, i) ((void*)(((u64)uni((v)[2 * (i) + 1]) << 32) | uni((v)[2 * (i)])))
  const void *x = ARG(ka, 0), *kc = ARG(ka, 1), *vc = ARG(ka, 2), *wq = ARG(ka, 3), *wo = ARG(ka, 4), *w1 = ARG(ka, 5), *b1 = ARG(ka, 6), *w2 = ARG(ka, 7),
             *b2 = ARG(kb, 0);
  void *out = ARG(kb, 1), *wsp = ARG(kc2, 0);
  __shared__ __attribute__((aligned(16))) float lds[L_END];
  LAS unsigned* cnt = (LAS unsigned*)(lds + L_CNT);
  u64 pc;
  int code_left, code_left1;
  asm volatile("s_getpc_b64 %0\n.Lk_fused_pc:\n\ts_mov_b32 %1, .Lk_fused_end0-.Lk_fused_pc\n\ts_mov_b32 %2, .Lk_fused_end1-.Lk_fused_pc"
               : "=s"(pc), "=s"(code_left), "=s"(code_left1));
  code_left = max(code_left, code_left1);
  const int id2 = x8 * 14 + i32 - 18, rb = id2 >> 3, c2 = id2 & 7;
  const bool isq = i32 < 16, is1 = i32 >= 16 && i32 < 23, is2 = i32 >= 18;
  const bool big = isq || is1;
  const rsrc_t rk = mk(kc, KV_BYTES);
  v4u K[16];
  int wv2 = wv;
  asm volatile("" : "+s"(wv2));
  if (wv2 >= 4 && !big) {
#pragma unroll
    for (int n = 0; n < KFRONT; ++n) { K[n] = ld16<AUX_NT>(rk, koff, PIECE(n)); SB(); }
  } else {
#pragma unroll
    for (int n = 0; n < KFRONT; ++n) asm volatile("" : "=v"(K[n]));
    if (tid < 6) cnt[tid] = 0u;
    if (tid == 6) cnt[L_T0 - L_CNT] = (unsigned)t_in;
    if (tid == 7) cnt[L_TAG + 2 - L_CNT] = 0u;
  }
  LDS_BARRIER();
  if (WGBASE) t_in = uni(cnt[L_T0 - L_CNT]);

  const int lb = (lane >> 2) & 1;

  if (wv >= 4) {
    const rsrc_t rv = mk(vc, KV_BYTES);
    const int voff = lane * 16;
    v4u V[16];
    int tau = big ? (TLAST - TBIG) * 16 / (32 - KFRONT) : (TLAST - TBASE) * 16 / (32 - KFRONT), base = TLAST * 16 - 31 * tau;
#define REPLAN(n, sh) do { const int end_ = (TLAST + (sh)) * 16; tau = (end_ - base - ((n) - 1) * tau) / (32 - (n)); base = end_ - 31 * tau; } while (0)
    if (big) {
      UNTIL(TBIG);
#pragma unroll
      for (int n = 0; n < KFRONT; ++n) { K[n] = ld16<AUX_NT>(rk, koff, PIECE(n)); SB(); }
    }
#pragma unroll
    for (int n = KFRONT; n < QPOS; ++n) {
      if (GLOBAL && n == NSH) REPLAN(n, (int)uni(__hip_atomic_load(cnt + (L_TAG + 2 - L_CNT), __ATOMIC_RELAXED, __HIP_MEMORY_SCOPE_WORKGROUP)));
      if (n < QPOS - BURST) PACE(n);
      if (n < 16) K[n] = ld16<AUX_NT>(rk, koff, PIECE(n));
      else V[n - 16] = ld16<AUX_NT>(rv, voff, PIECE(n - 16));
      SB();
    }
    ST(2);
    if (!(ABL & 8)) lds_wait(cnt + C_Q, 3u);
    ST(3);
    const int bank = (lane >> 2) & 3, hh = lane & 3;
    const v4u qf = *(const v4u*)(lds + L_Q + hh * 64 + g * 16 + bank * 4);
    const v4u lt = *(const v4u*)(lds + L_TAG);
    v4f d[16];
#pragma unroll
    for (int n = 0; n < 16; ++n) d[n] = mfma32(K[n], qf, v4f{0.f, 0.f, 0.f, 0.f});
    SB();
    ST(1);
    float S[4], sn[16];
#pragma unroll
    for (int n = 0; n < 16; ++n) { d[n][0] += dppf<SHL4>(d[n][1]); d[n][2] += dppf<SHL4>(d[n][3]); }
    SB();
#pragma unroll
    for (int n = 0; n < 16; ++n) sn[n] = d[n][0] + dppf<SHL8>(d[n][2]);
    SB();
#pragma unroll
    for (int z = 0; z < 4; ++z) S[z] = to_bank<1>(sn[4 * z], sn[4 * z + 1]);
#pragma unroll
    for (int z = 0; z < 4; ++z) S[z] = to_bank<2>(S[z], sn[4 * z + 2]);
#pragma unroll
    for (int z = 0; z < 4; ++z) S[z] = to_bank<3>(S[z], sn[4 * z + 3]);
    SB();
    ST(7);
#pragma unroll
    for (int n = QPOS; n < QPOS + MID; ++n) { PACE(n); ST(9); V[n - 16] = ld16<AUX_NT>(rv, voff, PIECE(n - 16)); SB(); }
    ST(10);
    float mw = fmaxf(fmaxf(S[0], S[1]), fmaxf(S[2], S[3]));
    mw = fmaxf(mw, dppf<ROR4>(mw)); mw = fmaxf(mw, dppf<ROR8>(mw)); mw = sw32_max(sw16_max(mw));
    mw *= SC2;
    float lw = 0.f;
#pragma unroll
    for (int z = 0; z < 4; ++z) { S[z] = exp2f_(fmaf(S[z], SC2, -mw)); lw += S[z]; }
    lw += dppf<ROR4>(lw); lw += dppf<ROR8>(lw); lw = sw16_add(lw, lw); lw = sw32_add(lw, lw);
    if (lane < 4) *(v2f*)(lds + L_ML + lane * 16 + w * 2) = v2f{mw, lw};
    lds_arrive(cnt + C_ML, lane);
    ST(4);
    if (GLOBAL) REPLAN(QPOS + MID, (int)uni(lt.z));
    unsigned P[4], bm[4];
#pragma unroll
    for (int z = 0; z < 4; ++z) { P[z] = pk(S[z], dppf<SHL4>(S[z])); bm[z] = bank == z ? 0xffffffffu : 0u; }
    v4u Bf[2][4];
#pragma unroll
    for (int be = 0; be < 2; ++be) {
      const unsigned p0 = P[2 * be], p1 = P[2 * be + 1];
      Bf[be][0] = v4u{p0 & bm[0], dppu<ROR8>(p0) & bm[0], p1 & bm[0], dppu<ROR8>(p1) & bm[0]};
      Bf[be][1] = v4u{dppu<ROR4>(p0) & bm[1], dppu<ROR12>(p0) & bm[1], dppu<ROR4>(p1) & bm[1], dppu<ROR12>(p1) & bm[1]};
      Bf[be][2] = v4u{dppu<ROR8>(p0) & bm[2], p0 & bm[2], dppu<ROR8>(p1) & bm[2], p1 & bm[2]};
      Bf[be][3] = v4u{dppu<ROR12>(p0) & bm[3], dppu<ROR4>(p0) & bm[3], dppu<ROR12>(p1) & bm[3], dppu<ROR4>(p1) & bm[3]};
    }
    SB();
    v4f acc[2] = {v4f{0.f, 0.f, 0.f, 0.f}, v4f{0.f, 0.f, 0.f, 0.f}};
#pragma unroll
    for (int be = 0; be < 2; ++be) {
#pragma unroll
      for (int n = be ? PV0 : QPOS + MID; n < (be ? 32 : PV0); ++n) { PACE(n); V[n - 16] = ld16<AUX_NT>(rv, voff, PIECE(n - 16)); SB(); }
      if (be) { lds_arrive(cnt + C_ISSUED, lane); ST(5); }
      v4u A[8];
#pragma unroll
      for (int t = 0; t < 4; ++t) {
#pragma unroll
        for (int u = 0; u < 4; ++u) {
          A[2 * u][t] = perm_lo(V[8 * be + 2 * t][u], V[8 * be + 2 * t + 1][u]);
          A[2 * u + 1][t] = perm_hi(V[8 * be + 2 * t][u], V[8 * be + 2 * t + 1][u]);
        }
        SB();
      }
#pragma unroll
      for (int u = 0; u < 4; ++u) { acc[0] = mfma32(A[2 * u], Bf[be][u], acc[0]); acc[1] = mfma32(A[2 * u + 1], Bf[be][u], acc[1]); }
      SB();
    }
    *(v4f*)(lds + L_O + w * 512 + lane * 4) = acc[0];
    *(v4f*)(lds + L_O + w * 512 + 256 + lane * 4) = acc[1];
    LDS_BARRIER();
    ST(6);
    const Tag tag = {uni(lt.x), uni(lt.y)};
    const rsrc_t rws = mk(wsp, WS_END);
    ST(8);
    if (w == 7) {
      const int gq = (i32 >> 2) & 3, jq = i32 & 3;
      const int fcol = x8 * 64 + gq * 16 + g * 4 + jq;
      const unsigned xo = __builtin_amdgcn_raw_buffer_load_b16(mk(x, 2 * D * 2), fcol * 2, bat * D * 2, 0);
      const unsigned bias2 = __builtin_amdgcn_raw_buffer_load_b16(mk(b2, D * 2), fcol * 2, 0, 0);
      SB();
      v4u fg[1], fs[2];
      const int go[1] = {blk_at<BLK>(R_GP, (c16 < 14 ? c16 : 0) * 8 + x8) + (bat * 32 + gq * 8 + g * 2 + (jq >> 1)) * 16};
      int so[2] = {blk_at<512>(R_SP, lane * 8 + x8) + (bat * 16 + gq * 4 + jq) * 16, blk_at<512>(R_SP, (lane + 64) * 8 + x8) + (bat * 16 + gq * 4 + jq) * 16};
      __builtin_amdgcn_s_sleep(GDELTA);
      request<1>(fg, go, rws);
      await<1, 6>(fg, go, rws, tag);
      float gs = c16 < 14 ? asf((jq & 1) ? fg[0].z : fg[0].x) : 0.f;
      gs += dppf<XOR1>(gs); gs += dppf<XOR2>(gs); gs += dppf<HMIR>(gs); gs += dppf<ROR8>(gs);
      const float mv = rbf(rbf(gs) + lo16(bias2));
      float gv = rbf(mv * rcpf_(1.f + exp2f_(-LOG2E * mv))), xv = lo16(xo);
      int ooff = c16 == 0 ? (bat * D + fcol) * 2 : 2 * D * 2;
      asm volatile("" : "+v"(gv), "+v"(xv), "+v"(ooff), "+v"(so[0]), "+v"(so[1]));
      lds_wait<0>(cnt + C_SP, 4u);
      { const unsigned t_sp = (unsigned)__builtin_amdgcn_s_memrealtime();
        while ((unsigned)__builtin_amdgcn_s_memrealtime() - t_sp < FDELTA) {} }
      request<2>(fs, so, rws);
      CNT(15, (await<2, FSLEEP>(fs, so, rws, tag)));
      ST(11);
      float sc = sw16_add(sw32_add(lo16(fs[0].x) + lo16(fs[1].x), lo16(fs[0].z) + lo16(fs[1].z)), sw32_add(hi16(fs[0].x) + hi16(fs[1].x), hi16(fs[0].z) + hi16(fs[1].z)));
      sc += dppf<XOR1>(sc); sc += dppf<XOR2>(sc); sc += dppf<HMIR>(sc); sc += dppf<ROR8>(sc);
      const float ov = xv + rbf(rbf(sc) * gv);
      __builtin_amdgcn_raw_buffer_store_b16((unsigned short)pk(ov, ov), mk(out, 2 * D * 2), ooff, 0, 0);
      if (blk == 0 && lane == 0) __builtin_amdgcn_raw_buffer_store_b64(v2u{tag.a, tag.b}, rws, 0, WS_E, AUX_SC1);
      UNTIL(min(4000 + (int)uni(lt.w), 6000));
      asm volatile(".Lk_fused_end0:");
      ST(12);
    }
    ST_FLUSH();
    return;
  }

  const int a = wv;
  if (PRIO) __builtin_amdgcn_s_setprio(2);
  const rsrc_t rws = mk(wsp, WS_END);
  unsigned mj[4];
#pragma unroll
  for (int j = 0; j < 4; ++j) mj[j] = (lane & 11) == j ? 0xffffffffu : 0u;
  const v2u ep = ld8<AUX_SC1>(rws, 0, WS_E);
  unsigned code_pf;
  Tag tag;
  v2f gpart = {0.f, 0.f};
  bool gpub = false;
  if (big) {
    const rsrc_t rx = mk(x, 2 * D * 2);
    const v4u xa0 = ld16<0>(rx, lane * 16, 0), xa1 = ld16<0>(rx, lane * 16, D * 2);
    const int ld = isq ? HQ * DH * 2 : F * 2, idb = isq ? x8 * 16 + (i32 & 7) * 2 + (i32 >> 3) : x8 * 7 + i32 - 16, cg = idb >> 1, row0 = (idb & 1) * 256 + a * 64;
    const rsrc_t rw = mk(isq ? wq : w1, (ABL & (isq ? 32 : 4)) ? 0 : D * ld);
    v2u wA[2][8];
    const int woff = g * 8 * ld + c16 * 8;
#pragma unroll
    for (int t = 0; t < 2; ++t)
#pragma unroll
      for (int i = 0; i < 8; ++i) wA[t][i] = ld8<AUX_NT>(rw, woff, (row0 + t * 32 + i) * ld + cg * 128);
    SB();
    tag = Tag{uni(ep.x) + 1u, uni(ep.y) + 0x9E3779B9u};
    if (GLOBAL && blk < 8 && tid == 0) st16(rec((unsigned)t_in, (unsigned)t_in, tag), rws, blk * 16, WS_T0);
    float u[8], v[8];
#pragma unroll
    for (int t = 0; t < 4; ++t) { u[2 * t] = lo16(xa0[t]); u[2 * t + 1] = hi16(xa0[t]); v[2 * t] = lo16(xa1[t]); v[2 * t + 1] = hi16(xa1[t]); }
#pragma nounroll
    for (int pass = isq ? 1 : 0; pass < 2; ++pass) {
      const Stat st = ln_stats(u, v);
#pragma unroll
      for (int j = 0; j < 8; ++j) { u[j] = ln_apply(u[j], st.m0, st.r0); v[j] = ln_apply(v[j], st.m1, st.r1); }
    }
    float* xs = lds + L_X + a * 512;
    *(v4u*)(xs + lane * 4) = v4u{pk(u[0], u[1]), pk(u[2], u[3]), pk(u[4], u[5]), pk(u[6], u[7])};
    *(v4u*)(xs + 256 + lane * 4) = v4u{pk(v[0], v[1]), pk(v[2], v[3]), pk(v[4], v[5]), pk(v[6], v[7])};
    asm volatile("s_waitcnt lgkmcnt(0)" ::: "memory");
    v4f acc[4];
#pragma unroll
    for (int t = 0; t < 2; ++t) {
      const v4u in = *(const v4u*)(xs + lb * 256 + (row0 >> 1) + (t * 4 + g) * 4);
#pragma unroll
      for (int j = 0; j < 4; ++j) {
        v4u f;
#pragma unroll
        for (int k = 0; k < 4; ++k) f[k] = (j & 1) ? perm_hi(wA[t][2 * k][j >> 1], wA[t][2 * k + 1][j >> 1]) : perm_lo(wA[t][2 * k][j >> 1], wA[t][2 * k + 1][j >> 1]);
        acc[j] = mfma32(f, in, t ? acc[j] : v4f{0.f, 0.f, 0.f, 0.f});
      }
    }
    const v4f p = (lane & 2) ? ((lane & 1) ? acc[3] : acc[2]) : ((lane & 1) ? acc[1] : acc[0]);
    v2f sum;
    if (sum_products(p, lds + L_R1, a, cnt + C_AUX, lane, sum)) {
      const unsigned pp = pk(sum.x, sum.y);
      st16(rec(pp, dppu<XOR1>(pp), tag), rws, (lane & 1) ? WS_END : lane * 8, blk_at<512>(isq ? R_QP : R_HP, idb));
    }
  } else {
    tag = Tag{uni(ep.x) + 1u, uni(ep.y) + 0x9E3779B9u};
  }
  ST(1);
  code_pf = ld4(mk((const void*)(pc & ~(u64)127), code_left + (int)(pc & 127)), lane * 128, (a & 1) * 8192);
  if (a < 2) {
    v4u qr[2];
    const int qo[2] = {blk_at<512>(R_QP, (x8 * 8 + a * 4 + g) * 2) + (bat * 16 + c16) * 16, blk_at<512>(R_QP, (x8 * 8 + a * 4 + g) * 2 + 1) + (bat * 16 + c16) * 16};
    UNTIL(QWAIT);
    request<2>(qr, qo, rws);
    CNT(13, (await<2, 16>(qr, qo, rws, tag)));
    *(v2u*)(lds + L_Q + (a * 64 + lane) * 2) = v2u{pk(lo16(qr[0].x) + lo16(qr[1].x), hi16(qr[0].x) + hi16(qr[1].x)), pk(lo16(qr[0].z) + lo16(qr[1].z), hi16(qr[0].z) + hi16(qr[1].z))};
    lds_arrive(cnt + C_Q, lane);
  }
  if (a == 2) {
    int sh = 0, mx = 0;
    if (GLOBAL) {
      v4u tr[1];
      const int to[1] = {WS_T0 + (lane & 7) * 16};
      UNTIL(TWAIT);
      request<1>(tr, to, rws);
      await<1, 16>(tr, to, rws, tag);
      mx = (int)(tr[0].x - (unsigned)t_in); sh = min(max(mx, -300), 300); if (HOLDX == 1) mx = -mx;
      sh += (int)dppu<XOR1>(sh); sh += (int)dppu<XOR2>(sh); sh += (int)dppu<HMIR>(sh);
      mx = max(mx, (int)dppu<XOR1>(mx)); mx = max(mx, (int)dppu<XOR2>(mx)); mx = max(mx, (int)dppu<HMIR>(mx));
      sh >>= 3; if (HOLDX == 1) mx = -mx;
    }
    if (lane == 0) *(v4u*)(lds + L_TAG) = v4u{tag.a, tag.b, (unsigned)sh, (unsigned)mx};
    lds_arrive(cnt + C_Q, lane);
  }
  ST(2);
  asm volatile("" :: "v"(code_pf));
  if (is2) {
    const rsrc_t r2 = mk(w2, (ABL & 2) ? 0 : D * F * 2);
    v2u wB[8];
    v4u hr[2];
    const int hoff = (lb * 16 + (a & 1) * 8 + g * 2 + ((lane >> 3) & 1)) * 16;
    const int ho[2] = {blk_at<512>(R_HP, (2 * rb + (a >> 1)) * 2) + hoff, blk_at<512>(R_HP, (2 * rb + (a >> 1)) * 2 + 1) + hoff};
    lds_wait(cnt + C_Q, 3u);
    UNTIL(HWAIT + 100);
    request<2>(hr, ho, rws);
    const v2u bias1 = ld8<0>(mk(b1, F * 2), g * 16 + ((lane >> 3) & 1) * 8, (rb * 128 + a * 32) * 2);
#pragma unroll
    for (int i = 0; i < 8; ++i) wB[i] = ld8<AUX_NT>(r2, g * 8 * 1024 + c16 * 8 + (i & 3) * 1024, (rb * 128 + a * 32 + (i & 4)) * 1024 + c2 * 128);
    SB();
    await<2, 16>(hr, ho, rws, tag);
    float h[4] = {lo16(hr[0].x) + lo16(hr[1].x), hi16(hr[0].x) + hi16(hr[1].x), lo16(hr[0].z) + lo16(hr[1].z), hi16(hr[0].z) + hi16(hr[1].z)};
    const float bb[4] = {lo16(bias1.x), hi16(bias1.x), lo16(bias1.y), hi16(bias1.y)};
    constexpr float G0 = -2.f * 0.7978845608028654f * LOG2E, G1 = G0 * 0.044715f;
#pragma unroll
    for (int j = 0; j < 4; ++j) {
      const float t = rbf(rbf(h[j]) + bb[j]);
      h[j] = t * rcpf_(1.f + exp2f_(t * fmaf(G1, t * t, G0)));
    }
    const unsigned h0 = pk(h[0], h[1]), h1 = pk(h[2], h[3]);
    gpub = sum_products(product32(wB, v4u{h0, h1, dppu<ROR8>(h0), dppu<ROR8>(h1)}, mj), lds + L_R2, a, cnt + C_G, lane, gpart);
    ST(11);
  }
  lds_wait(cnt + C_ML, 8u);
  ST(4);
  if (a == 3 && lane < 32) ((unsigned*)lds)[L_ON + lane] = 0xffffffffu;
  const int mh = (lane >> 1) & 3;
  float fw[8], lse;
  {
    v4f ml[4];
#pragma unroll
    for (int t = 0; t < 4; ++t) ml[t] = *(const v4f*)(lds + L_ML + mh * 16 + 4 * t);
    const float M = fmaxf(fmaxf(fmaxf(ml[0][0], ml[0][2]), fmaxf(ml[1][0], ml[1][2])), fmaxf(fmaxf(ml[2][0], ml[2][2]), fmaxf(ml[3][0], ml[3][2])));
    float L = 0.f;
#pragma unroll
    for (int t = 0; t < 4; ++t) { fw[2 * t] = exp2f_(ml[t][0] - M); fw[2 * t + 1] = exp2f_(ml[t][2] - M); L = fmaf(fw[2 * t], ml[t][1], fmaf(fw[2 * t + 1], ml[t][3], L)); }
    const float il = rcpf_(L);
#pragma unroll
    for (int t = 0; t < 8; ++t) fw[t] *= il;
    lse = M + __builtin_amdgcn_logf(L);
    asm volatile("" : "+v"(fw[0]), "+v"(fw[1]), "+v"(fw[2]), "+v"(fw[3]), "+v"(fw[4]), "+v"(fw[5]), "+v"(fw[6]), "+v"(fw[7]));
    if (a == 0 && lane < 8) st16(rec(asu(lse), asu(lse), tag), rws, lane * 16, blk_at<128>(R_LS, x8 * 32 + i32));
  }
  const int dq = k8 >> 1, ch = k8 & 1;
  v2u wO[8];
  v4u lr[1], pr[2];
  const int lo[1] = {blk_at<128>(R_LS, x8 * 32 + (a & 1) * 16 + (lane >> 3) + (lane & 1) * 8) + rg * 32};
  int po[2] = {blk_at<512>(R_R1, hg * 32 + (a & 1) * 16 + (lane >> 3)) + dq * 128 + (lane & 7) * 16,
               blk_at<512>(R_R1, hg * 32 + (a & 1) * 16 + (lane >> 3) + 8) + dq * 128 + (lane & 7) * 16};
  int on_w = (L_ON + a * 16 + (lane & 7) * 2 + (lane >> 5)) * 4, on_r = (L_ON + lb * 16 + g * 4) * 4;
  int soff = c16 < 8 ? blk_at<512>(R_SP, (hg * 4 + dq) * 8 + ch * 4 + a) + (lb * 16 + g * 4 + (lane & 3)) * 16 : WS_END;
  const rsrc_t ro = mk(wo, (ABL & 2) ? 0 : D * HQ * DH * 2);
  const int wvo = g * 8 * 1024 + c16 * 8, wso = (hg * 128 + dq * 32) * 1024 + ch * 512 + a * 128;
  const int shift = GLOBAL ? (int)uni(((const unsigned*)lds)[L_TAG + 2]) : 0;
  lds_wait(cnt + C_ISSUED, 8u);
  UNTIL(TLAST + WOD0 + shift);
  request<1>(lr, lo, mk(wsp, a < 2 ? WS_END : 0));
#pragma unroll
  for (int i = 0; i < WOPRE; ++i) {
    if (i == 4) UNTIL(TLAST + WOD1 + shift);
    wO[i] = ld8<AUX_NT>(ro, wvo + (i & 3) * 1024, wso + (i & 4) * 1024);
    SB();
  }
  const float* mp = lds + L_O + (a * 16 + ((lane >> 3) & 3) * 4 + mh) * 4 + (lane >> 5) * 2 + (lane & 1);
  int moff = (lane & 8) ? WS_END : blk_at<512>(R_R1, (x8 * 4 + mh) * 32 + i32) + (a * 8 + ((lane >> 5) * 2 + (lane & 1)) * 2 + ((lane >> 4) & 1)) * 16;
  asm volatile("" : "+v"(moff), "+v"(po[0]), "+v"(po[1]), "+v"(on_w), "+v"(on_r), "+v"(soff), "+v"(mj[0]), "+v"(mj[1]), "+v"(mj[2]), "+v"(mj[3]));
  LDS_BARRIER();
  ST(6);
  {
    v2f t[8];
#pragma unroll
    for (int ww = 0; ww < 8; ++ww) t[ww] = v2f{mp[ww * 512], mp[ww * 512 + 256]};
    const v2f s = (fw[0] * t[0] + fw[1] * t[1] + fw[2] * t[2] + fw[3] * t[3]) + (fw[4] * t[4] + fw[5] * t[5] + fw[6] * t[6] + fw[7] * t[7]);
    const unsigned p = pk(s.x, s.y);
    st16(rec(p, dppu<ROR8>(p), tag), rws, moff, 0);
    if (gpub) st16(rec(asu(gpart.x), asu(gpart.y), tag), rws, lane * 16, blk_at<BLK>(R_GP, rb * 8 + c2));
    SB();
#pragma unroll
    for (int i = WOPRE; i < 8; ++i) wO[i] = ld8<AUX_NT>(ro, wvo + (i & 3) * 1024, wso + (i & 4) * 1024);
  }
  SB();
  ST(7);
  const unsigned t_pub = (unsigned)__builtin_amdgcn_s_memrealtime();
  float wt0 = 0.f, wt1 = 0.f;
  if (a < 2) {
    await<1, 6>(lr, lo, rws, tag);
    const float ls = asf(lr[0].x);
    float M = fmaxf(ls, dppf<XOR1>(ls));
    M = fmaxf(M, dppf<ROR8>(M)); M = sw32_max(sw16_max(M));
    const float e = exp2f_(ls - M), ex = dppf<XOR1>(e);
    float z = e + ex;
    z += dppf<ROR8>(z); z = sw16_add(z, z); z = sw32_add(z, z);
    const float iz = rcpf_(z);
    wt0 = ((lane & 1) ? ex : e) * iz; wt1 = ((lane & 1) ? e : ex) * iz;
    asm volatile("" : "+v"(wt0), "+v"(wt1));
    while ((unsigned)__builtin_amdgcn_s_memrealtime() - t_pub < DELTA) {}
  }
  asm volatile("" :: "v"(lr[0]));
  SB();
  request<2>(pr, po, mk(wsp, a < 2 ? WS_END : 0));
  SB();
  v4u wf[4];
#pragma unroll
  for (int j = 0; j < 4; ++j)
#pragma unroll
    for (int t = 0; t < 4; ++t) wf[j][t] = (j & 1) ? perm_hi(wO[2 * t][j >> 1], wO[2 * t + 1][j >> 1]) : perm_lo(wO[2 * t][j >> 1], wO[2 * t + 1][j >> 1]);
  asm volatile("" : "+v"(wf[0]), "+v"(wf[1]), "+v"(wf[2]), "+v"(wf[3]));
  ST(8);
  if (a < 2) {
    CNT(14, (await<2, GSLEEP>(pr, po, rws, tag)));
    ST(9);
    const float o0 = fmaf(wt0, lo16(pr[0].x), wt1 * lo16(pr[1].x)), o1 = fmaf(wt0, hi16(pr[0].x), wt1 * hi16(pr[1].x)),
                o2 = fmaf(wt0, lo16(pr[0].z), wt1 * lo16(pr[1].z)), o3 = fmaf(wt0, hi16(pr[0].z), wt1 * hi16(pr[1].z));
    float oo = sw16_add(sw32_add(o0, o2), sw32_add(o1, o3));
    oo += dppf<ROR8>(oo);
    const auto t = __builtin_amdgcn_permlane16_swap(asu(oo), asu(oo), false, false);
    const unsigned ob = pk(asf(t[0]), asf(t[1]));
    *(LAS unsigned*)((LAS char*)lds + on_w) = ob == 0xffffffffu ? 0x7fc07fc0u : ob;
  }
  asm volatile("" :: "v"(pr[0]), "v"(pr[1]));
  {
    v4u in;
#pragma nounroll
    for (unsigned spin = 0; spin < (SPIN_CAP << 6); ++spin) {
      asm volatile("" ::: "memory");
      in = *(const LAS v4u*)((LAS char*)lds + on_r);
      if (__all(max(max(in[0], in[1]), max(in[2], in[3])) != 0xffffffffu)) break;
    }
    v4u bj[4];
#pragma unroll
    for (int j = 0; j < 4; ++j) bj[j] = v4u{in[0] & mj[j], in[1] & mj[j], in[2] & mj[j], in[3] & mj[j]};
    SB();
    v4f sp = {0.f, 0.f, 0.f, 0.f};
#pragma unroll
    for (int j = 0; j < 4; ++j) sp = mfma32(wf[j], bj[j], sp);
    st16(rec(pk(sp[0], sp[1]), pk(sp[2], sp[3]), tag), rws, soff, 0);
    lds_arrive(cnt + C_SP, lane);
    asm volatile(".Lk_fused_end1:");
  }
  ST(10);
  ST_FLUSH();
}
}

extern "C" void kernel_launch(void* const* d_in, const int* in_sizes, int n_in, void* d_out, int out_size, void* d_ws, size_t ws_size,
                              hipStream_t stream) {
  fused::k_fused<<<256, 768, 0, stream>>>(d_in[0], d_in[1], d_in[2], d_in[3], d_in[4], d_in[6], d_in[7], d_in[8], d_in[9], d_out, d_ws);
}
```

```cpp
#define EARLY 0
#define GAIN 80
#define PROBE 1
#include <hip/hip_runtime.h>
#include <hip/hip_bf16.h>
#include <stdint.h>

#define KFRONT 2
#define WIN 4
#define BURST 3
#define WIN2 64
#define QPOS 24
#define QWAIT 500
#define GSLEEP 0
#define DELTA 40
#define WOPOS 0
#define FINAL_AT 2u
#define FSLEEP 0
#define ONSLEEP 0
#define HWAIT 600
#define ROT 0
#define HEARLY 1
#define WINY 64
#define PRIO 1
#define TAU 335
#define TBASE 30
#define MID 0
#define FDELTA 35
#define PV0 28
#define TLAST 1080
#define TBIG 100
#define WGBASE 1
#define WOD0 45
#define WOD1 75
#define PSLEEP 0
#define GDELTA 22
#define WINB 5
#define SPREAD 1
#define TOUCH 0
#define WOPRE 8
#define GLOBAL 1
#define TWAIT 300
#define NSH 13
#define NSH0 4
#define CODE_PF 1
#define ABL 0

namespace fused {
typedef unsigned v16u __attribute__((ext_vector_type(16)));
typedef unsigned v4u __attribute__((ext_vector_type(4)));
typedef unsigned v2u __attribute__((ext_vector_type(2)));
typedef float v4f __attribute__((ext_vector_type(4)));
typedef float v2f __attribute__((ext_vector_type(2)));
typedef short s16x4 __attribute__((ext_vector_type(4)));
typedef __bf16 bf16x2 __attribute__((ext_vector_type(2)));
typedef __bf16 bf16x8 __attribute__((ext_vector_type(8)));
typedef __amdgpu_buffer_rsrc_t rsrc_t;
typedef unsigned long long u64;
#define LAS __attribute__((address_space(3)))

constexpr int D = 512, HQ = 32, HKV = 8, SK = 8192, DH = 128, F = 1792;
constexpr int AUX_NT = 2, AUX_SC1 = 16;
constexpr int KV_BYTES = 2 * HKV * SK * DH * 2;
constexpr int BLK = 1024;
constexpr int WS_E = 0, WS_T0 = 128, R_QP = 256, R_HP = R_QP + 8 * 512, R_GP = R_HP + 4 * 512, R_LS = R_GP + 7 * BLK, R_R1 = R_LS + 16 * 128, R_SP = R_R1 + 64 * 512,
              WS_END = SPREAD ? 16 << 17 : 16 << 17;
template <int S> __device__ __forceinline__ int blk_at(int region, int j) {
  return SPREAD ? (((j + (j >> 4)) & 15) << 17) + region + (j >> 4) * S : region * 16 + j * S; }
constexpr unsigned SPIN_CAP = 1u << 18;
constexpr float LOG2E = 1.4426950408889634f;
constexpr float SC2 = 0.08838834764831845f * LOG2E;
constexpr int PW = 192;
constexpr int L_O = 0, L_X = 4096, L_R1 = L_X + 2048, L_R2 = L_R1 + 4 * PW, L_Q = L_R2 + 4 * PW, L_ML = L_Q + 256, L_ON = L_ML + 64, L_CNT = L_ON + 32, L_T0 = L_CNT + 6, L_TAG = L_CNT + 8, L_PRB = L_CNT + 12, L_END = L_CNT + 16;
enum { C_AUX = 0, C_SP = 1, C_Q = 2, C_ML = 3, C_ISSUED = 4, C_G = 5 };

__device__ __forceinline__ rsrc_t mk(const void* p, int bytes) { return __builtin_amdgcn_make_buffer_rsrc((void*)p, 0, bytes, 0x00020000); }
template <int AUX> __device__ __forceinline__ v4u ld16(rsrc_t r, int voff, int soff) { return __builtin_amdgcn_raw_buffer_load_b128(r, voff, soff, AUX); }
template <int AUX> __device__ __forceinline__ v2u ld8(rsrc_t r, int voff, int soff) { return __builtin_amdgcn_raw_buffer_load_b64(r, voff, soff, AUX); }
__device__ __forceinline__ unsigned ld4(rsrc_t r, int voff, int soff) { return __builtin_amdgcn_raw_buffer_load_b32(r, voff, soff, 0); }
__device__ __forceinline__ void st16(v4u v, rsrc_t r, int voff, int soff) { __builtin_amdgcn_raw_buffer_store_b128(v, r, voff, soff, AUX_SC1); }
__device__ __forceinline__ float asf(unsigned u) { return __builtin_bit_cast(float, u); }
__device__ __forceinline__ unsigned asu(float f) { return __builtin_bit_cast(unsigned, f); }
__device__ __forceinline__ unsigned uni(unsigned u) { return (unsigned)__builtin_amdgcn_readfirstlane((int)u); }
__device__ __forceinline__ float lo16(unsigned u) { return asf(u << 16); }
__device__ __forceinline__ float hi16(unsigned u) { return asf(u & 0xffff0000u); }
__device__ __forceinline__ unsigned pk(float a, float b) { return __builtin_bit_cast(unsigned, bf16x2{(__bf16)a, (__bf16)b}); }
__device__ __forceinline__ float rbf(float a) { return (float)(__bf16)a; }
template <int C> __device__ __forceinline__ unsigned dppu(unsigned x) { return (unsigned)__builtin_amdgcn_mov_dpp((int)x, C, 0xf, 0xf, true); }
template <int C> __device__ __forceinline__ float dppf(float x) { return asf(dppu<C>(asu(x))); }
template <int I> __device__ __forceinline__ float to_bank(float old, float src) {
  return asf((unsigned)__builtin_amdgcn_update_dpp((int)asu(old), (int)asu(src), 0x110 + 4 * I, 0xf, 1 << I, false)); }
constexpr int XOR1 = 0xB1, XOR2 = 0x4E, HMIR = 0x141, SHL4 = 0x104, SHL8 = 0x108, ROR4 = 0x124, ROR8 = 0x128, ROR12 = 0x12C;
__device__ __forceinline__ float sw32_add(float a, float b) {
  auto t = __builtin_amdgcn_permlane32_swap(asu(a), asu(b), false, false); return asf(t[0]) + asf(t[1]); }
__device__ __forceinline__ float sw16_add(float a, float b) {
  auto t = __builtin_amdgcn_permlane16_swap(asu(a), asu(b), false, false); return asf(t[0]) + asf(t[1]); }
__device__ __forceinline__ float sw32_max(float a) {
  auto t = __builtin_amdgcn_permlane32_swap(asu(a), asu(a), false, false); return fmaxf(asf(t[0]), asf(t[1])); }
__device__ __forceinline__ float sw16_max(float a) {
  auto t = __builtin_amdgcn_permlane16_swap(asu(a), asu(a), false, false); return fmaxf(asf(t[0]), asf(t[1])); }
__device__ __forceinline__ v4f mfma32(v4u a, v4u b, v4f c) {
  return __builtin_amdgcn_mfma_f32_16x16x32_bf16(__builtin_bit_cast(bf16x8, a), __builtin_bit_cast(bf16x8, b), c, 0, 0, 0); }
__device__ __forceinline__ v4f mfma16(v2u a, v2u b, v4f c) {
  return __builtin_amdgcn_mfma_f32_16x16x16bf16_1k(__builtin_bit_cast(s16x4, a), __builtin_bit_cast(s16x4, b), c, 0, 0, 0); }
__device__ __forceinline__ unsigned perm_lo(unsigned even, unsigned odd) { return __builtin_amdgcn_perm(odd, even, 0x05040100u); }
__device__ __forceinline__ unsigned perm_hi(unsigned even, unsigned odd) { return __builtin_amdgcn_perm(odd, even, 0x07060302u); }
__device__ __forceinline__ float exp2f_(float x) { return __builtin_amdgcn_exp2f(x); }
__device__ __forceinline__ float rcpf_(float x) { return __builtin_amdgcn_rcpf(x); }
#define SB() __builtin_amdgcn_sched_barrier(0)
#define VMWAIT(n) __builtin_amdgcn_s_waitcnt(0x0F70 | ((n) & 15) | (((n) >> 4) << 14))
#define LDS_BARRIER() do { SB(); asm volatile("s_waitcnt lgkmcnt(0)\n\ts_barrier" ::: "memory"); SB(); } while (0)

struct Tag { unsigned a, b; };
__device__ __forceinline__ bool fresh(v4u r, Tag t) { return (r.y == t.a) & (r.w == t.b); }
__device__ __forceinline__ v4u rec(unsigned p0, unsigned p1, Tag t) { return v4u{p0, t.a, p1, t.b}; }
template <int N> __device__ __forceinline__ bool all_fresh(const v4u (&r)[N], Tag t) {
  bool ok = true;
#pragma unroll
  for (int k = 0; k < N; ++k) ok = ok & fresh(r[k], t);
  return ok;
}
template <int N> __device__ __forceinline__ void request(v4u (&r)[N], const int (&off)[N], rsrc_t ws) {
#pragma unroll
  for (int k = 0; k < N; ++k) r[k] = ld16<AUX_SC1>(ws, off[k], 0);
}
template <int N, int SLEEP = 0> __device__ __forceinline__ unsigned await(v4u (&r)[N], const int (&off)[N], rsrc_t ws, Tag t) {
  bool ok = all_fresh<N>(r, t);
  unsigned spin = 0;
  if (__builtin_expect(!__all(ok), 0)) {
#pragma nounroll
    do {
      if (SLEEP) __builtin_amdgcn_s_sleep(SLEEP);
      asm volatile("" ::: "memory");
      if (!ok) request<N>(r, off, ws);
      ok = all_fresh<N>(r, t);
      ++spin;
    } while (!__all(ok) && spin < SPIN_CAP);
  }
  asm volatile("" ::: "memory");
  return spin;
}
__device__ __forceinline__ void lds_arrive(LAS unsigned* c, int lane) {
  asm volatile("s_waitcnt lgkmcnt(0)" ::: "memory");
  if (lane == 0) __hip_atomic_fetch_add(c, 1u, __ATOMIC_RELAXED, __HIP_MEMORY_SCOPE_WORKGROUP);
}
template <int SLEEP = 1> __device__ __forceinline__ void lds_wait(LAS unsigned* c, unsigned n) {
#pragma nounroll
  for (unsigned spin = 0; spin < (SPIN_CAP << 4); ++spin) {
    if (uni(__hip_atomic_load(c, __ATOMIC_RELAXED, __HIP_MEMORY_SCOPE_WORKGROUP)) >= n) break;
    if (SLEEP) __builtin_amdgcn_s_sleep(SLEEP);
  }
  asm volatile("" ::: "memory");
}

__device__ __forceinline__ float half_sum(float a, float b) {
  float x = sw32_add(a, b);
  x += dppf<XOR1>(x); x += dppf<XOR2>(x); x += dppf<HMIR>(x); x += dppf<ROR8>(x); return sw16_add(x, x); }
struct Stat { float m0, r0, m1, r1; };
__device__ __forceinline__ Stat ln_stats(const float (&u)[8], const float (&v)[8]) {
  const float su = ((u[0] + u[1]) + (u[2] + u[3])) + ((u[4] + u[5]) + (u[6] + u[7]));
  const float sv = ((v[0] + v[1]) + (v[2] + v[3])) + ((v[4] + v[5]) + (v[6] + v[7]));
  const float mf = half_sum(su, sv) * (1.f / D);
  const float mu = asf(__builtin_amdgcn_readlane(asu(mf), 0)), mv = asf(__builtin_amdgcn_readlane(asu(mf), 32));
  float qu = 0.f, qv = 0.f;
#pragma unroll
  for (int j = 0; j < 8; ++j) { const float a = u[j] - mu, b = v[j] - mv; qu = fmaf(a, a, qu); qv = fmaf(b, b, qv); }
  const float var = rbf(half_sum(qu, qv) * (1.f / D));
  const float rs = rcpf_(rbf(__builtin_amdgcn_sqrtf(rbf(var + 1e-5f))));
  return Stat{rbf(mu), asf(__builtin_amdgcn_readlane(asu(rs), 0)), rbf(mv), asf(__builtin_amdgcn_readlane(asu(rs), 32))};
}
__device__ __forceinline__ float ln_apply(float x, float m, float rs) { return rbf(rbf(x - m) * rs); }

__device__ __forceinline__ v4f product32(const v2u (&w)[8], v4u in, const unsigned (&mj)[4]) {
  v4f acc = {0.f, 0.f, 0.f, 0.f};
#pragma unroll
  for (int j = 0; j < 4; ++j) {
    v4u a;
#pragma unroll
    for (int t = 0; t < 4; ++t) a[t] = (j & 1) ? perm_hi(w[2 * t][j >> 1], w[2 * t + 1][j >> 1]) : perm_lo(w[2 * t][j >> 1], w[2 * t + 1][j >> 1]);
    acc = mfma32(a, v4u{in[0] & mj[j], in[1] & mj[j], in[2] & mj[j], in[3] & mj[j]}, acc);
  }
  return acc;
}
__device__ __forceinline__ bool sum_products(v4f p, float* buf, int a, LAS unsigned* c, int lane, v2f& s) {
  if ((lane & 15) < 8) *(v4f*)(buf + a * PW + ((lane >> 2) & 1) * 96 + (lane & 3) * 24 + (lane >> 4) * 4) = p;
  asm volatile("s_waitcnt lgkmcnt(0)" ::: "memory");
  unsigned old = 0;
  if (lane == 0) old = __hip_atomic_fetch_add(c, 1u, __ATOMIC_RELAXED, __HIP_MEMORY_SCOPE_WORKGROUP);
  if (uni(old) != 3u) return false;
  asm volatile("" ::: "memory");
  const float* q = buf + (lane >> 5) * 96 + (lane & 1) * 48 + ((lane & 31) >> 1);
  s = (v2f{q[0], q[24]} + v2f{q[PW], q[PW + 24]}) + (v2f{q[2 * PW], q[2 * PW + 24]} + v2f{q[3 * PW], q[3 * PW + 24]});
  return true;
}

#define ST(i)
#define CNT(i, v) (void)(v)
#define ST_FLUSH()

__global__ void __launch_bounds__(768, 3)
k_fused(const void*, const void*, const void*, const void*, const void*, const void*, const void*, const void*, const void*, void*, void*) {
  v16u ka; v4u kb; v2u kc2; u64 t_in;
  asm volatile("s_memrealtime %3\n\ts_load_dwordx16 %0, %4, 0x0\n\ts_load_dwordx4 %1, %4, 0x40\n\ts_load_dwordx2 %2, %4, 0x50"
               : "=&s"(ka), "=&s"(kb), "=&s"(kc2), "=&s"(t_in) : "s"(__builtin_amdgcn_kernarg_segment_ptr()));
  SB();
  const int tid = threadIdx.x, lane = tid & 63, wv = uni(tid >> 6);
  const int blk = blockIdx.x, x8 = blk & 7, i32 = blk >> 3, rg = i32 >> 3, k8 = i32 & 7, bat = i32 >> 4;
  const int g = lane >> 4, c16 = lane & 15, hg = x8 * 4 + rg, w = wv - 4;
  int kvbase = (bat * HKV + x8) * SK * 256 + (ROT ? (i32 & 15) * 8192 : (i32 & 15) * 131072) + w * 1024;
#define UNTIL(t) do { while ((int)((unsigned)__builtin_amdgcn_s_memrealtime() - (unsigned)t_in) < (int)(t)) { if (PSLEEP) __builtin_amdgcn_s_sleep(1); } SB(); } while (0)
#define PACE(n) do { UNTIL((base + (n) * tau) >> 4); if (WINB < 16) VMWAIT(WINB - 1); if (WIN < WINB && !big) VMWAIT(WIN - 1); SB(); } while (0)
#define NOW() ((int)((unsigned)__builtin_amdgcn_s_memrealtime() - (unsigned)t_in))
#define PIECE(n) (kvbase + (ROT ? ((((n) + i32) & 15) << 17) : (n) * 8192))
  int koff = ((lane >> 2) & 3) * 256 + g * 64 + (lane & 3) * 16;
  asm volatile("s_waitcnt lgkmcnt(0)" : "+s"(ka), "+s"(kb), "+s"(kc2), "+s"(t_in), "+s"(kvbase), "+v"(koff));
#define ARG(v, i) ((void*)(((u64)uni((v)[2 * (i) + 1]) << 32) | uni((v)[2 * (i)])))
  const void *x = ARG(ka, 0), *kc = ARG(ka, 1), *vc = ARG(ka, 2), *wq = ARG(ka, 3), *wo = ARG(ka, 4), *w1 = ARG(ka, 5), *b1 = ARG(ka, 6), *w2 = ARG(ka, 7),
             *b2 = ARG(kb, 0);
  void *out = ARG(kb, 1), *wsp = ARG(kc2, 0);
  __shared__ __attribute__((aligned(16))) float lds[L_END];
  LAS unsigned* cnt = (LAS unsigned*)(lds + L_CNT);
  u64 pc;
  int code_left, code_left1;
  asm volatile("s_getpc_b64 %0\n.Lk_fused_pc:\n\ts_mov_b32 %1, .Lk_fused_end0-.Lk_fused_pc\n\ts_mov_b32 %2, .Lk_fused_end1-.Lk_fused_pc"
               : "=s"(pc), "=s"(code_left), "=s"(code_left1));
  code_left = max(code_left, code_left1);
  const int id2 = x8 * 14 + i32 - 18, rb = id2 >> 3, c2 = id2 & 7;
  const bool isq = i32 < 16, is1 = i32 >= 16 && i32 < 23, is2 = i32 >= 18;
  const bool big = isq || is1;
  const rsrc_t rk = mk(kc, KV_BYTES);
  v4u K[16];
  int wv2 = wv;
  asm volatile("" : "+s"(wv2));
  if (wv2 >= 4 && !big) {
#pragma unroll
    for (int n = 0; n < KFRONT; ++n) { K[n] = ld16<AUX_NT>(rk, koff, PIECE(n)); SB(); }
  } else {
#pragma unroll
    for (int n = 0; n < KFRONT; ++n) asm volatile("" : "=v"(K[n]));
    if (tid < 6) cnt[tid] = 0u;
    if (tid == 6) cnt[L_T0 - L_CNT] = (unsigned)t_in;
    if (tid == 7) cnt[L_TAG + 2 - L_CNT] = 0u;
  }
  LDS_BARRIER();
  if (WGBASE) t_in = uni(cnt[L_T0 - L_CNT]);

  const int lb = (lane >> 2) & 1;

  if (wv >= 4) {
    const rsrc_t rv = mk(vc, KV_BYTES);
    const int voff = lane * 16;
    v4u V[16];
    int probe = 0;
    int tau = big ? (TLAST - TBIG) * 16 / (32 - KFRONT) : (TLAST - TBASE) * 16 / (32 - KFRONT), base = TLAST * 16 - 31 * tau;
#define REPLAN(n, sh) do { const int end_ = (TLAST + (sh)) * 16; tau = (end_ - base - ((n) - 1) * tau) / (32 - (n)); base = end_ - 31 * tau; } while (0)
    if (big) {
      UNTIL(TBIG);
#pragma unroll
      for (int n = 0; n < KFRONT; ++n) { K[n] = ld16<AUX_NT>(rk, koff, PIECE(n)); SB(); }
    }
#pragma unroll
    for (int n = KFRONT; n < QPOS; ++n) {
      if (PROBE == 1 && n == KFRONT) { asm volatile("" :: "v"(K[0])); SB(); probe = NOW() - (big ? TBIG : 0); SB(); }
      if (GLOBAL && (n == NSH || (EARLY && n == NSH0))) REPLAN(n, (int)uni(__hip_atomic_load(cnt + (L_TAG + 2 - L_CNT), __ATOMIC_RELAXED, __HIP_MEMORY_SCOPE_WORKGROUP)));
      if (n < QPOS - BURST) PACE(n);
      if (n < 16) K[n] = ld16<AUX_NT>(rk, koff, PIECE(n));
      else V[n - 16] = ld16<AUX_NT>(rv, voff, PIECE(n - 16));
      SB();
    }
    ST(2);
    if (PROBE == 2) { SB(); probe = NOW(); SB(); }
    if (!(ABL & 8)) lds_wait(cnt + C_Q, 3u);
    ST(3);
    if (PROBE == 3) { SB(); probe = NOW(); SB(); }
    const int bank = (lane >> 2) & 3, hh = lane & 3;
    const v4u qf = *(const v4u*)(lds + L_Q + hh * 64 + g * 16 + bank * 4);
    const v4u lt = *(const v4u*)(lds + L_TAG);
    v4f d[16];
#pragma unroll
    for (int n = 0; n < 16; ++n) d[n] = mfma32(K[n], qf, v4f{0.f, 0.f, 0.f, 0.f});
    SB();
    if (PROBE == 4) { SB(); probe = NOW(); SB(); }
    ST(1);
    float S[4], sn[16];
#pragma unroll
    for (int n = 0; n < 16; ++n) { d[n][0] += dppf<SHL4>(d[n][1]); d[n][2] += dppf<SHL4>(d[n][3]); }
    SB();
#pragma unroll
    for (int n = 0; n < 16; ++n) sn[n] = d[n][0] + dppf<SHL8>(d[n][2]);
    SB();
#pragma unroll
    for (int z = 0; z < 4; ++z) S[z] = to_bank<1>(sn[4 * z], sn[4 * z + 1]);
#pragma unroll
    for (int z = 0; z < 4; ++z) S[z] = to_bank<2>(S[z], sn[4 * z + 2]);
#pragma unroll
    for (int z = 0; z < 4; ++z) S[z] = to_bank<3>(S[z], sn[4 * z + 3]);
    SB();
    ST(7);
#pragma unroll
    for (int n = QPOS; n < QPOS + MID; ++n) { PACE(n); ST(9); V[n - 16] = ld16<AUX_NT>(rv, voff, PIECE(n - 16)); SB(); }
    ST(10);
    float mw = fmaxf(fmaxf(S[0], S[1]), fmaxf(S[2], S[3]));
    mw = fmaxf(mw, dppf<ROR4>(mw)); mw = fmaxf(mw, dppf<ROR8>(mw)); mw = sw32_max(sw16_max(mw));
    mw *= SC2;
    float lw = 0.f;
#pragma unroll
    for (int z = 0; z < 4; ++z) { S[z] = exp2f_(fmaf(S[z], SC2, -mw)); lw += S[z]; }
    lw += dppf<ROR4>(lw); lw += dppf<ROR8>(lw); lw = sw16_add(lw, lw); lw = sw32_add(lw, lw);
    if (lane < 4) *(v2f*)(lds + L_ML + lane * 16 + w * 2) = v2f{mw, lw};
    lds_arrive(cnt + C_ML, lane);
    ST(4);
    if (GLOBAL) REPLAN(QPOS + MID, (int)uni(lt.z));
    unsigned P[4], bm[4];
#pragma unroll
    for (int z = 0; z < 4; ++z) { P[z] = pk(S[z], dppf<SHL4>(S[z])); bm[z] = bank == z ? 0xffffffffu : 0u; }
    v4u Bf[2][4];
#pragma unroll
    for (int be = 0; be < 2; ++be) {
      const unsigned p0 = P[2 * be], p1 = P[2 * be + 1];
      Bf[be][0] = v4u{p0 & bm[0], dppu<ROR8>(p0) & bm[0], p1 & bm[0], dppu<ROR8>(p1) & bm[0]};
      Bf[be][1] = v4u{dppu<ROR4>(p0) & bm[1], dppu<ROR12>(p0) & bm[1], dppu<ROR4>(p1) & bm[1], dppu<ROR12>(p1) & bm[1]};
      Bf[be][2] = v4u{dppu<ROR8>(p0) & bm[2], p0 & bm[2], dppu<ROR8>(p1) & bm[2], p1 & bm[2]};
      Bf[be][3] = v4u{dppu<ROR12>(p0) & bm[3], dppu<ROR4>(p0) & bm[3], dppu<ROR12>(p1) & bm[3], dppu<ROR4>(p1) & bm[3]};
    }
    SB();
    v4f acc[2] = {v4f{0.f, 0.f, 0.f, 0.f}, v4f{0.f, 0.f, 0.f, 0.f}};
#pragma unroll
    for (int be = 0; be < 2; ++be) {
#pragma unroll
      for (int n = be ? PV0 : QPOS + MID; n < (be ? 32 : PV0); ++n) { PACE(n); V[n - 16] = ld16<AUX_NT>(rv, voff, PIECE(n - 16)); SB(); }
      if (be) { lds_arrive(cnt + C_ISSUED, lane); ST(5); if (PROBE == 5) { SB(); probe = NOW(); SB(); } }
      v4u A[8];
#pragma unroll
      for (int t = 0; t < 4; ++t) {
#pragma unroll
        for (int u = 0; u < 4; ++u) {
          A[2 * u][t] = perm_lo(V[8 * be + 2 * t][u], V[8 * be + 2 * t + 1][u]);
          A[2 * u + 1][t] = perm_hi(V[8 * be + 2 * t][u], V[8 * be + 2 * t + 1][u]);
        }
        SB();
      }
#pragma unroll
      for (int u = 0; u < 4; ++u) { acc[0] = mfma32(A[2 * u], Bf[be][u], acc[0]); acc[1] = mfma32(A[2 * u + 1], Bf[be][u], acc[1]); }
      SB();
    }
    *(v4f*)(lds + L_O + w * 512 + lane * 4) = acc[0];
    *(v4f*)(lds + L_O + w * 512 + 256 + lane * 4) = acc[1];
    LDS_BARRIER();
    if (PROBE == 6) { SB(); probe = NOW(); SB(); }
    ST(6);
    const Tag tag = {uni(lt.x), uni(lt.y)};
    const rsrc_t rws = mk(wsp, WS_END);
    ST(8);
    if (w == 7) {
      const int gq = (i32 >> 2) & 3, jq = i32 & 3;
      const int fcol = x8 * 64 + gq * 16 + g * 4 + jq;
      const unsigned xo = __builtin_amdgcn_raw_buffer_load_b16(mk(x, 2 * D * 2), fcol * 2, bat * D * 2, 0);
      const unsigned bias2 = __builtin_amdgcn_raw_buffer_load_b16(mk(b2, D * 2), fcol * 2, 0, 0);
      SB();
      v4u fg[1], fs[2];
      const int go[1] = {blk_at<BLK>(R_GP, (c16 < 14 ? c16 : 0) * 8 + x8) + (bat * 32 + gq * 8 + g * 2 + (jq >> 1)) * 16};
      int so[2] = {blk_at<512>(R_SP, lane * 8 + x8) + (bat * 16 + gq * 4 + jq) * 16, blk_at<512>(R_SP, (lane + 64) * 8 + x8) + (bat * 16 + gq * 4 + jq) * 16};
      __builtin_amdgcn_s_sleep(GDELTA);
      request<1>(fg, go, rws);
      await<1, 6>(fg, go, rws, tag);
      float gs = c16 < 14 ? asf((jq & 1) ? fg[0].z : fg[0].x) : 0.f;
      gs += dppf<XOR1>(gs); gs += dppf<XOR2>(gs); gs += dppf<HMIR>(gs); gs += dppf<ROR8>(gs);
      const float mv = rbf(rbf(gs) + lo16(bias2));
      float gv = rbf(mv * rcpf_(1.f + exp2f_(-LOG2E * mv))), xv = lo16(xo);
      int ooff = c16 == 0 ? (bat * D + fcol) * 2 : 2 * D * 2;
      asm volatile("" : "+v"(gv), "+v"(xv), "+v"(ooff), "+v"(so[0]), "+v"(so[1]));
      lds_wait<0>(cnt + C_SP, 4u);
      if (PROBE == 7) { SB(); probe = NOW(); SB(); }
      { const unsigned t_sp = (unsigned)__builtin_amdgcn_s_memrealtime();
        while ((unsigned)__builtin_amdgcn_s_memrealtime() - t_sp < FDELTA) {} }
      request<2>(fs, so, rws);
      CNT(15, (await<2, FSLEEP>(fs, so, rws, tag)));
      ST(11);
      float sc = sw16_add(sw32_add(lo16(fs[0].x) + lo16(fs[1].x), lo16(fs[0].z) + lo16(fs[1].z)), sw32_add(hi16(fs[0].x) + hi16(fs[1].x), hi16(fs[0].z) + hi16(fs[1].z)));
      sc += dppf<XOR1>(sc); sc += dppf<XOR2>(sc); sc += dppf<HMIR>(sc); sc += dppf<ROR8>(sc);
      const float ov = xv + rbf(rbf(sc) * gv);
      __builtin_amdgcn_raw_buffer_store_b16((unsigned short)pk(ov, ov), mk(out, 2 * D * 2), ooff, 0, 0);
      if (blk == 0 && lane == 0) __builtin_amdgcn_raw_buffer_store_b64(v2u{tag.a, tag.b}, rws, 0, WS_E, AUX_SC1);
      { if (PROBE == 8) probe = NOW();
        const int mn = (int)uni(cnt[L_PRB - L_CNT]);
        const int own = PROBE == 1;
        UNTIL(min(3000 + mn + (own ? probe : probe - mn), 8000)); }
      asm volatile(".Lk_fused_end0:");
      ST(12);
    }
    ST_FLUSH();
    return;
  }

  const int a = wv;
  if (PRIO) __builtin_amdgcn_s_setprio(2);
  const rsrc_t rws = mk(wsp, WS_END);
  unsigned mj[4];
#pragma unroll
  for (int j = 0; j < 4; ++j) mj[j] = (lane & 11) == j ? 0xffffffffu : 0u;
  const v2u ep = ld8<AUX_SC1>(rws, 0, WS_E);
  const v4u te = ld16<AUX_SC1>(mk(wsp, EARLY && a == 2 ? WS_END : 0), (lane & 7) * 16, WS_T0);
  unsigned code_pf;
  Tag tag;
  v2f gpart = {0.f, 0.f};
  bool gpub = false;
  if (big) {
    const rsrc_t rx = mk(x, 2 * D * 2);
    const v4u xa0 = ld16<0>(rx, lane * 16, 0), xa1 = ld16<0>(rx, lane * 16, D * 2);
    const int ld = isq ? HQ * DH * 2 : F * 2, idb = isq ? x8 * 16 + (i32 & 7) * 2 + (i32 >> 3) : x8 * 7 + i32 - 16, cg = idb >> 1, row0 = (idb & 1) * 256 + a * 64;
    const rsrc_t rw = mk(isq ? wq : w1, (ABL & (isq ? 32 : 4)) ? 0 : D * ld);
    v2u wA[2][8];
    const int woff = g * 8 * ld + c16 * 8;
#pragma unroll
    for (int t = 0; t < 2; ++t)
#pragma unroll
      for (int i = 0; i < 8; ++i) wA[t][i] = ld8<AUX_NT>(rw, woff, (row0 + t * 32 + i) * ld + cg * 128);
    SB();
    tag = Tag{uni(ep.x) + 1u, uni(ep.y) + 0x9E3779B9u};
    if (GLOBAL && blk < 8 && tid == 0) st16(rec((unsigned)t_in, (unsigned)t_in, tag), rws, blk * 16, WS_T0);
    if (EARLY && a == 2) {
      int s0 = fresh(te, tag) ? max((int)(te.x - (unsigned)t_in), -300) : 0;
      s0 += (int)dppu<XOR1>(s0); s0 += (int)dppu<XOR2>(s0); s0 += (int)dppu<HMIR>(s0);
      if (lane == 0) cnt[L_TAG + 2 - L_CNT] = (unsigned)(s0 >> 3);
    }
    float u[8], v[8];
#pragma unroll
    for (int t = 0; t < 4; ++t) { u[2 * t] = lo16(xa0[t]); u[2 * t + 1] = hi16(xa0[t]); v[2 * t] = lo16(xa1[t]); v[2 * t + 1] = hi16(xa1[t]); }
#pragma nounroll
    for (int pass = isq ? 1 : 0; pass < 2; ++pass) {
      const Stat st = ln_stats(u, v);
#pragma unroll
      for (int j = 0; j < 8; ++j) { u[j] = ln_apply(u[j], st.m0, st.r0); v[j] = ln_apply(v[j], st.m1, st.r1); }
    }
    float* xs = lds + L_X + a * 512;
    *(v4u*)(xs + lane * 4) = v4u{pk(u[0], u[1]), pk(u[2], u[3]), pk(u[4], u[5]), pk(u[6], u[7])};
    *(v4u*)(xs + 256 + lane * 4) = v4u{pk(v[0], v[1]), pk(v[2], v[3]), pk(v[4], v[5]), pk(v[6], v[7])};
    asm volatile("s_waitcnt lgkmcnt(0)" ::: "memory");
    v4f acc[4];
#pragma unroll
    for (int t = 0; t < 2; ++t) {
      const v4u in = *(const v4u*)(xs + lb * 256 + (row0 >> 1) + (t * 4 + g) * 4);
#pragma unroll
      for (int j = 0; j < 4; ++j) {
        v4u f;
#pragma unroll
        for (int k = 0; k < 4; ++k) f[k] = (j & 1) ? perm_hi(wA[t][2 * k][j >> 1], wA[t][2 * k + 1][j >> 1]) : perm_lo(wA[t][2 * k][j >> 1], wA[t][2 * k + 1][j >> 1]);
        acc[j] = mfma32(f, in, t ? acc[j] : v4f{0.f, 0.f, 0.f, 0.f});
      }
    }
    const v4f p = (lane & 2) ? ((lane & 1) ? acc[3] : acc[2]) : ((lane & 1) ? acc[1] : acc[0]);
    v2f sum;
    if (sum_products(p, lds + L_R1, a, cnt + C_AUX, lane, sum)) {
      const unsigned pp = pk(sum.x, sum.y);
      st16(rec(pp, dppu<XOR1>(pp), tag), rws, (lane & 1) ? WS_END : lane * 8, blk_at<512>(isq ? R_QP : R_HP, idb));
    }
  } else {
    tag = Tag{uni(ep.x) + 1u, uni(ep.y) + 0x9E3779B9u};
    if (EARLY && a == 2) {
      int s0 = fresh(te, tag) ? max((int)(te.x - (unsigned)t_in), -300) : 0;
      s0 += (int)dppu<XOR1>(s0); s0 += (int)dppu<XOR2>(s0); s0 += (int)dppu<HMIR>(s0);
      if (lane == 0) cnt[L_TAG + 2 - L_CNT] = (unsigned)(s0 >> 3);
    }
  }
  ST(1);
  code_pf = ld4(mk((const void*)(pc & ~(u64)127), code_left + (int)(pc & 127)), lane * 128, (a & 1) * 8192);
  if (a < 2) {
    v4u qr[2];
    const int qo[2] = {blk_at<512>(R_QP, (x8 * 8 + a * 4 + g) * 2) + (bat * 16 + c16) * 16, blk_at<512>(R_QP, (x8 * 8 + a * 4 + g) * 2 + 1) + (bat * 16 + c16) * 16};
    UNTIL(QWAIT);
    request<2>(qr, qo, rws);
    CNT(13, (await<2, 16>(qr, qo, rws, tag)));
    *(v2u*)(lds + L_Q + (a * 64 + lane) * 2) = v2u{pk(lo16(qr[0].x) + lo16(qr[1].x), hi16(qr[0].x) + hi16(qr[1].x)), pk(lo16(qr[0].z) + lo16(qr[1].z), hi16(qr[0].z) + hi16(qr[1].z))};
    lds_arrive(cnt + C_Q, lane);
  }
  if (a == 2) {
    int sh = 0, mx = 0;
    if (GLOBAL) {
      v4u tr[1];
      const int to[1] = {WS_T0 + (lane & 7) * 16};
      UNTIL(TWAIT);
      request<1>(tr, to, rws);
      await<1, 16>(tr, to, rws, tag);
      sh = mx = min(max((int)(tr[0].x - (unsigned)t_in), -300), 300);
      sh += (int)dppu<XOR1>(sh); sh += (int)dppu<XOR2>(sh); sh += (int)dppu<HMIR>(sh);
      mx = max(mx, (int)dppu<XOR1>(mx)); mx = max(mx, (int)dppu<XOR2>(mx)); mx = max(mx, (int)dppu<HMIR>(mx));
      sh = max(sh >> 3, mx - GAIN);
      int mn = (int)(tr[0].x - (unsigned)t_in);
      mn = min(mn, (int)dppu<XOR1>(mn)); mn = min(mn, (int)dppu<XOR2>(mn)); mn = min(mn, (int)dppu<HMIR>(mn));
      if (lane == 0) cnt[L_PRB - L_CNT] = (unsigned)mn;
    }
    if (lane == 0) *(v4u*)(lds + L_TAG) = v4u{tag.a, tag.b, (unsigned)sh, (unsigned)mx};
    lds_arrive(cnt + C_Q, lane);
  }
  ST(2);
  asm volatile("" :: "v"(code_pf));
  if (is2) {
    const rsrc_t r2 = mk(w2, (ABL & 2) ? 0 : D * F * 2);
    v2u wB[8];
    v4u hr[2];
    const int hoff = (lb * 16 + (a & 1) * 8 + g * 2 + ((lane >> 3) & 1)) * 16;
    const int ho[2] = {blk_at<512>(R_HP, (2 * rb + (a >> 1)) * 2) + hoff, blk_at<512>(R_HP, (2 * rb + (a >> 1)) * 2 + 1) + hoff};
    lds_wait(cnt + C_Q, 3u);
    UNTIL(HWAIT + (int)uni(((const unsigned*)lds)[L_TAG + 3]));
    request<2>(hr, ho, rws);
    const v2u bias1 = ld8<0>(mk(b1, F * 2), g * 16 + ((lane >> 3) & 1) * 8, (rb * 128 + a * 32) * 2);
#pragma unroll
    for (int i = 0; i < 8; ++i) wB[i] = ld8<AUX_NT>(r2, g * 8 * 1024 + c16 * 8 + (i & 3) * 1024, (rb * 128 + a * 32 + (i & 4)) * 1024 + c2 * 128);
    SB();
    await<2, 16>(hr, ho, rws, tag);
    float h[4] = {lo16(hr[0].x) + lo16(hr[1].x), hi16(hr[0].x) + hi16(hr[1].x), lo16(hr[0].z) + lo16(hr[1].z), hi16(hr[0].z) + hi16(hr[1].z)};
    const float bb[4] = {lo16(bias1.x), hi16(bias1.x), lo16(bias1.y), hi16(bias1.y)};
    constexpr float G0 = -2.f * 0.7978845608028654f * LOG2E, G1 = G0 * 0.044715f;
#pragma unroll
    for (int j = 0; j < 4; ++j) {
      const float t = rbf(rbf(h[j]) + bb[j]);
      h[j] = t * rcpf_(1.f + exp2f_(t * fmaf(G1, t * t, G0)));
    }
    const unsigned h0 = pk(h[0], h[1]), h1 = pk(h[2], h[3]);
    gpub = sum_products(product32(wB, v4u{h0, h1, dppu<ROR8>(h0), dppu<ROR8>(h1)}, mj), lds + L_R2, a, cnt + C_G, lane, gpart);
    ST(11);
  }
  lds_wait(cnt + C_ML, 8u);
  ST(4);
  if (a == 3 && lane < 32) ((unsigned*)lds)[L_ON + lane] = 0xffffffffu;
  const int mh = (lane >> 1) & 3;
  float fw[8], lse;
  {
    v4f ml[4];
#pragma unroll
    for (int t = 0; t < 4; ++t) ml[t] = *(const v4f*)(lds + L_ML + mh * 16 + 4 * t);
    const float M = fmaxf(fmaxf(fmaxf(ml[0][0], ml[0][2]), fmaxf(ml[1][0], ml[1][2])), fmaxf(fmaxf(ml[2][0], ml[2][2]), fmaxf(ml[3][0], ml[3][2])));
    float L = 0.f;
#pragma unroll
    for (int t = 0; t < 4; ++t) { fw[2 * t] = exp2f_(ml[t][0] - M); fw[2 * t + 1] = exp2f_(ml[t][2] - M); L = fmaf(fw[2 * t], ml[t][1], fmaf(fw[2 * t + 1], ml[t][3], L)); }
    const float il = rcpf_(L);
#pragma unroll
    for (int t = 0; t < 8; ++t) fw[t] *= il;
    lse = M + __builtin_amdgcn_logf(L);
    asm volatile("" : "+v"(fw[0]), "+v"(fw[1]), "+v"(fw[2]), "+v"(fw[3]), "+v"(fw[4]), "+v"(fw[5]), "+v"(fw[6]), "+v"(fw[7]));
    if (a == 0 && lane < 8) st16(rec(asu(lse), asu(lse), tag), rws, lane * 16, blk_at<128>(R_LS, x8 * 32 + i32));
  }
  const int dq = k8 >> 1, ch = k8 & 1;
  v2u wO[8];
  v4u lr[1], pr[2];
  const int lo[1] = {blk_at<128>(R_LS, x8 * 32 + (a & 1) * 16 + (lane >> 3) + (lane & 1) * 8) + rg * 32};
  int po[2] = {blk_at<512>(R_R1, hg * 32 + (a & 1) * 16 + (lane >> 3)) + dq * 128 + (lane & 7) * 16,
               blk_at<512>(R_R1, hg * 32 + (a & 1) * 16 + (lane >> 3) + 8) + dq * 128 + (lane & 7) * 16};
  int on_w = (L_ON + a * 16 + (lane & 7) * 2 + (lane >> 5)) * 4, on_r = (L_ON + lb * 16 + g * 4) * 4;
  int soff = c16 < 8 ? blk_at<512>(R_SP, (hg * 4 + dq) * 8 + ch * 4 + a) + (lb * 16 + g * 4 + (lane & 3)) * 16 : WS_END;
  const rsrc_t ro = mk(wo, (ABL & 2) ? 0 : D * HQ * DH * 2);
  const int wvo = g * 8 * 1024 + c16 * 8, wso = (hg * 128 + dq * 32) * 1024 + ch * 512 + a * 128;
  const int shift = GLOBAL ? (int)uni(((const unsigned*)lds)[L_TAG + 2]) : 0;
  lds_wait(cnt + C_ISSUED, 8u);
  UNTIL(TLAST + WOD0 + shift);
  request<1>(lr, lo, mk(wsp, a < 2 ? WS_END : 0));
#pragma unroll
  for (int i = 0; i < WOPRE; ++i) {
    if (i == 4) UNTIL(TLAST + WOD1 + shift);
    wO[i] = ld8<AUX_NT>(ro, wvo + (i & 3) * 1024, wso + (i & 4) * 1024);
    SB();
  }
  const float* mp = lds + L_O + (a * 16 + ((lane >> 3) & 3) * 4 + mh) * 4 + (lane >> 5) * 2 + (lane & 1);
  int moff = (lane & 8) ? WS_END : blk_at<512>(R_R1, (x8 * 4 + mh) * 32 + i32) + (a * 8 + ((lane >> 5) * 2 + (lane & 1)) * 2 + ((lane >> 4) & 1)) * 16;
  asm volatile("" : "+v"(moff), "+v"(po[0]), "+v"(po[1]), "+v"(on_w), "+v"(on_r), "+v"(soff), "+v"(mj[0]), "+v"(mj[1]), "+v"(mj[2]), "+v"(mj[3]));
  LDS_BARRIER();
  ST(6);
  {
    v2f t[8];
#pragma unroll
    for (int ww = 0; ww < 8; ++ww) t[ww] = v2f{mp[ww * 512], mp[ww * 512 + 256]};
    const v2f s = (fw[0] * t[0] + fw[1] * t[1] + fw[2] * t[2] + fw[3] * t[3]) + (fw[4] * t[4] + fw[5] * t[5] + fw[6] * t[6] + fw[7] * t[7]);
    const unsigned p = pk(s.x, s.y);
    st16(rec(p, dppu<ROR8>(p), tag), rws, moff, 0);
    if (gpub) st16(rec(asu(gpart.x), asu(gpart.y), tag), rws, lane * 16, blk_at<BLK>(R_GP, rb * 8 + c2));
    SB();
#pragma unroll
    for (int i = WOPRE; i < 8; ++i) wO[i] = ld8<AUX_NT>(ro, wvo + (i & 3) * 1024, wso + (i & 4) * 1024);
  }
  SB();
  ST(7);
  const unsigned t_pub = (unsigned)__builtin_amdgcn_s_memrealtime();
  float wt0 = 0.f, wt1 = 0.f;
  if (a < 2) {
    await<1, 6>(lr, lo, rws, tag);
    const float ls = asf(lr[0].x);
    float M = fmaxf(ls, dppf<XOR1>(ls));
    M = fmaxf(M, dppf<ROR8>(M)); M = sw32_max(sw16_max(M));
    const float e = exp2f_(ls - M), ex = dppf<XOR1>(e);
    float z = e + ex;
    z += dppf<ROR8>(z); z = sw16_add(z, z); z = sw32_add(z, z);
    const float iz = rcpf_(z);
    wt0 = ((lane & 1) ? ex : e) * iz; wt1 = ((lane & 1) ? e : ex) * iz;
    asm volatile("" : "+v"(wt0), "+v"(wt1));
    while ((unsigned)__builtin_amdgcn_s_memrealtime() - t_pub < DELTA) {}
  }
  asm volatile("" :: "v"(lr[0]));
  SB();
  request<2>(pr, po, mk(wsp, a < 2 ? WS_END : 0));
  SB();
  v4u wf[4];
#pragma unroll
  for (int j = 0; j < 4; ++j)
#pragma unroll
    for (int t = 0; t < 4; ++t) wf[j][t] = (j & 1) ? perm_hi(wO[2 * t][j >> 1], wO[2 * t + 1][j >> 1]) : perm_lo(wO[2 * t][j >> 1], wO[2 * t + 1][j >> 1]);
  asm volatile("" : "+v"(wf[0]), "+v"(wf[1]), "+v"(wf[2]), "+v"(wf[3]));
  ST(8);
  if (a < 2) {
    CNT(14, (await<2, GSLEEP>(pr, po, rws, tag)));
    ST(9);
    const float o0 = fmaf(wt0, lo16(pr[0].x), wt1 * lo16(pr[1].x)), o1 = fmaf(wt0, hi16(pr[0].x), wt1 * hi16(pr[1].x)),
                o2 = fmaf(wt0, lo16(pr[0].z), wt1 * lo16(pr[1].z)), o3 = fmaf(wt0, hi16(pr[0].z), wt1 * hi16(pr[1].z));
    float oo = sw16_add(sw32_add(o0, o2), sw32_add(o1, o3));
    oo += dppf<ROR8>(oo);
    const auto t = __builtin_amdgcn_permlane16_swap(asu(oo), asu(oo), false, false);
    const unsigned ob = pk(asf(t[0]), asf(t[1]));
    *(LAS unsigned*)((LAS char*)lds + on_w) = ob == 0xffffffffu ? 0x7fc07fc0u : ob;
  }
  asm volatile("" :: "v"(pr[0]), "v"(pr[1]));
  {
    v4u in;
#pragma nounroll
    for (unsigned spin = 0; spin < (SPIN_CAP << 6); ++spin) {
      asm volatile("" ::: "memory");
      in = *(const LAS v4u*)((LAS char*)lds + on_r);
      if (__all(max(max(in[0], in[1]), max(in[2], in[3])) != 0xffffffffu)) break;
    }
    v4u bj[4];
#pragma unroll
    for (int j = 0; j < 4; ++j) bj[j] = v4u{in[0] & mj[j], in[1] & mj[j], in[2] & mj[j], in[3] & mj[j]};
    SB();
    v4f sp = {0.f, 0.f, 0.f, 0.f};
#pragma unroll
    for (int j = 0; j < 4; ++j) sp = mfma32(wf[j], bj[j], sp);
    st16(rec(pk(sp[0], sp[1]), pk(sp[2], sp[3]), tag), rws, soff, 0);
    lds_arrive(cnt + C_SP, lane);
    asm volatile(".Lk_fused_end1:");
  }
  ST(10);
  ST_FLUSH();
}
}

extern "C" void kernel_launch(void* const* d_in, const int* in_sizes, int n_in, void* d_out, int out_size, void* d_ws, size_t ws_size,
                              hipStream_t stream) {
  fused::k_fused<<<256, 768, 0, stream>>>(d_in[0], d_in[1], d_in[2], d_in[3], d_in[4], d_in[6], d_in[7], d_in[8], d_in[9], d_out, d_ws);
}
```

```cpp
#define LGAIN 3
#define T1NOM 105
#define PROBE 1
#include <hip/hip_runtime.h>
#include <hip/hip_bf16.h>
#include <stdint.h>

#define KFRONT 2
#define WIN 4
#define BURST 3
#define WIN2 64
#define QPOS 24
#define QWAIT 500
#define GSLEEP 0
#define DELTA 40
#define WOPOS 0
#define FINAL_AT 2u
#define FSLEEP 0
#define ONSLEEP 0
#define HWAIT 600
#define ROT 0
#define HEARLY 1
#define WINY 64
#define PRIO 1
#define TAU 335
#define TBASE 30
#define MID 0
#define FDELTA 35
#define PV0 28
#define TLAST 1080
#define TBIG 100
#define WGBASE 1
#define WOD0 45
#define WOD1 75
#define PSLEEP 0
#define GDELTA 22
#define WINB 5
#define SPREAD 1
#define TOUCH 1
#define WOPRE 8
#define GLOBAL 1
#define TWAIT 300
#define NSH 13
#define NSH0 4
#define EARLY 0
#define GAIN 80
#define LATE 1
#define CODE_PF 1
#define ABL 0

namespace fused {
typedef unsigned v16u __attribute__((ext_vector_type(16)));
typedef unsigned v4u __attribute__((ext_vector_type(4)));
typedef unsigned v2u __attribute__((ext_vector_type(2)));
typedef float v4f __attribute__((ext_vector_type(4)));
typedef float v2f __attribute__((ext_vector_type(2)));
typedef short s16x4 __attribute__((ext_vector_type(4)));
typedef __bf16 bf16x2 __attribute__((ext_vector_type(2)));
typedef __bf16 bf16x8 __attribute__((ext_vector_type(8)));
typedef __amdgpu_buffer_rsrc_t rsrc_t;
typedef unsigned long long u64;
#define LAS __attribute__((address_space(3)))

constexpr int D = 512, HQ = 32, HKV = 8, SK = 8192, DH = 128, F = 1792;
constexpr int AUX_NT = 2, AUX_SC1 = 16;
constexpr int KV_BYTES = 2 * HKV * SK * DH * 2;
constexpr int BLK = 1024;
constexpr int WS_E = 0, WS_T0 = 128, R_QP = 256, R_HP = R_QP + 8 * 512, R_GP = R_HP + 4 * 512, R_LS = R_GP + 7 * BLK, R_R1 = R_LS + 16 * 128, R_SP = R_R1 + 64 * 512,
              WS_END = SPREAD ? 16 << 17 : 16 << 17;
template <int S> __device__ __forceinline__ int blk_at(int region, int j) {
  return SPREAD ? (((j + (j >> 4)) & 15) << 17) + region + (j >> 4) * S : region * 16 + j * S; }
constexpr unsigned SPIN_CAP = 1u << 18;
constexpr float LOG2E = 1.4426950408889634f;
constexpr float SC2 = 0.08838834764831845f * LOG2E;
constexpr int PW = 192;
constexpr int L_O = 0, L_X = 4096, L_R1 = L_X + 2048, L_R2 = L_R1 + 4 * PW, L_Q = L_R2 + 4 * PW, L_ML = L_Q + 256, L_ON = L_ML + 64, L_CNT = L_ON + 32, L_T0 = L_CNT + 6, L_LATE = L_CNT + 7, L_TAG = L_CNT + 8, L_PRB = L_CNT + 12, L_END = L_CNT + 16;
enum { C_AUX = 0, C_SP = 1, C_Q = 2, C_ML = 3, C_ISSUED = 4, C_G = 5 };

__device__ __forceinline__ rsrc_t mk(const void* p, int bytes) { return __builtin_amdgcn_make_buffer_rsrc((void*)p, 0, bytes, 0x00020000); }
template <int AUX> __device__ __forceinline__ v4u ld16(rsrc_t r, int voff, int soff) { return __builtin_amdgcn_raw_buffer_load_b128(r, voff, soff, AUX); }
template <int AUX> __device__ __forceinline__ v2u ld8(rsrc_t r, int voff, int soff) { return __builtin_amdgcn_raw_buffer_load_b64(r, voff, soff, AUX); }
__device__ __forceinline__ unsigned ld4(rsrc_t r, int voff, int soff) { return __builtin_amdgcn_raw_buffer_load_b32(r, voff, soff, 0); }
__device__ __forceinline__ void st16(v4u v, rsrc_t r, int voff, int soff) { __builtin_amdgcn_raw_buffer_store_b128(v, r, voff, soff, AUX_SC1); }
__device__ __forceinline__ float asf(unsigned u) { return __builtin_bit_cast(float, u); }
__device__ __forceinline__ unsigned asu(float f) { return __builtin_bit_cast(unsigned, f); }
__device__ __forceinline__ unsigned uni(unsigned u) { return (unsigned)__builtin_amdgcn_readfirstlane((int)u); }
__device__ __forceinline__ float lo16(unsigned u) { return asf(u << 16); }
__device__ __forceinline__ float hi16(unsigned u) { return asf(u & 0xffff0000u); }
__device__ __forceinline__ unsigned pk(float a, float b) { return __builtin_bit_cast(unsigned, bf16x2{(__bf16)a, (__bf16)b}); }
__device__ __forceinline__ float rbf(float a) { return (float)(__bf16)a; }
template <int C> __device__ __forceinline__ unsigned dppu(unsigned x) { return (unsigned)__builtin_amdgcn_mov_dpp((int)x, C, 0xf, 0xf, true); }
template <int C> __device__ __forceinline__ float dppf(float x) { return asf(dppu<C>(asu(x))); }
template <int I> __device__ __forceinline__ float to_bank(float old, float src) {
  return asf((unsigned)__builtin_amdgcn_update_dpp((int)asu(old), (int)asu(src), 0x110 + 4 * I, 0xf, 1 << I, false)); }
constexpr int XOR1 = 0xB1, XOR2 = 0x4E, HMIR = 0x141, SHL4 = 0x104, SHL8 = 0x108, ROR4 = 0x124, ROR8 = 0x128, ROR12 = 0x12C;
__device__ __forceinline__ float sw32_add(float a, float b) {
  auto t = __builtin_amdgcn_permlane32_swap(asu(a), asu(b), false, false); return asf(t[0]) + asf(t[1]); }
__device__ __forceinline__ float sw16_add(float a, float b) {
  auto t = __builtin_amdgcn_permlane16_swap(asu(a), asu(b), false, false); return asf(t[0]) + asf(t[1]); }
__device__ __forceinline__ float sw32_max(float a) {
  auto t = __builtin_amdgcn_permlane32_swap(asu(a), asu(a), false, false); return fmaxf(asf(t[0]), asf(t[1])); }
__device__ __forceinline__ float sw16_max(float a) {
  auto t = __builtin_amdgcn_permlane16_swap(asu(a), asu(a), false, false); return fmaxf(asf(t[0]), asf(t[1])); }
__device__ __forceinline__ v4f mfma32(v4u a, v4u b, v4f c) {
  return __builtin_amdgcn_mfma_f32_16x16x32_bf16(__builtin_bit_cast(bf16x8, a), __builtin_bit_cast(bf16x8, b), c, 0, 0, 0); }
__device__ __forceinline__ v4f mfma16(v2u a, v2u b, v4f c) {
  return __builtin_amdgcn_mfma_f32_16x16x16bf16_1k(__builtin_bit_cast(s16x4, a), __builtin_bit_cast(s16x4, b), c, 0, 0, 0); }
__device__ __forceinline__ unsigned perm_lo(unsigned even, unsigned odd) { return __builtin_amdgcn_perm(odd, even, 0x05040100u); }
__device__ __forceinline__ unsigned perm_hi(unsigned even, unsigned odd) { return __builtin_amdgcn_perm(odd, even, 0x07060302u); }
__device__ __forceinline__ float exp2f_(float x) { return __builtin_amdgcn_exp2f(x); }
__device__ __forceinline__ float rcpf_(float x) { return __builtin_amdgcn_rcpf(x); }
#define SB() __builtin_amdgcn_sched_barrier(0)
#define VMWAIT(n) __builtin_amdgcn_s_waitcnt(0x0F70 | ((n) & 15) | (((n) >> 4) << 14))
#define LDS_BARRIER() do { SB(); asm volatile("s_waitcnt lgkmcnt(0)\n\ts_barrier" ::: "memory"); SB(); } while (0)

struct Tag { unsigned a, b; };
__device__ __forceinline__ bool fresh(v4u r, Tag t) { return (r.y == t.a) & (r.w == t.b); }
__device__ __forceinline__ v4u rec(unsigned p0, unsigned p1, Tag t) { return v4u{p0, t.a, p1, t.b}; }
template <int N> __device__ __forceinline__ bool all_fresh(const v4u (&r)[N], Tag t) {
  bool ok = true;
#pragma unroll
  for (int k = 0; k < N; ++k) ok = ok & fresh(r[k], t);
  return ok;
}
template <int N> __device__ __forceinline__ void request(v4u (&r)[N], const int (&off)[N], rsrc_t ws) {
#pragma unroll
  for (int k = 0; k < N; ++k) r[k] = ld16<AUX_SC1>(ws, off[k], 0);
}
template <int N, int SLEEP = 0> __device__ __forceinline__ unsigned await(v4u (&r)[N], const int (&off)[N], rsrc_t ws, Tag t) {
  bool ok = all_fresh<N>(r, t);
  unsigned spin = 0;
  if (__builtin_expect(!__all(ok), 0)) {
#pragma nounroll
    do {
      if (SLEEP) __builtin_amdgcn_s_sleep(SLEEP);
      asm volatile("" ::: "memory");
      if (!ok) request<N>(r, off, ws);
      ok = all_fresh<N>(r, t);
      ++spin;
    } while (!__all(ok) && spin < SPIN_CAP);
  }
  asm volatile("" ::: "memory");
  return spin;
}
__device__ __forceinline__ void lds_arrive(LAS unsigned* c, int lane) {
  asm volatile("s_waitcnt lgkmcnt(0)" ::: "memory");
  if (lane == 0) __hip_atomic_fetch_add(c, 1u, __ATOMIC_RELAXED, __HIP_MEMORY_SCOPE_WORKGROUP);
}
template <int SLEEP = 1> __device__ __forceinline__ void lds_wait(LAS unsigned* c, unsigned n) {
#pragma nounroll
  for (unsigned spin = 0; spin < (SPIN_CAP << 4); ++spin) {
    if (uni(__hip_atomic_load(c, __ATOMIC_RELAXED, __HIP_MEMORY_SCOPE_WORKGROUP)) >= n) break;
    if (SLEEP) __builtin_amdgcn_s_sleep(SLEEP);
  }
  asm volatile("" ::: "memory");
}

__device__ __forceinline__ float half_sum(float a, float b) {
  float x = sw32_add(a, b);
  x += dppf<XOR1>(x); x += dppf<XOR2>(x); x += dppf<HMIR>(x); x += dppf<ROR8>(x); return sw16_add(x, x); }
struct Stat { float m0, r0, m1, r1; };
__device__ __forceinline__ Stat ln_stats(const float (&u)[8], const float (&v)[8]) {
  const float su = ((u[0] + u[1]) + (u[2] + u[3])) + ((u[4] + u[5]) + (u[6] + u[7]));
  const float sv = ((v[0] + v[1]) + (v[2] + v[3])) + ((v[4] + v[5]) + (v[6] + v[7]));
  const float mf = half_sum(su, sv) * (1.f / D);
  const float mu = asf(__builtin_amdgcn_readlane(asu(mf), 0)), mv = asf(__builtin_amdgcn_readlane(asu(mf), 32));
  float qu = 0.f, qv = 0.f;
#pragma unroll
  for (int j = 0; j < 8; ++j) { const float a = u[j] - mu, b = v[j] - mv; qu = fmaf(a, a, qu); qv = fmaf(b, b, qv); }
  const float var = rbf(half_sum(qu, qv) * (1.f / D));
  const float rs = rcpf_(rbf(__builtin_amdgcn_sqrtf(rbf(var + 1e-5f))));
  return Stat{rbf(mu), asf(__builtin_amdgcn_readlane(asu(rs), 0)), rbf(mv), asf(__builtin_amdgcn_readlane(asu(rs), 32))};
}
__device__ __forceinline__ float ln_apply(float x, float m, float rs) { return rbf(rbf(x - m) * rs); }

__device__ __forceinline__ v4f product32(const v2u (&w)[8], v4u in, const unsigned (&mj)[4]) {
  v4f acc = {0.f, 0.f, 0.f, 0.f};
#pragma unroll
  for (int j = 0; j < 4; ++j) {
    v4u a;
#pragma unroll
    for (int t = 0; t < 4; ++t) a[t] = (j & 1) ? perm_hi(w[2 * t][j >> 1], w[2 * t + 1][j >> 1]) : perm_lo(w[2 * t][j >> 1], w[2 * t + 1][j >> 1]);
    acc = mfma32(a, v4u{in[0] & mj[j], in[1] & mj[j], in[2] & mj[j], in[3] & mj[j]}, acc);
  }
  return acc;
}
__device__ __forceinline__ bool sum_products(v4f p, float* buf, int a, LAS unsigned* c, int lane, v2f& s) {
  if ((lane & 15) < 8) *(v4f*)(buf + a * PW + ((lane >> 2) & 1) * 96 + (lane & 3) * 24 + (lane >> 4) * 4) = p;
  asm volatile("s_waitcnt lgkmcnt(0)" ::: "memory");
  unsigned old = 0;
  if (lane == 0) old = __hip_atomic_fetch_add(c, 1u, __ATOMIC_RELAXED, __HIP_MEMORY_SCOPE_WORKGROUP);
  if (uni(old) != 3u) return false;
  asm volatile("" ::: "memory");
  const float* q = buf + (lane >> 5) * 96 + (lane & 1) * 48 + ((lane & 31) >> 1);
  s = (v2f{q[0], q[24]} + v2f{q[PW], q[PW + 24]}) + (v2f{q[2 * PW], q[2 * PW + 24]} + v2f{q[3 * PW], q[3 * PW + 24]});
  return true;
}

#define ST(i)
#define CNT(i, v) (void)(v)
#define ST_FLUSH()

__global__ void __launch_bounds__(768, 3)
k_fused(const void*, const void*, const void*, const void*, const void*, const void*, const void*, const void*, const void*, void*, void*) {
  v16u ka; v4u kb; v2u kc2; u64 t_in;
  asm volatile("s_memrealtime %3\n\ts_load_dwordx16 %0, %4, 0x0\n\ts_load_dwordx4 %1, %4, 0x40\n\ts_load_dwordx2 %2, %4, 0x50"
               : "=&s"(ka), "=&s"(kb), "=&s"(kc2), "=&s"(t_in) : "s"(__builtin_amdgcn_kernarg_segment_ptr()));
  SB();
  const int tid = threadIdx.x, lane = tid & 63, wv = uni(tid >> 6);
  const int blk = blockIdx.x, x8 = blk & 7, i32 = blk >> 3, rg = i32 >> 3, k8 = i32 & 7, bat = i32 >> 4;
  const int g = lane >> 4, c16 = lane & 15, hg = x8 * 4 + rg, w = wv - 4;
  int kvbase = (bat * HKV + x8) * SK * 256 + (ROT ? (i32 & 15) * 8192 : (i32 & 15) * 131072) + w * 1024;
#define UNTIL(t) do { while ((int)((unsigned)__builtin_amdgcn_s_memrealtime() - (unsigned)t_in) < (int)(t)) { if (PSLEEP) __builtin_amdgcn_s_sleep(1); } SB(); } while (0)
#define PACE(n) do { UNTIL((base + (n) * tau) >> 4); if (WINB < 16) VMWAIT(WINB - 1); if (WIN < WINB && !big) VMWAIT(WIN - 1); SB(); } while (0)
#define PIECE(n) (kvbase + (ROT ? ((((n) + i32) & 15) << 17) : (n) * 8192))
  int koff = ((lane >> 2) & 3) * 256 + g * 64 + (lane & 3) * 16;
  asm volatile("s_waitcnt lgkmcnt(0)" : "+s"(ka), "+s"(kb), "+s"(kc2), "+s"(t_in), "+s"(kvbase), "+v"(koff));
#define ARG(v, i) ((void*)(((u64)uni((v)[2 * (i) + 1]) << 32) | uni((v)[2 * (i)])))
  const void *x = ARG(ka, 0), *kc = ARG(ka, 1), *vc = ARG(ka, 2), *wq = ARG(ka, 3), *wo = ARG(ka, 4), *w1 = ARG(ka, 5), *b1 = ARG(ka, 6), *w2 = ARG(ka, 7),
             *b2 = ARG(kb, 0);
  void *out = ARG(kb, 1), *wsp = ARG(kc2, 0);
  __shared__ __attribute__((aligned(16))) float lds[L_END];
  LAS unsigned* cnt = (LAS unsigned*)(lds + L_CNT);
  u64 pc;
  int code_left, code_left1;
  asm volatile("s_getpc_b64 %0\n.Lk_fused_pc:\n\ts_mov_b32 %1, .Lk_fused_end0-.Lk_fused_pc\n\ts_mov_b32 %2, .Lk_fused_end1-.Lk_fused_pc"
               : "=s"(pc), "=s"(code_left), "=s"(code_left1));
  code_left = max(code_left, code_left1);
  const int id2 = x8 * 14 + i32 - 18, rb = id2 >> 3, c2 = id2 & 7;
  const bool isq = i32 < 16, is1 = i32 >= 16 && i32 < 23, is2 = i32 >= 18;
  const bool big = isq || is1;
  const rsrc_t rk = mk(kc, KV_BYTES);
  v4u K[16];
  int wv2 = wv;
  asm volatile("" : "+s"(wv2));
#pragma unroll
  for (int n = 0; n < KFRONT; ++n) asm volatile("" : "=v"(K[n]));
  if (wv2 >= 4) {
    K[0] = ld16<AUX_NT>(rk, koff, PIECE(0));
    SB();
    if (!big) {
#pragma unroll
      for (int n = 1; n < KFRONT; ++n) { K[n] = ld16<AUX_NT>(rk, koff, PIECE(n)); SB(); }
    }
  } else if (tid < 12) {
    cnt[tid] = tid == L_T0 - L_CNT ? (unsigned)t_in : 0u;
  }
  LDS_BARRIER();
  if (WGBASE) t_in = uni(cnt[L_T0 - L_CNT]);

  const int lb = (lane >> 2) & 1;

  if (wv >= 4) {
    const rsrc_t rv = mk(vc, KV_BYTES);
    const int voff = lane * 16;
    v4u V[16];
    int tau = big ? (TLAST - TBIG) * 16 / (32 - KFRONT) : (TLAST - TBASE) * 16 / (32 - KFRONT), base = TLAST * 16 - 31 * tau;
#define REPLAN(n, sh) do { const int end_ = (TLAST + (sh)) * 16; tau = (end_ - base - ((n) - 1) * tau) / (32 - (n)); base = end_ - 31 * tau; } while (0)
    int late = 0;
    if (LATE) {
      if (big) VMWAIT(0); else VMWAIT(KFRONT - 1);
      SB();
      late = min(max((int)((unsigned)__builtin_amdgcn_s_memrealtime() - (unsigned)t_in) - T1NOM, 0) * LGAIN / 2, 400);
      base += late * 16;
      if (tid == 256) { cnt[L_PRB + 1 - L_CNT] = (unsigned)__builtin_amdgcn_s_memrealtime(); asm volatile("s_waitcnt lgkmcnt(0)" ::: "memory"); cnt[L_LATE - L_CNT] = (unsigned)(late + 1); }
    }
    if (big) {
      UNTIL(TBIG + late);
#pragma unroll
      for (int n = 1; n < KFRONT; ++n) { K[n] = ld16<AUX_NT>(rk, koff, PIECE(n)); SB(); }
    }
#pragma unroll
    for (int n = KFRONT; n < QPOS; ++n) {
      if (GLOBAL && (n == NSH || (EARLY && n == NSH0))) REPLAN(n, (int)uni(__hip_atomic_load(cnt + (L_TAG + 2 - L_CNT), __ATOMIC_RELAXED, __HIP_MEMORY_SCOPE_WORKGROUP)));
      if (n < QPOS - BURST) PACE(n);
      if (n < 16) K[n] = ld16<AUX_NT>(rk, koff, PIECE(n));
      else V[n - 16] = ld16<AUX_NT>(rv, voff, PIECE(n - 16));
      SB();
    }
    ST(2);
    if (!(ABL & 8)) lds_wait(cnt + C_Q, 3u);
    ST(3);
    const int bank = (lane >> 2) & 3, hh = lane & 3;
    const v4u qf = *(const v4u*)(lds + L_Q + hh * 64 + g * 16 + bank * 4);
    const v4u lt = *(const v4u*)(lds + L_TAG);
    v4f d[16];
#pragma unroll
    for (int n = 0; n < 16; ++n) d[n] = mfma32(K[n], qf, v4f{0.f, 0.f, 0.f, 0.f});
    SB();
    ST(1);
    float S[4], sn[16];
#pragma unroll
    for (int n = 0; n < 16; ++n) { d[n][0] += dppf<SHL4>(d[n][1]); d[n][2] += dppf<SHL4>(d[n][3]); }
    SB();
#pragma unroll
    for (int n = 0; n < 16; ++n) sn[n] = d[n][0] + dppf<SHL8>(d[n][2]);
    SB();
#pragma unroll
    for (int z = 0; z < 4; ++z) S[z] = to_bank<1>(sn[4 * z], sn[4 * z + 1]);
#pragma unroll
    for (int z = 0; z < 4; ++z) S[z] = to_bank<2>(S[z], sn[4 * z + 2]);
#pragma unroll
    for (int z = 0; z < 4; ++z) S[z] = to_bank<3>(S[z], sn[4 * z + 3]);
    SB();
    ST(7);
#pragma unroll
    for (int n = QPOS; n < QPOS + MID; ++n) { PACE(n); ST(9); V[n - 16] = ld16<AUX_NT>(rv, voff, PIECE(n - 16)); SB(); }
    ST(10);
    float mw = fmaxf(fmaxf(S[0], S[1]), fmaxf(S[2], S[3]));
    mw = fmaxf(mw, dppf<ROR4>(mw)); mw = fmaxf(mw, dppf<ROR8>(mw)); mw = sw32_max(sw16_max(mw));
    mw *= SC2;
    float lw = 0.f;
#pragma unroll
    for (int z = 0; z < 4; ++z) { S[z] = exp2f_(fmaf(S[z], SC2, -mw)); lw += S[z]; }
    lw += dppf<ROR4>(lw); lw += dppf<ROR8>(lw); lw = sw16_add(lw, lw); lw = sw32_add(lw, lw);
    if (lane < 4) *(v2f*)(lds + L_ML + lane * 16 + w * 2) = v2f{mw, lw};
    lds_arrive(cnt + C_ML, lane);
    ST(4);
    if (GLOBAL) REPLAN(QPOS + MID, (int)uni(lt.z));
    unsigned P[4], bm[4];
#pragma unroll
    for (int z = 0; z < 4; ++z) { P[z] = pk(S[z], dppf<SHL4>(S[z])); bm[z] = bank == z ? 0xffffffffu : 0u; }
    v4u Bf[2][4];
#pragma unroll
    for (int be = 0; be < 2; ++be) {
      const unsigned p0 = P[2 * be], p1 = P[2 * be + 1];
      Bf[be][0] = v4u{p0 & bm[0], dppu<ROR8>(p0) & bm[0], p1 & bm[0], dppu<ROR8>(p1) & bm[0]};
      Bf[be][1] = v4u{dppu<ROR4>(p0) & bm[1], dppu<ROR12>(p0) & bm[1], dppu<ROR4>(p1) & bm[1], dppu<ROR12>(p1) & bm[1]};
      Bf[be][2] = v4u{dppu<ROR8>(p0) & bm[2], p0 & bm[2], dppu<ROR8>(p1) & bm[2], p1 & bm[2]};
      Bf[be][3] = v4u{dppu<ROR12>(p0) & bm[3], dppu<ROR4>(p0) & bm[3], dppu<ROR12>(p1) & bm[3], dppu<ROR4>(p1) & bm[3]};
    }
    SB();
    v4f acc[2] = {v4f{0.f, 0.f, 0.f, 0.f}, v4f{0.f, 0.f, 0.f, 0.f}};
#pragma unroll
    for (int be = 0; be < 2; ++be) {
#pragma unroll
      for (int n = be ? PV0 : QPOS + MID; n < (be ? 32 : PV0); ++n) { PACE(n); V[n - 16] = ld16<AUX_NT>(rv, voff, PIECE(n - 16)); SB(); }
      if (be) { lds_arrive(cnt + C_ISSUED, lane); ST(5); }
      v4u A[8];
#pragma unroll
      for (int t = 0; t < 4; ++t) {
#pragma unroll
        for (int u = 0; u < 4; ++u) {
          A[2 * u][t] = perm_lo(V[8 * be + 2 * t][u], V[8 * be + 2 * t + 1][u]);
          A[2 * u + 1][t] = perm_hi(V[8 * be + 2 * t][u], V[8 * be + 2 * t + 1][u]);
        }
        SB();
      }
#pragma unroll
      for (int u = 0; u < 4; ++u) { acc[0] = mfma32(A[2 * u], Bf[be][u], acc[0]); acc[1] = mfma32(A[2 * u + 1], Bf[be][u], acc[1]); }
      SB();
    }
    *(v4f*)(lds + L_O + w * 512 + lane * 4) = acc[0];
    *(v4f*)(lds + L_O + w * 512 + 256 + lane * 4) = acc[1];
    LDS_BARRIER();
    ST(6);
    const Tag tag = {uni(lt.x), uni(lt.y)};
    const rsrc_t rws = mk(wsp, WS_END);
    ST(8);
    if (w == 7) {
      const int gq = (i32 >> 2) & 3, jq = i32 & 3;
      const int fcol = x8 * 64 + gq * 16 + g * 4 + jq;
      const unsigned xo = __builtin_amdgcn_raw_buffer_load_b16(mk(x, 2 * D * 2), fcol * 2, bat * D * 2, 0);
      const unsigned bias2 = __builtin_amdgcn_raw_buffer_load_b16(mk(b2, D * 2), fcol * 2, 0, 0);
      SB();
      v4u fg[1], fs[2];
      const int go[1] = {blk_at<BLK>(R_GP, (c16 < 14 ? c16 : 0) * 8 + x8) + (bat * 32 + gq * 8 + g * 2 + (jq >> 1)) * 16};
      int so[2] = {blk_at<512>(R_SP, lane * 8 + x8) + (bat * 16 + gq * 4 + jq) * 16, blk_at<512>(R_SP, (lane + 64) * 8 + x8) + (bat * 16 + gq * 4 + jq) * 16};
      __builtin_amdgcn_s_sleep(GDELTA);
      request<1>(fg, go, rws);
      await<1, 6>(fg, go, rws, tag);
      float gs = c16 < 14 ? asf((jq & 1) ? fg[0].z : fg[0].x) : 0.f;
      gs += dppf<XOR1>(gs); gs += dppf<XOR2>(gs); gs += dppf<HMIR>(gs); gs += dppf<ROR8>(gs);
      const float mv = rbf(rbf(gs) + lo16(bias2));
      float gv = rbf(mv * rcpf_(1.f + exp2f_(-LOG2E * mv))), xv = lo16(xo);
      int ooff = c16 == 0 ? (bat * D + fcol) * 2 : 2 * D * 2;
      asm volatile("" : "+v"(gv), "+v"(xv), "+v"(ooff), "+v"(so[0]), "+v"(so[1]));
      lds_wait<0>(cnt + C_SP, 4u);
      { const unsigned t_sp = (unsigned)__builtin_amdgcn_s_memrealtime();
        while ((unsigned)__builtin_amdgcn_s_memrealtime() - t_sp < FDELTA) {} }
      request<2>(fs, so, rws);
      CNT(15, (await<2, FSLEEP>(fs, so, rws, tag)));
      ST(11);
      float sc = sw16_add(sw32_add(lo16(fs[0].x) + lo16(fs[1].x), lo16(fs[0].z) + lo16(fs[1].z)), sw32_add(hi16(fs[0].x) + hi16(fs[1].x), hi16(fs[0].z) + hi16(fs[1].z)));
      sc += dppf<XOR1>(sc); sc += dppf<XOR2>(sc); sc += dppf<HMIR>(sc); sc += dppf<ROR8>(sc);
      const float ov = xv + rbf(rbf(sc) * gv);
      __builtin_amdgcn_raw_buffer_store_b16((unsigned short)pk(ov, ov), mk(out, 2 * D * 2), ooff, 0, 0);
      if (blk == 0 && lane == 0) __builtin_amdgcn_raw_buffer_store_b64(v2u{tag.a, tag.b}, rws, 0, WS_E, AUX_SC1);
      UNTIL(min(3000 + (int)uni(cnt[L_PRB - L_CNT]), 8000));
      asm volatile(".Lk_fused_end0:");
      ST(12);
    }
    ST_FLUSH();
    return;
  }

  const int a = wv;
  if (PRIO) __builtin_amdgcn_s_setprio(2);
  const rsrc_t rws = mk(wsp, WS_END);
  unsigned mj[4];
#pragma unroll
  for (int j = 0; j < 4; ++j) mj[j] = (lane & 11) == j ? 0xffffffffu : 0u;
  const v2u ep = ld8<AUX_SC1>(rws, 0, WS_E);
  unsigned touch = 0;
  if (TOUCH && a == 3 && i32 == 31) {
    const char* tp = lane < 2 ? (const char*)vc + (size_t)(lane * HKV + x8) * (SK * 256) + 65536
                   : lane == 2 ? (const char*)wo + x8 * 512 * 1024 + 4096
                   : lane < 5 ? (const char*)w2 + ((x8 * 14 + (lane - 3) * 13) >> 3) * 131072 + x8 * 128
                   : lane == 5 ? (const char*)b1 : lane == 6 ? (const char*)b2 : (const char*)out;
    if (lane < 8) touch = *(const __attribute__((address_space(1))) unsigned*)tp;
  }
  unsigned code_pf;
  Tag tag;
  v2f gpart = {0.f, 0.f};
  bool gpub = false;
  if (big) {
    const rsrc_t rx = mk(x, 2 * D * 2);
    const v4u xa0 = ld16<0>(rx, lane * 16, 0), xa1 = ld16<0>(rx, lane * 16, D * 2);
    const int ld = isq ? HQ * DH * 2 : F * 2, idb = isq ? x8 * 16 + (i32 & 7) * 2 + (i32 >> 3) : x8 * 7 + i32 - 16, cg = idb >> 1, row0 = (idb & 1) * 256 + a * 64;
    const rsrc_t rw = mk(isq ? wq : w1, (ABL & (isq ? 32 : 4)) ? 0 : D * ld);
    v2u wA[2][8];
    const int woff = g * 8 * ld + c16 * 8;
#pragma unroll
    for (int t = 0; t < 2; ++t)
#pragma unroll
      for (int i = 0; i < 8; ++i) wA[t][i] = ld8<AUX_NT>(rw, woff, (row0 + t * 32 + i) * ld + cg * 128);
    SB();
    tag = Tag{uni(ep.x) + 1u, uni(ep.y) + 0x9E3779B9u};
    float u[8], v[8];
#pragma unroll
    for (int t = 0; t < 4; ++t) { u[2 * t] = lo16(xa0[t]); u[2 * t + 1] = hi16(xa0[t]); v[2 * t] = lo16(xa1[t]); v[2 * t + 1] = hi16(xa1[t]); }
#pragma nounroll
    for (int pass = isq ? 1 : 0; pass < 2; ++pass) {
      const Stat st = ln_stats(u, v);
#pragma unroll
      for (int j = 0; j < 8; ++j) { u[j] = ln_apply(u[j], st.m0, st.r0); v[j] = ln_apply(v[j], st.m1, st.r1); }
    }
    float* xs = lds + L_X + a * 512;
    *(v4u*)(xs + lane * 4) = v4u{pk(u[0], u[1]), pk(u[2], u[3]), pk(u[4], u[5]), pk(u[6], u[7])};
    *(v4u*)(xs + 256 + lane * 4) = v4u{pk(v[0], v[1]), pk(v[2], v[3]), pk(v[4], v[5]), pk(v[6], v[7])};
    asm volatile("s_waitcnt lgkmcnt(0)" ::: "memory");
    v4f acc[4];
#pragma unroll
    for (int t = 0; t < 2; ++t) {
      const v4u in = *(const v4u*)(xs + lb * 256 + (row0 >> 1) + (t * 4 + g) * 4);
#pragma unroll
      for (int j = 0; j < 4; ++j) {
        v4u f;
#pragma unroll
        for (int k = 0; k < 4; ++k) f[k] = (j & 1) ? perm_hi(wA[t][2 * k][j >> 1], wA[t][2 * k + 1][j >> 1]) : perm_lo(wA[t][2 * k][j >> 1], wA[t][2 * k + 1][j >> 1]);
        acc[j] = mfma32(f, in, t ? acc[j] : v4f{0.f, 0.f, 0.f, 0.f});
      }
    }
    const v4f p = (lane & 2) ? ((lane & 1) ? acc[3] : acc[2]) : ((lane & 1) ? acc[1] : acc[0]);
    v2f sum;
    if (sum_products(p, lds + L_R1, a, cnt + C_AUX, lane, sum)) {
      const unsigned pp = pk(sum.x, sum.y);
      st16(rec(pp, dppu<XOR1>(pp), tag), rws, (lane & 1) ? WS_END : lane * 8, blk_at<512>(isq ? R_QP : R_HP, idb));
    }
  } else {
    tag = Tag{uni(ep.x) + 1u, uni(ep.y) + 0x9E3779B9u};
  }
  ST(1);
  if (a == 3) {
    if (GLOBAL && blk >= 248) {
      if (LATE) lds_wait(cnt + (L_LATE - L_CNT), 1u);
      const unsigned ts = (unsigned)t_in + uni(cnt[L_LATE - L_CNT]);
      if (lane == 0) st16(rec(ts, uni(cnt[L_PRB + 1 - L_CNT]), tag), rws, x8 * 16, WS_T0);
    }
  }
  code_pf = ld4(mk((const void*)(pc & ~(u64)127), code_left + (int)(pc & 127)), lane * 128, (a & 1) * 8192);
  if (a < 2) {
    v4u qr[2];
    const int qo[2] = {blk_at<512>(R_QP, (x8 * 8 + a * 4 + g) * 2) + (bat * 16 + c16) * 16, blk_at<512>(R_QP, (x8 * 8 + a * 4 + g) * 2 + 1) + (bat * 16 + c16) * 16};
    UNTIL(QWAIT + (LATE ? (int)uni(cnt[L_LATE - L_CNT]) : 0));
    request<2>(qr, qo, rws);
    CNT(13, (await<2, 16>(qr, qo, rws, tag)));
    *(v2u*)(lds + L_Q + (a * 64 + lane) * 2) = v2u{pk(lo16(qr[0].x) + lo16(qr[1].x), hi16(qr[0].x) + hi16(qr[1].x)), pk(lo16(qr[0].z) + lo16(qr[1].z), hi16(qr[0].z) + hi16(qr[1].z))};
    lds_arrive(cnt + C_Q, lane);
  }
  if (a == 2) {
    int sh = 0, mx = 0;
    if (GLOBAL) {
      v4u tr[1];
      const int to[1] = {WS_T0 + (lane & 7) * 16};
      UNTIL(TWAIT + (LATE ? (int)uni(cnt[L_LATE - L_CNT]) : 0));
      request<1>(tr, to, rws);
      await<1, 16>(tr, to, rws, tag);
      sh = mx = min(max((int)(tr[0].x - (unsigned)t_in), -400), 400);
      sh += (int)dppu<XOR1>(sh); sh += (int)dppu<XOR2>(sh); sh += (int)dppu<HMIR>(sh);
      mx = max(mx, (int)dppu<XOR1>(mx)); mx = max(mx, (int)dppu<XOR2>(mx)); mx = max(mx, (int)dppu<HMIR>(mx));
      sh = max(sh >> 3, mx - GAIN);
      int pv = (int)(tr[0].z - (unsigned)t_in);
      if (PROBE == 1) { pv = min(pv, (int)dppu<XOR1>(pv)); pv = min(pv, (int)dppu<XOR2>(pv)); pv = min(pv, (int)dppu<HMIR>(pv)); }
      if (PROBE == 2) { pv += (int)dppu<XOR1>(pv); pv += (int)dppu<XOR2>(pv); pv += (int)dppu<HMIR>(pv); pv >>= 3; }
      if (PROBE == 3) { pv = max(pv, (int)dppu<XOR1>(pv)); pv = max(pv, (int)dppu<XOR2>(pv)); pv = max(pv, (int)dppu<HMIR>(pv)); }
      if (lane == 0) cnt[L_PRB - L_CNT] = (unsigned)pv;
    }
    if (lane == 0) *(v4u*)(lds + L_TAG) = v4u{tag.a, tag.b, (unsigned)sh, (unsigned)mx};
    lds_arrive(cnt + C_Q, lane);
  }
  ST(2);
  asm volatile("" :: "v"(code_pf));
  asm volatile("" :: "v"(touch));
  if (is2) {
    const rsrc_t r2 = mk(w2, (ABL & 2) ? 0 : D * F * 2);
    v2u wB[8];
    v4u hr[2];
    const int hoff = (lb * 16 + (a & 1) * 8 + g * 2 + ((lane >> 3) & 1)) * 16;
    const int ho[2] = {blk_at<512>(R_HP, (2 * rb + (a >> 1)) * 2) + hoff, blk_at<512>(R_HP, (2 * rb + (a >> 1)) * 2 + 1) + hoff};
    lds_wait(cnt + C_Q, 3u);
    UNTIL(HWAIT + (int)uni(((const unsigned*)lds)[L_TAG + 3]));
    request<2>(hr, ho, rws);
    const v2u bias1 = ld8<0>(mk(b1, F * 2), g * 16 + ((lane >> 3) & 1) * 8, (rb * 128 + a * 32) * 2);
#pragma unroll
    for (int i = 0; i < 8; ++i) wB[i] = ld8<AUX_NT>(r2, g * 8 * 1024 + c16 * 8 + (i & 3) * 1024, (rb * 128 + a * 32 + (i & 4)) * 1024 + c2 * 128);
    SB();
    await<2, 16>(hr, ho, rws, tag);
    float h[4] = {lo16(hr[0].x) + lo16(hr[1].x), hi16(hr[0].x) + hi16(hr[1].x), lo16(hr[0].z) + lo16(hr[1].z), hi16(hr[0].z) + hi16(hr[1].z)};
    const float bb[4] = {lo16(bias1.x), hi16(bias1.x), lo16(bias1.y), hi16(bias1.y)};
    constexpr float G0 = -2.f * 0.7978845608028654f * LOG2E, G1 = G0 * 0.044715f;
#pragma unroll
    for (int j = 0; j < 4; ++j) {
      const float t = rbf(rbf(h[j]) + bb[j]);
      h[j] = t * rcpf_(1.f + exp2f_(t * fmaf(G1, t * t, G0)));
    }
    const unsigned h0 = pk(h[0], h[1]), h1 = pk(h[2], h[3]);
    gpub = sum_products(product32(wB, v4u{h0, h1, dppu<ROR8>(h0), dppu<ROR8>(h1)}, mj), lds + L_R2, a, cnt + C_G, lane, gpart);
    ST(11);
  }
  lds_wait(cnt + C_ML, 8u);
  ST(4);
  if (a == 3 && lane < 32) ((unsigned*)lds)[L_ON + lane] = 0xffffffffu;
  const int mh = (lane >> 1) & 3;
  float fw[8], lse;
  {
    v4f ml[4];
#pragma unroll
    for (int t = 0; t < 4; ++t) ml[t] = *(const v4f*)(lds + L_ML + mh * 16 + 4 * t);
    const float M = fmaxf(fmaxf(fmaxf(ml[0][0], ml[0][2]), fmaxf(ml[1][0], ml[1][2])), fmaxf(fmaxf(ml[2][0], ml[2][2]), fmaxf(ml[3][0], ml[3][2])));
    float L = 0.f;
#pragma unroll
    for (int t = 0; t < 4; ++t) { fw[2 * t] = exp2f_(ml[t][0] - M); fw[2 * t + 1] = exp2f_(ml[t][2] - M); L = fmaf(fw[2 * t], ml[t][1], fmaf(fw[2 * t + 1], ml[t][3], L)); }
    const float il = rcpf_(L);
#pragma unroll
    for (int t = 0; t < 8; ++t) fw[t] *= il;
    lse = M + __builtin_amdgcn_logf(L);
    asm volatile("" : "+v"(fw[0]), "+v"(fw[1]), "+v"(fw[2]), "+v"(fw[3]), "+v"(fw[4]), "+v"(fw[5]), "+v"(fw[6]), "+v"(fw[7]));
    if (a == 0 && lane < 8) st16(rec(asu(lse), asu(lse), tag), rws, lane * 16, blk_at<128>(R_LS, x8 * 32 + i32));
  }
  const int dq = k8 >> 1, ch = k8 & 1;
  v2u wO[8];
  v4u lr[1], pr[2];
  const int lo[1] = {blk_at<128>(R_LS, x8 * 32 + (a & 1) * 16 + (lane >> 3) + (lane & 1) * 8) + rg * 32};
  int po[2] = {blk_at<512>(R_R1, hg * 32 + (a & 1) * 16 + (lane >> 3)) + dq * 128 + (lane & 7) * 16,
               blk_at<512>(R_R1, hg * 32 + (a & 1) * 16 + (lane >> 3) + 8) + dq * 128 + (lane & 7) * 16};
  int on_w = (L_ON + a * 16 + (lane & 7) * 2 + (lane >> 5)) * 4, on_r = (L_ON + lb * 16 + g * 4) * 4;
  int soff = c16 < 8 ? blk_at<512>(R_SP, (hg * 4 + dq) * 8 + ch * 4 + a) + (lb * 16 + g * 4 + (lane & 3)) * 16 : WS_END;
  const rsrc_t ro = mk(wo, (ABL & 2) ? 0 : D * HQ * DH * 2);
  const int wvo = g * 8 * 1024 + c16 * 8, wso = (hg * 128 + dq * 32) * 1024 + ch * 512 + a * 128;
  const int shift = GLOBAL ? (int)uni(((const unsigned*)lds)[L_TAG + 2]) : 0;
  lds_wait(cnt + C_ISSUED, 8u);
  UNTIL(TLAST + WOD0 + shift);
  request<1>(lr, lo, mk(wsp, a < 2 ? WS_END : 0));
#pragma unroll
  for (int i = 0; i < WOPRE; ++i) {
    if (i == 4) UNTIL(TLAST + WOD1 + shift);
    wO[i] = ld8<AUX_NT>(ro, wvo + (i & 3) * 1024, wso + (i & 4) * 1024);
    SB();
  }
  const float* mp = lds + L_O + (a * 16 + ((lane >> 3) & 3) * 4 + mh) * 4 + (lane >> 5) * 2 + (lane & 1);
  int moff = (lane & 8) ? WS_END : blk_at<512>(R_R1, (x8 * 4 + mh) * 32 + i32) + (a * 8 + ((lane >> 5) * 2 + (lane & 1)) * 2 + ((lane >> 4) & 1)) * 16;
  asm volatile("" : "+v"(moff), "+v"(po[0]), "+v"(po[1]), "+v"(on_w), "+v"(on_r), "+v"(soff), "+v"(mj[0]), "+v"(mj[1]), "+v"(mj[2]), "+v"(mj[3]));
  LDS_BARRIER();
  ST(6);
  {
    v2f t[8];
#pragma unroll
    for (int ww = 0; ww < 8; ++ww) t[ww] = v2f{mp[ww * 512], mp[ww * 512 + 256]};
    const v2f s = (fw[0] * t[0] + fw[1] * t[1] + fw[2] * t[2] + fw[3] * t[3]) + (fw[4] * t[4] + fw[5] * t[5] + fw[6] * t[6] + fw[7] * t[7]);
    const unsigned p = pk(s.x, s.y);
    st16(rec(p, dppu<ROR8>(p), tag), rws, moff, 0);
    if (gpub) st16(rec(asu(gpart.x), asu(gpart.y), tag), rws, lane * 16, blk_at<BLK>(R_GP, rb * 8 + c2));
    SB();
#pragma unroll
    for (int i = WOPRE; i < 8; ++i) wO[i] = ld8<AUX_NT>(ro, wvo + (i & 3) * 1024, wso + (i & 4) * 1024);
  }
  SB();
  ST(7);
  const unsigned t_pub = (unsigned)__builtin_amdgcn_s_memrealtime();
  float wt0 = 0.f, wt1 = 0.f;
  if (a < 2) {
    await<1, 6>(lr, lo, rws, tag);
    const float ls = asf(lr[0].x);
    float M = fmaxf(ls, dppf<XOR1>(ls));
    M = fmaxf(M, dppf<ROR8>(M)); M = sw32_max(sw16_max(M));
    const float e = exp2f_(ls - M), ex = dppf<XOR1>(e);
    float z = e + ex;
    z += dppf<ROR8>(z); z = sw16_add(z, z); z = sw32_add(z, z);
    const float iz = rcpf_(z);
    wt0 = ((lane & 1) ? ex : e) * iz; wt1 = ((lane & 1) ? e : ex) * iz;
    asm volatile("" : "+v"(wt0), "+v"(wt1));
    while ((unsigned)__builtin_amdgcn_s_memrealtime() - t_pub < DELTA) {}
  }
  asm volatile("" :: "v"(lr[0]));
  SB();
  request<2>(pr, po, mk(wsp, a < 2 ? WS_END : 0));
  SB();
  v4u wf[4];
#pragma unroll
  for (int j = 0; j < 4; ++j)
#pragma unroll
    for (int t = 0; t < 4; ++t) wf[j][t] = (j & 1) ? perm_hi(wO[2 * t][j >> 1], wO[2 * t + 1][j >> 1]) : perm_lo(wO[2 * t][j >> 1], wO[2 * t + 1][j >> 1]);
  asm volatile("" : "+v"(wf[0]), "+v"(wf[1]), "+v"(wf[2]), "+v"(wf[3]));
  ST(8);
  if (a < 2) {
    CNT(14, (await<2, GSLEEP>(pr, po, rws, tag)));
    ST(9);
    const float o0 = fmaf(wt0, lo16(pr[0].x), wt1 * lo16(pr[1].x)), o1 = fmaf(wt0, hi16(pr[0].x), wt1 * hi16(pr[1].x)),
                o2 = fmaf(wt0, lo16(pr[0].z), wt1 * lo16(pr[1].z)), o3 = fmaf(wt0, hi16(pr[0].z), wt1 * hi16(pr[1].z));
    float oo = sw16_add(sw32_add(o0, o2), sw32_add(o1, o3));
    oo += dppf<ROR8>(oo);
    const auto t = __builtin_amdgcn_permlane16_swap(asu(oo), asu(oo), false, false);
    const unsigned ob = pk(asf(t[0]), asf(t[1]));
    *(LAS unsigned*)((LAS char*)lds + on_w) = ob == 0xffffffffu ? 0x7fc07fc0u : ob;
  }
  asm volatile("" :: "v"(pr[0]), "v"(pr[1]));
  {
    v4u in;
#pragma nounroll
    for (unsigned spin = 0; spin < (SPIN_CAP << 6); ++spin) {
      asm volatile("" ::: "memory");
      in = *(const LAS v4u*)((LAS char*)lds + on_r);
      if (__all(max(max(in[0], in[1]), max(in[2], in[3])) != 0xffffffffu)) break;
    }
    v4u bj[4];
#pragma unroll
    for (int j = 0; j < 4; ++j) bj[j] = v4u{in[0] & mj[j], in[1] & mj[j], in[2] & mj[j], in[3] & mj[j]};
    SB();
    v4f sp = {0.f, 0.f, 0.f, 0.f};
#pragma unroll
    for (int j = 0; j < 4; ++j) sp = mfma32(wf[j], bj[j], sp);
    st16(rec(pk(sp[0], sp[1]), pk(sp[2], sp[3]), tag), rws, soff, 0);
    lds_arrive(cnt + C_SP, lane);
    asm volatile(".Lk_fused_end1:");
  }
  ST(10);
  ST_FLUSH();
}
}

extern "C" void kernel_launch(void* const* d_in, const int* in_sizes, int n_in, void* d_out, int out_size, void* d_ws, size_t ws_size,
                              hipStream_t stream) {
  fused::k_fused<<<256, 768, 0, stream>>>(d_in[0], d_in[1], d_in[2], d_in[3], d_in[4], d_in[6], d_in[7], d_in[8], d_in[9], d_out, d_ws);
}
```

```cpp
#define LGAIN 3
#define T1NOM 105
#define PROBE 1
#include <hip/hip_runtime.h>
#include <hip/hip_bf16.h>
#include <stdint.h>

#define KFRONT 2
#define WIN 4
#define BURST 3
#define WIN2 64
#define QPOS 24
#define QWAIT 500
#define GSLEEP 0
#define DELTA 40
#define WOPOS 0
#define FINAL_AT 2u
#define FSLEEP 0
#define ONSLEEP 0
#define HWAIT 600
#define ROT 0
#define HEARLY 1
#define WINY 64
#define PRIO 1
#define TAU 335
#define TBASE 30
#define MID 0
#define FDELTA 35
#define PV0 28
#define TLAST 1080
#define TBIG 100
#define WGBASE 1
#define WOD0 45
#define WOD1 75
#define PSLEEP 0
#define GDELTA 22
#define WINB 5
#define SPREAD 1
#define TOUCH 1
#define WOPRE 8
#define GLOBAL 1
#define TWAIT 300
#define NSH 13
#define NSH0 4
#define EARLY 0
#define GAIN 80
#define LATE 1
#define CODE_PF 1
#define ABL 0

namespace fused {
typedef unsigned v16u __attribute__((ext_vector_type(16)));
typedef unsigned v4u __attribute__((ext_vector_type(4)));
typedef unsigned v2u __attribute__((ext_vector_type(2)));
typedef float v4f __attribute__((ext_vector_type(4)));
typedef float v2f __attribute__((ext_vector_type(2)));
typedef short s16x4 __attribute__((ext_vector_type(4)));
typedef __bf16 bf16x2 __attribute__((ext_vector_type(2)));
typedef __bf16 bf16x8 __attribute__((ext_vector_type(8)));
typedef __amdgpu_buffer_rsrc_t rsrc_t;
typedef unsigned long long u64;
#define LAS __attribute__((address_space(3)))

constexpr int D = 512, HQ = 32, HKV = 8, SK = 8192, DH = 128, F = 1792;
constexpr int AUX_NT = 2, AUX_SC1 = 16;
constexpr int KV_BYTES = 2 * HKV * SK * DH * 2;
constexpr int BLK = 1024;
constexpr int WS_E = 0, WS_T0 = 128, R_QP = 256, R_HP = R_QP + 8 * 512, R_GP = R_HP + 4 * 512, R_LS = R_GP + 7 * BLK, R_R1 = R_LS + 16 * 128, R_SP = R_R1 + 64 * 512,
              WS_END = SPREAD ? 16 << 17 : 16 << 17;
template <int S> __device__ __forceinline__ int blk_at(int region, int j) {
  return SPREAD ? (((j + (j >> 4)) & 15) << 17) + region + (j >> 4) * S : region * 16 + j * S; }
constexpr unsigned SPIN_CAP = 1u << 18;
constexpr float LOG2E = 1.4426950408889634f;
constexpr float SC2 = 0.08838834764831845f * LOG2E;
constexpr int PW = 192;
constexpr int L_O = 0, L_X = 4096, L_R1 = L_X + 2048, L_R2 = L_R1 + 4 * PW, L_Q = L_R2 + 4 * PW, L_ML = L_Q + 256, L_ON = L_ML + 64, L_CNT = L_ON + 32, L_T0 = L_CNT + 6, L_LATE = L_CNT + 7, L_TAG = L_CNT + 8, L_PRB = L_CNT + 12, L_END = L_CNT + 16;
enum { C_AUX = 0, C_SP = 1, C_Q = 2, C_ML = 3, C_ISSUED = 4, C_G = 5 };

__device__ __forceinline__ rsrc_t mk(const void* p, int bytes) { return __builtin_amdgcn_make_buffer_rsrc((void*)p, 0, bytes, 0x00020000); }
template <int AUX> __device__ __forceinline__ v4u ld16(rsrc_t r, int voff, int soff) { return __builtin_amdgcn_raw_buffer_load_b128(r, voff, soff, AUX); }
template <int AUX> __device__ __forceinline__ v2u ld8(rsrc_t r, int voff, int soff) { return __builtin_amdgcn_raw_buffer_load_b64(r, voff, soff, AUX); }
__device__ __forceinline__ unsigned ld4(rsrc_t r, int voff, int soff) { return __builtin_amdgcn_raw_buffer_load_b32(r, voff, soff, 0); }
__device__ __forceinline__ void st16(v4u v, rsrc_t r, int voff, int soff) { __builtin_amdgcn_raw_buffer_store_b128(v, r, voff, soff, AUX_SC1); }
__device__ __forceinline__ float asf(unsigned u) { return __builtin_bit_cast(float, u); }
__device__ __forceinline__ unsigned asu(float f) { return __builtin_bit_cast(unsigned, f); }
__device__ __forceinline__ unsigned uni(unsigned u) { return (unsigned)__builtin_amdgcn_readfirstlane((int)u); }
__device__ __forceinline__ float lo16(unsigned u) { return asf(u << 16); }
__device__ __forceinline__ float hi16(unsigned u) { return asf(u & 0xffff0000u); }
__device__ __forceinline__ unsigned pk(float a, float b) { return __builtin_bit_cast(unsigned, bf16x2{(__bf16)a, (__bf16)b}); }
__device__ __forceinline__ float rbf(float a) { return (float)(__bf16)a; }
template <int C> __device__ __forceinline__ unsigned dppu(unsigned x) { return (unsigned)__builtin_amdgcn_mov_dpp((int)x, C, 0xf, 0xf, true); }
template <int C> __device__ __forceinline__ float dppf(float x) { return asf(dppu<C>(asu(x))); }
template <int I> __device__ __forceinline__ float to_bank(float old, float src) {
  return asf((unsigned)__builtin_amdgcn_update_dpp((int)asu(old), (int)asu(src), 0x110 + 4 * I, 0xf, 1 << I, false)); }
constexpr int XOR1 = 0xB1, XOR2 = 0x4E, HMIR = 0x141, SHL4 = 0x104, SHL8 = 0x108, ROR4 = 0x124, ROR8 = 0x128, ROR12 = 0x12C;
__device__ __forceinline__ float sw32_add(float a, float b) {
  auto t = __builtin_amdgcn_permlane32_swap(asu(a), asu(b), false, false); return asf(t[0]) + asf(t[1]); }
__device__ __forceinline__ float sw16_add(float a, float b) {
  auto t = __builtin_amdgcn_permlane16_swap(asu(a), asu(b), false, false); return asf(t[0]) + asf(t[1]); }
__device__ __forceinline__ float sw32_max(float a) {
  auto t = __builtin_amdgcn_permlane32_swap(asu(a), asu(a), false, false); return fmaxf(asf(t[0]), asf(t[1])); }
__device__ __forceinline__ float sw16_max(float a) {
  auto t = __builtin_amdgcn_permlane16_swap(asu(a), asu(a), false, false); return fmaxf(asf(t[0]), asf(t[1])); }
__device__ __forceinline__ v4f mfma32(v4u a, v4u b, v4f c) {
  return __builtin_amdgcn_mfma_f32_16x16x32_bf16(__builtin_bit_cast(bf16x8, a), __builtin_bit_cast(bf16x8, b), c, 0, 0, 0); }
__device__ __forceinline__ v4f mfma16(v2u a, v2u b, v4f c) {
  return __builtin_amdgcn_mfma_f32_16x16x16bf16_1k(__builtin_bit_cast(s16x4, a), __builtin_bit_cast(s16x4, b), c, 0, 0, 0); }
__device__ __forceinline__ unsigned perm_lo(unsigned even, unsigned odd) { return __builtin_amdgcn_perm(odd, even, 0x05040100u); }
__device__ __forceinline__ unsigned perm_hi(unsigned even, unsigned odd) { return __builtin_amdgcn_perm(odd, even, 0x07060302u); }
__device__ __forceinline__ float exp2f_(float x) { return __builtin_amdgcn_exp2f(x); }
__device__ __forceinline__ float rcpf_(float x) { return __builtin_amdgcn_rcpf(x); }
#define SB() __builtin_amdgcn_sched_barrier(0)
#define VMWAIT(n) __builtin_amdgcn_s_waitcnt(0x0F70 | ((n) & 15) | (((n) >> 4) << 14))
#define LDS_BARRIER() do { SB(); asm volatile("s_waitcnt lgkmcnt(0)\n\ts_barrier" ::: "memory"); SB(); } while (0)

struct Tag { unsigned a, b; };
__device__ __forceinline__ bool fresh(v4u r, Tag t) { return (r.y == t.a) & (r.w == t.b); }
__device__ __forceinline__ v4u rec(unsigned p0, unsigned p1, Tag t) { return v4u{p0, t.a, p1, t.b}; }
template <int N> __device__ __forceinline__ bool all_fresh(const v4u (&r)[N], Tag t) {
  bool ok = true;
#pragma unroll
  for (int k = 0; k < N; ++k) ok = ok & fresh(r[k], t);
  return ok;
}
template <int N> __device__ __forceinline__ void request(v4u (&r)[N], const int (&off)[N], rsrc_t ws) {
#pragma unroll
  for (int k = 0; k < N; ++k) r[k] = ld16<AUX_SC1>(ws, off[k], 0);
}
template <int N, int SLEEP = 0> __device__ __forceinline__ unsigned await(v4u (&r)[N], const int (&off)[N], rsrc_t ws, Tag t) {
  bool ok = all_fresh<N>(r, t);
  unsigned spin = 0;
  if (__builtin_expect(!__all(ok), 0)) {
#pragma nounroll
    do {
      if (SLEEP) __builtin_amdgcn_s_sleep(SLEEP);
      asm volatile("" ::: "memory");
      if (!ok) request<N>(r, off, ws);
      ok = all_fresh<N>(r, t);
      ++spin;
    } while (!__all(ok) && spin < SPIN_CAP);
  }
  asm volatile("" ::: "memory");
  return spin;
}
__device__ __forceinline__ void lds_arrive(LAS unsigned* c, int lane) {
  asm volatile("s_waitcnt lgkmcnt(0)" ::: "memory");
  if (lane == 0) __hip_atomic_fetch_add(c, 1u, __ATOMIC_RELAXED, __HIP_MEMORY_SCOPE_WORKGROUP);
}
template <int SLEEP = 1> __device__ __forceinline__ void lds_wait(LAS unsigned* c, unsigned n) {
#pragma nounroll
  for (unsigned spin = 0; spin < (SPIN_CAP << 4); ++spin) {
    if (uni(__hip_atomic_load(c, __ATOMIC_RELAXED, __HIP_MEMORY_SCOPE_WORKGROUP)) >= n) break;
    if (SLEEP) __builtin_amdgcn_s_sleep(SLEEP);
  }
  asm volatile("" ::: "memory");
}

__device__ __forceinline__ float half_sum(float a, float b) {
  float x = sw32_add(a, b);
  x += dppf<XOR1>(x); x += dppf<XOR2>(x); x += dppf<HMIR>(x); x += dppf<ROR8>(x); return sw16_add(x, x); }
struct Stat { float m0, r0, m1, r1; };
__device__ __forceinline__ Stat ln_stats(const float (&u)[8], const float (&v)[8]) {
  const float su = ((u[0] + u[1]) + (u[2] + u[3])) + ((u[4] + u[5]) + (u[6] + u[7]));
  const float sv = ((v[0] + v[1]) + (v[2] + v[3])) + ((v[4] + v[5]) + (v[6] + v[7]));
  const float mf = half_sum(su, sv) * (1.f / D);
  const float mu = asf(__builtin_amdgcn_readlane(asu(mf), 0)), mv = asf(__builtin_amdgcn_readlane(asu(mf), 32));
  float qu = 0.f, qv = 0.f;
#pragma unroll
  for (int j = 0; j < 8; ++j) { const float a = u[j] - mu, b = v[j] - mv; qu = fmaf(a, a, qu); qv = fmaf(b, b, qv); }
  const float var = rbf(half_sum(qu, qv) * (1.f / D));
  const float rs = rcpf_(rbf(__builtin_amdgcn_sqrtf(rbf(var + 1e-5f))));
  return Stat{rbf(mu), asf(__builtin_amdgcn_readlane(asu(rs), 0)), rbf(mv), asf(__builtin_amdgcn_readlane(asu(rs), 32))};
}
__device__ __forceinline__ float ln_apply(float x, float m, float rs) { return rbf(rbf(x - m) * rs); }

__device__ __forceinline__ v4f product32(const v2u (&w)[8], v4u in, const unsigned (&mj)[4]) {
  v4f acc = {0.f, 0.f, 0.f, 0.f};
#pragma unroll
  for (int j = 0; j < 4; ++j) {
    v4u a;
#pragma unroll
    for (int t = 0; t < 4; ++t) a[t] = (j & 1) ? perm_hi(w[2 * t][j >> 1], w[2 * t + 1][j >> 1]) : perm_lo(w[2 * t][j >> 1], w[2 * t + 1][j >> 1]);
    acc = mfma32(a, v4u{in[0] & mj[j], in[1] & mj[j], in[2] & mj[j], in[3] & mj[j]}, acc);
  }
  return acc;
}
__device__ __forceinline__ bool sum_products(v4f p, float* buf, int a, LAS unsigned* c, int lane, v2f& s) {
  if ((lane & 15) < 8) *(v4f*)(buf + a * PW + ((lane >> 2) & 1) * 96 + (lane & 3) * 24 + (lane >> 4) * 4) = p;
  asm volatile("s_waitcnt lgkmcnt(0)" ::: "memory");
  unsigned old = 0;
  if (lane == 0) old = __hip_atomic_fetch_add(c, 1u, __ATOMIC_RELAXED, __HIP_MEMORY_SCOPE_WORKGROUP);
  if (uni(old) != 3u) return false;
  asm volatile("" ::: "memory");
  const float* q = buf + (lane >> 5) * 96 + (lane & 1) * 48 + ((lane & 31) >> 1);
  s = (v2f{q[0], q[24]} + v2f{q[PW], q[PW + 24]}) + (v2f{q[2 * PW], q[2 * PW + 24]} + v2f{q[3 * PW], q[3 * PW + 24]});
  return true;
}

#define ST(i)
#define CNT(i, v) (void)(v)
#define ST_FLUSH()

__global__ void __launch_bounds__(768, 3)
k_fused(const void*, const void*, const void*, const void*, const void*, const void*, const void*, const void*, const void*, void*, void*) {
  v16u ka; v4u kb; v2u kc2; u64 t_in;
  asm volatile("s_memrealtime %3\n\ts_load_dwordx16 %0, %4, 0x0\n\ts_load_dwordx4 %1, %4, 0x40\n\ts_load_dwordx2 %2, %4, 0x50"
               : "=&s"(ka), "=&s"(kb), "=&s"(kc2), "=&s"(t_in) : "s"(__builtin_amdgcn_kernarg_segment_ptr()));
  SB();
  const int tid = threadIdx.x, lane = tid & 63, wv = uni(tid >> 6);
  const int blk = blockIdx.x, x8 = blk & 7, i32 = blk >> 3, rg = i32 >> 3, k8 = i32 & 7, bat = i32 >> 4;
  const int g = lane >> 4, c16 = lane & 15, hg = x8 * 4 + rg, w = wv - 4;
  int kvbase = (bat * HKV + x8) * SK * 256 + (ROT ? (i32 & 15) * 8192 : (i32 & 15) * 131072) + w * 1024;
#define UNTIL(t) do { while ((int)((unsigned)__builtin_amdgcn_s_memrealtime() - (unsigned)t_in) < (int)(t)) { if (PSLEEP) __builtin_amdgcn_s_sleep(1); } SB(); } while (0)
#define PACE(n) do { UNTIL((base + (n) * tau) >> 4); if (WINB < 16) VMWAIT(WINB - 1); if (WIN < WINB && !big) VMWAIT(WIN - 1); SB(); } while (0)
#define PIECE(n) (kvbase + (ROT ? ((((n) + i32) & 15) << 17) : (n) * 8192))
  int koff = ((lane >> 2) & 3) * 256 + g * 64 + (lane & 3) * 16;
  asm volatile("s_waitcnt lgkmcnt(0)" : "+s"(ka), "+s"(kb), "+s"(kc2), "+s"(t_in), "+s"(kvbase), "+v"(koff));
  SB();
  const unsigned t_arg = (unsigned)__builtin_amdgcn_s_memrealtime();
  SB();
#define ARG(v, i) ((void*)(((u64)uni((v)[2 * (i) + 1]) << 32) | uni((v)[2 * (i)])))
  const void *x = ARG(ka, 0), *kc = ARG(ka, 1), *vc = ARG(ka, 2), *wq = ARG(ka, 3), *wo = ARG(ka, 4), *w1 = ARG(ka, 5), *b1 = ARG(ka, 6), *w2 = ARG(ka, 7),
             *b2 = ARG(kb, 0);
  void *out = ARG(kb, 1), *wsp = ARG(kc2, 0);
  __shared__ __attribute__((aligned(16))) float lds[L_END];
  LAS unsigned* cnt = (LAS unsigned*)(lds + L_CNT);
  u64 pc;
  int code_left, code_left1;
  asm volatile("s_getpc_b64 %0\n.Lk_fused_pc:\n\ts_mov_b32 %1, .Lk_fused_end0-.Lk_fused_pc\n\ts_mov_b32 %2, .Lk_fused_end1-.Lk_fused_pc"
               : "=s"(pc), "=s"(code_left), "=s"(code_left1));
  code_left = max(code_left, code_left1);
  const int id2 = x8 * 14 + i32 - 18, rb = id2 >> 3, c2 = id2 & 7;
  const bool isq = i32 < 16, is1 = i32 >= 16 && i32 < 23, is2 = i32 >= 18;
  const bool big = isq || is1;
  const rsrc_t rk = mk(kc, KV_BYTES);
  v4u K[16];
  int wv2 = wv;
  asm volatile("" : "+s"(wv2));
#pragma unroll
  for (int n = 0; n < KFRONT; ++n) asm volatile("" : "=v"(K[n]));
  if (wv2 >= 4) {
    K[0] = ld16<AUX_NT>(rk, koff, PIECE(0));
    SB();
    if (!big) {
#pragma unroll
      for (int n = 1; n < KFRONT; ++n) { K[n] = ld16<AUX_NT>(rk, koff, PIECE(n)); SB(); }
    }
  } else if (tid < 12) {
    cnt[tid] = tid == L_T0 - L_CNT ? (unsigned)t_in : 0u;
  }
  LDS_BARRIER();
  if (WGBASE) t_in = uni(cnt[L_T0 - L_CNT]);

  const int lb = (lane >> 2) & 1;

  if (wv >= 4) {
    const rsrc_t rv = mk(vc, KV_BYTES);
    const int voff = lane * 16;
    v4u V[16];
    int tau = big ? (TLAST - TBIG) * 16 / (32 - KFRONT) : (TLAST - TBASE) * 16 / (32 - KFRONT), base = TLAST * 16 - 31 * tau;
#define REPLAN(n, sh) do { const int end_ = (TLAST + (sh)) * 16; tau = (end_ - base - ((n) - 1) * tau) / (32 - (n)); base = end_ - 31 * tau; } while (0)
    int late = 0;
    if (LATE) {
      if (big) VMWAIT(0); else VMWAIT(KFRONT - 1);
      SB();
      late = min(max((int)((unsigned)__builtin_amdgcn_s_memrealtime() - (unsigned)t_in) - T1NOM, 0) * LGAIN / 2, 400);
      base += late * 16;
      if (tid == 256) cnt[L_LATE - L_CNT] = (unsigned)(late + 1);
    }
    if (big) {
      UNTIL(TBIG + late);
#pragma unroll
      for (int n = 1; n < KFRONT; ++n) { K[n] = ld16<AUX_NT>(rk, koff, PIECE(n)); SB(); }
    }
#pragma unroll
    for (int n = KFRONT; n < QPOS; ++n) {
      if (GLOBAL && (n == NSH || (EARLY && n == NSH0))) REPLAN(n, (int)uni(__hip_atomic_load(cnt + (L_TAG + 2 - L_CNT), __ATOMIC_RELAXED, __HIP_MEMORY_SCOPE_WORKGROUP)));
      if (n < QPOS - BURST) PACE(n);
      if (n < 16) K[n] = ld16<AUX_NT>(rk, koff, PIECE(n));
      else V[n - 16] = ld16<AUX_NT>(rv, voff, PIECE(n - 16));
      SB();
    }
    ST(2);
    if (!(ABL & 8)) lds_wait(cnt + C_Q, 3u);
    ST(3);
    const int bank = (lane >> 2) & 3, hh = lane & 3;
    const v4u qf = *(const v4u*)(lds + L_Q + hh * 64 + g * 16 + bank * 4);
    const v4u lt = *(const v4u*)(lds + L_TAG);
    v4f d[16];
#pragma unroll
    for (int n = 0; n < 16; ++n) d[n] = mfma32(K[n], qf, v4f{0.f, 0.f, 0.f, 0.f});
    SB();
    ST(1);
    float S[4], sn[16];
#pragma unroll
    for (int n = 0; n < 16; ++n) { d[n][0] += dppf<SHL4>(d[n][1]); d[n][2] += dppf<SHL4>(d[n][3]); }
    SB();
#pragma unroll
    for (int n = 0; n < 16; ++n) sn[n] = d[n][0] + dppf<SHL8>(d[n][2]);
    SB();
#pragma unroll
    for (int z = 0; z < 4; ++z) S[z] = to_bank<1>(sn[4 * z], sn[4 * z + 1]);
#pragma unroll
    for (int z = 0; z < 4; ++z) S[z] = to_bank<2>(S[z], sn[4 * z + 2]);
#pragma unroll
    for (int z = 0; z < 4; ++z) S[z] = to_bank<3>(S[z], sn[4 * z + 3]);
    SB();
    ST(7);
#pragma unroll
    for (int n = QPOS; n < QPOS + MID; ++n) { PACE(n); ST(9); V[n - 16] = ld16<AUX_NT>(rv, voff, PIECE(n - 16)); SB(); }
    ST(10);
    float mw = fmaxf(fmaxf(S[0], S[1]), fmaxf(S[2], S[3]));
    mw = fmaxf(mw, dppf<ROR4>(mw)); mw = fmaxf(mw, dppf<ROR8>(mw)); mw = sw32_max(sw16_max(mw));
    mw *= SC2;
    float lw = 0.f;
#pragma unroll
    for (int z = 0; z < 4; ++z) { S[z] = exp2f_(fmaf(S[z], SC2, -mw)); lw += S[z]; }
    lw += dppf<ROR4>(lw); lw += dppf<ROR8>(lw); lw = sw16_add(lw, lw); lw = sw32_add(lw, lw);
    if (lane < 4) *(v2f*)(lds + L_ML + lane * 16 + w * 2) = v2f{mw, lw};
    lds_arrive(cnt + C_ML, lane);
    ST(4);
    if (GLOBAL) REPLAN(QPOS + MID, (int)uni(lt.z));
    unsigned P[4], bm[4];
#pragma unroll
    for (int z = 0; z < 4; ++z) { P[z] = pk(S[z], dppf<SHL4>(S[z])); bm[z] = bank == z ? 0xffffffffu : 0u; }
    v4u Bf[2][4];
#pragma unroll
    for (int be = 0; be < 2; ++be) {
      const unsigned p0 = P[2 * be], p1 = P[2 * be + 1];
      Bf[be][0] = v4u{p0 & bm[0], dppu<ROR8>(p0) & bm[0], p1 & bm[0], dppu<ROR8>(p1) & bm[0]};
      Bf[be][1] = v4u{dppu<ROR4>(p0) & bm[1], dppu<ROR12>(p0) & bm[1], dppu<ROR4>(p1) & bm[1], dppu<ROR12>(p1) & bm[1]};
      Bf[be][2] = v4u{dppu<ROR8>(p0) & bm[2], p0 & bm[2], dppu<ROR8>(p1) & bm[2], p1 & bm[2]};
      Bf[be][3] = v4u{dppu<ROR12>(p0) & bm[3], dppu<ROR4>(p0) & bm[3], dppu<ROR12>(p1) & bm[3], dppu<ROR4>(p1) & bm[3]};
    }
    SB();
    v4f acc[2] = {v4f{0.f, 0.f, 0.f, 0.f}, v4f{0.f, 0.f, 0.f, 0.f}};
#pragma unroll
    for (int be = 0; be < 2; ++be) {
#pragma unroll
      for (int n = be ? PV0 : QPOS + MID; n < (be ? 32 : PV0); ++n) { PACE(n); V[n - 16] = ld16<AUX_NT>(rv, voff, PIECE(n - 16)); SB(); }
      if (be) { lds_arrive(cnt + C_ISSUED, lane); ST(5); }
      v4u A[8];
#pragma unroll
      for (int t = 0; t < 4; ++t) {
#pragma unroll
        for (int u = 0; u < 4; ++u) {
          A[2 * u][t] = perm_lo(V[8 * be + 2 * t][u], V[8 * be + 2 * t + 1][u]);
          A[2 * u + 1][t] = perm_hi(V[8 * be + 2 * t][u], V[8 * be + 2 * t + 1][u]);
        }
        SB();
      }
#pragma unroll
      for (int u = 0; u < 4; ++u) { acc[0] = mfma32(A[2 * u], Bf[be][u], acc[0]); acc[1] = mfma32(A[2 * u + 1], Bf[be][u], acc[1]); }
      SB();
    }
    *(v4f*)(lds + L_O + w * 512 + lane * 4) = acc[0];
    *(v4f*)(lds + L_O + w * 512 + 256 + lane * 4) = acc[1];
    LDS_BARRIER();
    ST(6);
    const Tag tag = {uni(lt.x), uni(lt.y)};
    const rsrc_t rws = mk(wsp, WS_END);
    ST(8);
    if (w == 7) {
      const int gq = (i32 >> 2) & 3, jq = i32 & 3;
      const int fcol = x8 * 64 + gq * 16 + g * 4 + jq;
      const unsigned xo = __builtin_amdgcn_raw_buffer_load_b16(mk(x, 2 * D * 2), fcol * 2, bat * D * 2, 0);
      const unsigned bias2 = __builtin_amdgcn_raw_buffer_load_b16(mk(b2, D * 2), fcol * 2, 0, 0);
      SB();
      v4u fg[1], fs[2];
      const int go[1] = {blk_at<BLK>(R_GP, (c16 < 14 ? c16 : 0) * 8 + x8) + (bat * 32 + gq * 8 + g * 2 + (jq >> 1)) * 16};
      int so[2] = {blk_at<512>(R_SP, lane * 8 + x8) + (bat * 16 + gq * 4 + jq) * 16, blk_at<512>(R_SP, (lane + 64) * 8 + x8) + (bat * 16 + gq * 4 + jq) * 16};
      __builtin_amdgcn_s_sleep(GDELTA);
      request<1>(fg, go, rws);
      await<1, 6>(fg, go, rws, tag);
      float gs = c16 < 14 ? asf((jq & 1) ? fg[0].z : fg[0].x) : 0.f;
      gs += dppf<XOR1>(gs); gs += dppf<XOR2>(gs); gs += dppf<HMIR>(gs); gs += dppf<ROR8>(gs);
      const float mv = rbf(rbf(gs) + lo16(bias2));
      float gv = rbf(mv * rcpf_(1.f + exp2f_(-LOG2E * mv))), xv = lo16(xo);
      int ooff = c16 == 0 ? (bat * D + fcol) * 2 : 2 * D * 2;
      asm volatile("" : "+v"(gv), "+v"(xv), "+v"(ooff), "+v"(so[0]), "+v"(so[1]));
      lds_wait<0>(cnt + C_SP, 4u);
      { const unsigned t_sp = (unsigned)__builtin_amdgcn_s_memrealtime();
        while ((unsigned)__builtin_amdgcn_s_memrealtime() - t_sp < FDELTA) {} }
      request<2>(fs, so, rws);
      CNT(15, (await<2, FSLEEP>(fs, so, rws, tag)));
      ST(11);
      float sc = sw16_add(sw32_add(lo16(fs[0].x) + lo16(fs[1].x), lo16(fs[0].z) + lo16(fs[1].z)), sw32_add(hi16(fs[0].x) + hi16(fs[1].x), hi16(fs[0].z) + hi16(fs[1].z)));
      sc += dppf<XOR1>(sc); sc += dppf<XOR2>(sc); sc += dppf<HMIR>(sc); sc += dppf<ROR8>(sc);
      const float ov = xv + rbf(rbf(sc) * gv);
      __builtin_amdgcn_raw_buffer_store_b16((unsigned short)pk(ov, ov), mk(out, 2 * D * 2), ooff, 0, 0);
      if (blk == 0 && lane == 0) __builtin_amdgcn_raw_buffer_store_b64(v2u{tag.a, tag.b}, rws, 0, WS_E, AUX_SC1);
      { const unsigned ev = PROBE == 1 ? t_arg : big ? (unsigned)t_in : uni(cnt[L_PRB - L_CNT]);
        UNTIL(min(3000 + (int)(ev - (unsigned)t_in), 8000)); }
      asm volatile(".Lk_fused_end0:");
      ST(12);
    }
    ST_FLUSH();
    return;
  }

  const int a = wv;
  if (PRIO) __builtin_amdgcn_s_setprio(2);
  const rsrc_t rws = mk(wsp, WS_END);
  unsigned mj[4];
#pragma unroll
  for (int j = 0; j < 4; ++j) mj[j] = (lane & 11) == j ? 0xffffffffu : 0u;
  const v2u ep = ld8<AUX_SC1>(rws, 0, WS_E);
  unsigned touch = 0;
  if (TOUCH && a == 3 && i32 == 31) {
    const char* tp = lane < 2 ? (const char*)vc + (size_t)(lane * HKV + x8) * (SK * 256) + 65536
                   : lane == 2 ? (const char*)wo + x8 * 512 * 1024 + 4096
                   : lane < 5 ? (const char*)w2 + ((x8 * 14 + (lane - 3) * 13) >> 3) * 131072 + x8 * 128
                   : lane == 5 ? (const char*)b1 : lane == 6 ? (const char*)b2 : (const char*)out;
    if (lane < 8) touch = *(const __attribute__((address_space(1))) unsigned*)tp;
  }
  unsigned code_pf;
  Tag tag;
  v2f gpart = {0.f, 0.f};
  bool gpub = false;
  if (big) {
    const rsrc_t rx = mk(x, 2 * D * 2);
    const v4u xa0 = ld16<0>(rx, lane * 16, 0), xa1 = ld16<0>(rx, lane * 16, D * 2);
    const int ld = isq ? HQ * DH * 2 : F * 2, idb = isq ? x8 * 16 + (i32 & 7) * 2 + (i32 >> 3) : x8 * 7 + i32 - 16, cg = idb >> 1, row0 = (idb & 1) * 256 + a * 64;
    const rsrc_t rw = mk(isq ? wq : w1, (ABL & (isq ? 32 : 4)) ? 0 : D * ld);
    v2u wA[2][8];
    const int woff = g * 8 * ld + c16 * 8;
#pragma unroll
    for (int t = 0; t < 2; ++t)
#pragma unroll
      for (int i = 0; i < 8; ++i) wA[t][i] = ld8<AUX_NT>(rw, woff, (row0 + t * 32 + i) * ld + cg * 128);
    SB();
    tag = Tag{uni(ep.x) + 1u, uni(ep.y) + 0x9E3779B9u};
    float u[8], v[8];
#pragma unroll
    for (int t = 0; t < 4; ++t) { u[2 * t] = lo16(xa0[t]); u[2 * t + 1] = hi16(xa0[t]); v[2 * t] = lo16(xa1[t]); v[2 * t + 1] = hi16(xa1[t]); }
#pragma nounroll
    for (int pass = isq ? 1 : 0; pass < 2; ++pass) {
      const Stat st = ln_stats(u, v);
#pragma unroll
      for (int j = 0; j < 8; ++j) { u[j] = ln_apply(u[j], st.m0, st.r0); v[j] = ln_apply(v[j], st.m1, st.r1); }
    }
    float* xs = lds + L_X + a * 512;
    *(v4u*)(xs + lane * 4) = v4u{pk(u[0], u[1]), pk(u[2], u[3]), pk(u[4], u[5]), pk(u[6], u[7])};
    *(v4u*)(xs + 256 + lane * 4) = v4u{pk(v[0], v[1]), pk(v[2], v[3]), pk(v[4], v[5]), pk(v[6], v[7])};
    asm volatile("s_waitcnt lgkmcnt(0)" ::: "memory");
    v4f acc[4];
#pragma unroll
    for (int t = 0; t < 2; ++t) {
      const v4u in = *(const v4u*)(xs + lb * 256 + (row0 >> 1) + (t * 4 + g) * 4);
#pragma unroll
      for (int j = 0; j < 4; ++j) {
        v4u f;
#pragma unroll
        for (int k = 0; k < 4; ++k) f[k] = (j & 1) ? perm_hi(wA[t][2 * k][j >> 1], wA[t][2 * k + 1][j >> 1]) : perm_lo(wA[t][2 * k][j >> 1], wA[t][2 * k + 1][j >> 1]);
        acc[j] = mfma32(f, in, t ? acc[j] : v4f{0.f, 0.f, 0.f, 0.f});
      }
    }
    const v4f p = (lane & 2) ? ((lane & 1) ? acc[3] : acc[2]) : ((lane & 1) ? acc[1] : acc[0]);
    v2f sum;
    if (sum_products(p, lds + L_R1, a, cnt + C_AUX, lane, sum)) {
      const unsigned pp = pk(sum.x, sum.y);
      st16(rec(pp, dppu<XOR1>(pp), tag), rws, (lane & 1) ? WS_END : lane * 8, blk_at<512>(isq ? R_QP : R_HP, idb));
    }
  } else {
    tag = Tag{uni(ep.x) + 1u, uni(ep.y) + 0x9E3779B9u};
    SB();
    if (PROBE == 2 && tid == 0) cnt[L_PRB - L_CNT] = (unsigned)__builtin_amdgcn_s_memrealtime();
    SB();
  }
  ST(1);
  if (a == 3) {
    if (GLOBAL && blk >= 248) {
      if (LATE) lds_wait(cnt + (L_LATE - L_CNT), 1u);
      const unsigned ts = (unsigned)t_in + uni(cnt[L_LATE - L_CNT]);
      if (lane == 0) st16(rec(ts, ts, tag), rws, x8 * 16, WS_T0);
    }
  }
  code_pf = ld4(mk((const void*)(pc & ~(u64)127), code_left + (int)(pc & 127)), lane * 128, (a & 1) * 8192);
  if (a < 2) {
    v4u qr[2];
    const int qo[2] = {blk_at<512>(R_QP, (x8 * 8 + a * 4 + g) * 2) + (bat * 16 + c16) * 16, blk_at<512>(R_QP, (x8 * 8 + a * 4 + g) * 2 + 1) + (bat * 16 + c16) * 16};
    UNTIL(QWAIT + (LATE ? (int)uni(cnt[L_LATE - L_CNT]) : 0));
    request<2>(qr, qo, rws);
    CNT(13, (await<2, 16>(qr, qo, rws, tag)));
    *(v2u*)(lds + L_Q + (a * 64 + lane) * 2) = v2u{pk(lo16(qr[0].x) + lo16(qr[1].x), hi16(qr[0].x) + hi16(qr[1].x)), pk(lo16(qr[0].z) + lo16(qr[1].z), hi16(qr[0].z) + hi16(qr[1].z))};
    lds_arrive(cnt + C_Q, lane);
  }
  if (a == 2) {
    int sh = 0, mx = 0;
    if (GLOBAL) {
      v4u tr[1];
      const int to[1] = {WS_T0 + (lane & 7) * 16};
      UNTIL(TWAIT + (LATE ? (int)uni(cnt[L_LATE - L_CNT]) : 0));
      request<1>(tr, to, rws);
      await<1, 16>(tr, to, rws, tag);
      sh = mx = min(max((int)(tr[0].x - (unsigned)t_in), -400), 400);
      sh += (int)dppu<XOR1>(sh); sh += (int)dppu<XOR2>(sh); sh += (int)dppu<HMIR>(sh);
      mx = max(mx, (int)dppu<XOR1>(mx)); mx = max(mx, (int)dppu<XOR2>(mx)); mx = max(mx, (int)dppu<HMIR>(mx));
      sh = max(sh >> 3, mx - GAIN);
    }
    if (lane == 0) *(v4u*)(lds + L_TAG) = v4u{tag.a, tag.b, (unsigned)sh, (unsigned)mx};
    lds_arrive(cnt + C_Q, lane);
  }
  ST(2);
  asm volatile("" :: "v"(code_pf));
  asm volatile("" :: "v"(touch));
  if (is2) {
    const rsrc_t r2 = mk(w2, (ABL & 2) ? 0 : D * F * 2);
    v2u wB[8];
    v4u hr[2];
    const int hoff = (lb * 16 + (a & 1) * 8 + g * 2 + ((lane >> 3) & 1)) * 16;
    const int ho[2] = {blk_at<512>(R_HP, (2 * rb + (a >> 1)) * 2) + hoff, blk_at<512>(R_HP, (2 * rb + (a >> 1)) * 2 + 1) + hoff};
    lds_wait(cnt + C_Q, 3u);
    UNTIL(HWAIT + (int)uni(((const unsigned*)lds)[L_TAG + 3]));
    request<2>(hr, ho, rws);
    const v2u bias1 = ld8<0>(mk(b1, F * 2), g * 16 + ((lane >> 3) & 1) * 8, (rb * 128 + a * 32) * 2);
#pragma unroll
    for (int i = 0; i < 8; ++i) wB[i] = ld8<AUX_NT>(r2, g * 8 * 1024 + c16 * 8 + (i & 3) * 1024, (rb * 128 + a * 32 + (i & 4)) * 1024 + c2 * 128);
    SB();
    await<2, 16>(hr, ho, rws, tag);
    float h[4] = {lo16(hr[0].x) + lo16(hr[1].x), hi16(hr[0].x) + hi16(hr[1].x), lo16(hr[0].z) + lo16(hr[1].z), hi16(hr[0].z) + hi16(hr[1].z)};
    const float bb[4] = {lo16(bias1.x), hi16(bias1.x), lo16(bias1.y), hi16(bias1.y)};
    constexpr float G0 = -2.f * 0.7978845608028654f * LOG2E, G1 = G0 * 0.044715f;
#pragma unroll
    for (int j = 0; j < 4; ++j) {
      const float t = rbf(rbf(h[j]) + bb[j]);
      h[j] = t * rcpf_(1.f + exp2f_(t * fmaf(G1, t * t, G0)));
    }
    const unsigned h0 = pk(h[0], h[1]), h1 = pk(h[2], h[3]);
    gpub = sum_products(product32(wB, v4u{h0, h1, dppu<ROR8>(h0), dppu<ROR8>(h1)}, mj), lds + L_R2, a, cnt + C_G, lane, gpart);
    ST(11);
  }
  lds_wait(cnt + C_ML, 8u);
  ST(4);
  if (a == 3 && lane < 32) ((unsigned*)lds)[L_ON + lane] = 0xffffffffu;
  const int mh = (lane >> 1) & 3;
  float fw[8], lse;
  {
    v4f ml[4];
#pragma unroll
    for (int t = 0; t < 4; ++t) ml[t] = *(const v4f*)(lds + L_ML + mh * 16 + 4 * t);
    const float M = fmaxf(fmaxf(fmaxf(ml[0][0], ml[0][2]), fmaxf(ml[1][0], ml[1][2])), fmaxf(fmaxf(ml[2][0], ml[2][2]), fmaxf(ml[3][0], ml[3][2])));
    float L = 0.f;
#pragma unroll
    for (int t = 0; t < 4; ++t) { fw[2 * t] = exp2f_(ml[t][0] - M); fw[2 * t + 1] = exp2f_(ml[t][2] - M); L = fmaf(fw[2 * t], ml[t][1], fmaf(fw[2 * t + 1], ml[t][3], L)); }
    const float il = rcpf_(L);
#pragma unroll
    for (int t = 0; t < 8; ++t) fw[t] *= il;
    lse = M + __builtin_amdgcn_logf(L);
    asm volatile("" : "+v"(fw[0]), "+v"(fw[1]), "+v"(fw[2]), "+v"(fw[3]), "+v"(fw[4]), "+v"(fw[5]), "+v"(fw[6]), "+v"(fw[7]));
    if (a == 0 && lane < 8) st16(rec(asu(lse), asu(lse), tag), rws, lane * 16, blk_at<128>(R_LS, x8 * 32 + i32));
  }
  const int dq = k8 >> 1, ch = k8 & 1;
  v2u wO[8];
  v4u lr[1], pr[2];
  const int lo[1] = {blk_at<128>(R_LS, x8 * 32 + (a & 1) * 16 + (lane >> 3) + (lane & 1) * 8) + rg * 32};
  int po[2] = {blk_at<512>(R_R1, hg * 32 + (a & 1) * 16 + (lane >> 3)) + dq * 128 + (lane & 7) * 16,
               blk_at<512>(R_R1, hg * 32 + (a & 1) * 16 + (lane >> 3) + 8) + dq * 128 + (lane & 7) * 16};
  int on_w = (L_ON + a * 16 + (lane & 7) * 2 + (lane >> 5)) * 4, on_r = (L_ON + lb * 16 + g * 4) * 4;
  int soff = c16 < 8 ? blk_at<512>(R_SP, (hg * 4 + dq) * 8 + ch * 4 + a) + (lb * 16 + g * 4 + (lane & 3)) * 16 : WS_END;
  const rsrc_t ro = mk(wo, (ABL & 2) ? 0 : D * HQ * DH * 2);
  const int wvo = g * 8 * 1024 + c16 * 8, wso = (hg * 128 + dq * 32) * 1024 + ch * 512 + a * 128;
  const int shift = GLOBAL ? (int)uni(((const unsigned*)lds)[L_TAG + 2]) : 0;
  lds_wait(cnt + C_ISSUED, 8u);
  UNTIL(TLAST + WOD0 + shift);
  request<1>(lr, lo, mk(wsp, a < 2 ? WS_END : 0));
#pragma unroll
  for (int i = 0; i < WOPRE; ++i) {
    if (i == 4) UNTIL(TLAST + WOD1 + shift);
    wO[i] = ld8<AUX_NT>(ro, wvo + (i & 3) * 1024, wso + (i & 4) * 1024);
    SB();
  }
  const float* mp = lds + L_O + (a * 16 + ((lane >> 3) & 3) * 4 + mh) * 4 + (lane >> 5) * 2 + (lane & 1);
  int moff = (lane & 8) ? WS_END : blk_at<512>(R_R1, (x8 * 4 + mh) * 32 + i32) + (a * 8 + ((lane >> 5) * 2 + (lane & 1)) * 2 + ((lane >> 4) & 1)) * 16;
  asm volatile("" : "+v"(moff), "+v"(po[0]), "+v"(po[1]), "+v"(on_w), "+v"(on_r), "+v"(soff), "+v"(mj[0]), "+v"(mj[1]), "+v"(mj[2]), "+v"(mj[3]));
  LDS_BARRIER();
  ST(6);
  {
    v2f t[8];
#pragma unroll
    for (int ww = 0; ww < 8; ++ww) t[ww] = v2f{mp[ww * 512], mp[ww * 512 + 256]};
    const v2f s = (fw[0] * t[0] + fw[1] * t[1] + fw[2] * t[2] + fw[3] * t[3]) + (fw[4] * t[4] + fw[5] * t[5] + fw[6] * t[6] + fw[7] * t[7]);
    const unsigned p = pk(s.x, s.y);
    st16(rec(p, dppu<ROR8>(p), tag), rws, moff, 0);
    if (gpub) st16(rec(asu(gpart.x), asu(gpart.y), tag), rws, lane * 16, blk_at<BLK>(R_GP, rb * 8 + c2));
    SB();
#pragma unroll
    for (int i = WOPRE; i < 8; ++i) wO[i] = ld8<AUX_NT>(ro, wvo + (i & 3) * 1024, wso + (i & 4) * 1024);
  }
  SB();
  ST(7);
  const unsigned t_pub = (unsigned)__builtin_amdgcn_s_memrealtime();
  float wt0 = 0.f, wt1 = 0.f;
  if (a < 2) {
    await<1, 6>(lr, lo, rws, tag);
    const float ls = asf(lr[0].x);
    float M = fmaxf(ls, dppf<XOR1>(ls));
    M = fmaxf(M, dppf<ROR8>(M)); M = sw32_max(sw16_max(M));
    const float e = exp2f_(ls - M), ex = dppf<XOR1>(e);
    float z = e + ex;
    z += dppf<ROR8>(z); z = sw16_add(z, z); z = sw32_add(z, z);
    const float iz = rcpf_(z);
    wt0 = ((lane & 1) ? ex : e) * iz; wt1 = ((lane & 1) ? e : ex) * iz;
    asm volatile("" : "+v"(wt0), "+v"(wt1));
    while ((unsigned)__builtin_amdgcn_s_memrealtime() - t_pub < DELTA) {}
  }
  asm volatile("" :: "v"(lr[0]));
  SB();
  request<2>(pr, po, mk(wsp, a < 2 ? WS_END : 0));
  SB();
  v4u wf[4];
#pragma unroll
  for (int j = 0; j < 4; ++j)
#pragma unroll
    for (int t = 0; t < 4; ++t) wf[j][t] = (j & 1) ? perm_hi(wO[2 * t][j >> 1], wO[2 * t + 1][j >> 1]) : perm_lo(wO[2 * t][j >> 1], wO[2 * t + 1][j >> 1]);
  asm volatile("" : "+v"(wf[0]), "+v"(wf[1]), "+v"(wf[2]), "+v"(wf[3]));
  ST(8);
  if (a < 2) {
    CNT(14, (await<2, GSLEEP>(pr, po, rws, tag)));
    ST(9);
    const float o0 = fmaf(wt0, lo16(pr[0].x), wt1 * lo16(pr[1].x)), o1 = fmaf(wt0, hi16(pr[0].x), wt1 * hi16(pr[1].x)),
                o2 = fmaf(wt0, lo16(pr[0].z), wt1 * lo16(pr[1].z)), o3 = fmaf(wt0, hi16(pr[0].z), wt1 * hi16(pr[1].z));
    float oo = sw16_add(sw32_add(o0, o2), sw32_add(o1, o3));
    oo += dppf<ROR8>(oo);
    const auto t = __builtin_amdgcn_permlane16_swap(asu(oo), asu(oo), false, false);
    const unsigned ob = pk(asf(t[0]), asf(t[1]));
    *(LAS unsigned*)((LAS char*)lds + on_w) = ob == 0xffffffffu ? 0x7fc07fc0u : ob;
  }
  asm volatile("" :: "v"(pr[0]), "v"(pr[1]));
  {
    v4u in;
#pragma nounroll
    for (unsigned spin = 0; spin < (SPIN_CAP << 6); ++spin) {
      asm volatile("" ::: "memory");
      in = *(const LAS v4u*)((LAS char*)lds + on_r);
      if (__all(max(max(in[0], in[1]), max(in[2], in[3])) != 0xffffffffu)) break;
    }
    v4u bj[4];
#pragma unroll
    for (int j = 0; j < 4; ++j) bj[j] = v4u{in[0] & mj[j], in[1] & mj[j], in[2] & mj[j], in[3] & mj[j]};
    SB();
    v4f sp = {0.f, 0.f, 0.f, 0.f};
#pragma unroll
    for (int j = 0; j < 4; ++j) sp = mfma32(wf[j], bj[j], sp);
    st16(rec(pk(sp[0], sp[1]), pk(sp[2], sp[3]), tag), rws, soff, 0);
    lds_arrive(cnt + C_SP, lane);
    asm volatile(".Lk_fused_end1:");
  }
  ST(10);
  ST_FLUSH();
}
}

extern "C" void kernel_launch(void* const* d_in, const int* in_sizes, int n_in, void* d_out, int out_size, void* d_ws, size_t ws_size,
                              hipStream_t stream) {
  fused::k_fused<<<256, 768, 0, stream>>>(d_in[0], d_in[1], d_in[2], d_in[3], d_in[4], d_in[6], d_in[7], d_in[8], d_in[9], d_out, d_ws);
}
```

```cpp
#include <hip/hip_runtime.h>
#include <hip/hip_bf16.h>
#include <stdint.h>

namespace fused {
typedef unsigned v16u __attribute__((ext_vector_type(16)));
typedef unsigned v4u __attribute__((ext_vector_type(4)));
typedef unsigned v2u __attribute__((ext_vector_type(2)));
typedef float v4f __attribute__((ext_vector_type(4)));
typedef float v2f __attribute__((ext_vector_type(2)));
typedef __bf16 bf16x2 __attribute__((ext_vector_type(2)));
typedef __bf16 bf16x8 __attribute__((ext_vector_type(8)));
typedef __amdgpu_buffer_rsrc_t rsrc_t;
typedef unsigned long long u64;
#define LAS __attribute__((address_space(3)))
#define GAS __attribute__((address_space(1)))

constexpr int D = 512, HQ = 32, HKV = 8, SK = 8192, DH = 128, F = 1792;
constexpr int AUX_NT = 2, AUX_SC1 = 16;
constexpr int KV_BYTES = 2 * HKV * SK * DH * 2;
constexpr float LOG2E = 1.4426950408889634f;
constexpr float SC2 = 0.08838834764831845f * LOG2E;
constexpr unsigned SPIN_CAP = 1u << 18;

constexpr int TLAST = 1068;
constexpr int TBASE = 30, TBIG = 125;
constexpr int STAG = 4;
constexpr int KFRONT = 2, NSH = 13, QPOS = 24, BURST = 2, PV0 = 28;
constexpr int WIN = 4, WINB = 5;
constexpr int T1THR = 125, T1NOM = 85;
constexpr int TWAIT = 300, QWAIT = 500, HWAIT = 650;
constexpr int GAIN = 80;
constexpr int WOD0 = 30, WOD1 = 60;
constexpr int DELTA = 32, GDELTA = 62, FDELTA = 26;

constexpr int WS_E = 0, WS_T0 = 128, R_QP = 256, R_HP = R_QP + 8 * 512, R_GP = R_HP + 4 * 512, R_LS = R_GP + 7 * 1024, R_R1 = R_LS + 16 * 128,
              R_SP = R_R1 + 64 * 512, WS_END = 16 << 17;
static_assert(R_SP + 64 * 512 <= (1 << 17), "record kinds fit one chunk");
template <int S> __device__ __forceinline__ int blk_at(int region, int j) { return (((j + (j >> 4)) & 15) << 17) + region + (j >> 4) * S; }

constexpr int PW = 192;
constexpr int L_O = 0, L_X = 4096, L_R1 = L_X + 2048, L_R2 = L_R1 + 4 * PW, L_Q = L_R2 + 4 * PW, L_ML = L_Q + 256, L_ON = L_ML + 64, L_CNT = L_ON + 32,
              L_END = L_CNT + 12;
enum { C_AUX, C_SP, C_Q, C_ML, C_ISSUED, C_G, W_T0, W_LATE, W_TAGA, W_TAGB, W_SHIFT, W_LATEST };
constexpr unsigned NO_SHIFT = 0x80000000u, NOT_YET = 0xffffffffu;

__device__ __forceinline__ rsrc_t mk(const void* p, int bytes) { return __builtin_amdgcn_make_buffer_rsrc((void*)p, 0, bytes, 0x00020000); }
template <int AUX> __device__ __forceinline__ v4u ld16(rsrc_t r, int voff, int soff) { return __builtin_amdgcn_raw_buffer_load_b128(r, voff, soff, AUX); }
template <int AUX> __device__ __forceinline__ v2u ld8(rsrc_t r, int voff, int soff) { return __builtin_amdgcn_raw_buffer_load_b64(r, voff, soff, AUX); }
__device__ __forceinline__ void st16(v4u v, rsrc_t r, int voff, int soff) { __builtin_amdgcn_raw_buffer_store_b128(v, r, voff, soff, AUX_SC1); }
__device__ __forceinline__ float asf(unsigned u) { return __builtin_bit_cast(float, u); }
__device__ __forceinline__ unsigned asu(float f) { return __builtin_bit_cast(unsigned, f); }
__device__ __forceinline__ unsigned uni(unsigned u) { return (unsigned)__builtin_amdgcn_readfirstlane((int)u); }
__device__ __forceinline__ unsigned now() { return (unsigned)__builtin_amdgcn_s_memrealtime(); }
__device__ __forceinline__ float lo16(unsigned u) { return asf(u << 16); }
__device__ __forceinline__ float hi16(unsigned u) { return asf(u & 0xffff0000u); }
__device__ __forceinline__ unsigned pk(float a, float b) { return __builtin_bit_cast(unsigned, bf16x2{(__bf16)a, (__bf16)b}); }
__device__ __forceinline__ float rbf(float a) { return (float)(__bf16)a; }
template <int C> __device__ __forceinline__ unsigned dppu(unsigned x) { return (unsigned)__builtin_amdgcn_mov_dpp((int)x, C, 0xf, 0xf, true); }
template <int C> __device__ __forceinline__ float dppf(float x) { return asf(dppu<C>(asu(x))); }
template <int I> __device__ __forceinline__ float to_bank(float old, float src) {
  return asf((unsigned)__builtin_amdgcn_update_dpp((int)asu(old), (int)asu(src), 0x110 + 4 * I, 0xf, 1 << I, false)); }
constexpr int XOR1 = 0xB1, XOR2 = 0x4E, HMIR = 0x141, SHL4 = 0x104, SHL8 = 0x108, ROR4 = 0x124, ROR8 = 0x128, ROR12 = 0x12C;
__device__ __forceinline__ float sw32_add(float a, float b) {
  auto t = __builtin_amdgcn_permlane32_swap(asu(a), asu(b), false, false); return asf(t[0]) + asf(t[1]); }
__device__ __forceinline__ float sw16_add(float a, float b) {
  auto t = __builtin_amdgcn_permlane16_swap(asu(a), asu(b), false, false); return asf(t[0]) + asf(t[1]); }
__device__ __forceinline__ float sw32_max(float a) {
  auto t = __builtin_amdgcn_permlane32_swap(asu(a), asu(a), false, false); return fmaxf(asf(t[0]), asf(t[1])); }
__device__ __forceinline__ float sw16_max(float a) {
  auto t = __builtin_amdgcn_permlane16_swap(asu(a), asu(a), false, false); return fmaxf(asf(t[0]), asf(t[1])); }
__device__ __forceinline__ v4f mfma32(v4u a, v4u b, v4f c) {
  return __builtin_amdgcn_mfma_f32_16x16x32_bf16(__builtin_bit_cast(bf16x8, a), __builtin_bit_cast(bf16x8, b), c, 0, 0, 0); }
__device__ __forceinline__ unsigned perm_lo(unsigned even, unsigned odd) { return __builtin_amdgcn_perm(odd, even, 0x05040100u); }
__device__ __forceinline__ unsigned perm_hi(unsigned even, unsigned odd) { return __builtin_amdgcn_perm(odd, even, 0x07060302u); }
__device__ __forceinline__ float exp2f_(float x) { return __builtin_amdgcn_exp2f(x); }
__device__ __forceinline__ float rcpf_(float x) { return __builtin_amdgcn_rcpf(x); }
#define SB() __builtin_amdgcn_sched_barrier(0)
#define VMWAIT(n) __builtin_amdgcn_s_waitcnt(0x0F70 | ((n) & 15) | (((n) >> 4) << 14))
#define LDS_BARRIER() do { SB(); asm volatile("s_waitcnt lgkmcnt(0)\n\ts_barrier" ::: "memory"); SB(); } while (0)
#define UNTIL(t0, t) do { while ((int)(now() - (t0)) < (int)(t)) {} SB(); } while (0)

struct Tag { unsigned a, b; };
__device__ __forceinline__ v4u rec(unsigned p0, unsigned p1, Tag t) { return v4u{p0, t.a, p1, t.b}; }
template <int N> __device__ __forceinline__ bool all_fresh(const v4u (&r)[N], Tag t) {
  bool ok = true;
#pragma unroll
  for (int k = 0; k < N; ++k) ok = ok & (r[k].y == t.a) & (r[k].w == t.b);
  return ok;
}
template <int N> __device__ __forceinline__ void request(v4u (&r)[N], const int (&off)[N], rsrc_t ws) {
#pragma unroll
  for (int k = 0; k < N; ++k) r[k] = ld16<AUX_SC1>(ws, off[k], 0);
}
template <int N, int SLEEP> __device__ __forceinline__ void await(v4u (&r)[N], const int (&off)[N], rsrc_t ws, Tag t) {
  bool ok = all_fresh<N>(r, t);
  if (__builtin_expect(!__all(ok), 0)) {
    unsigned spin = 0;
#pragma nounroll
    do {
      if (SLEEP) __builtin_amdgcn_s_sleep(SLEEP);
      asm volatile("" ::: "memory");
      if (!ok) request<N>(r, off, ws);
      ok = all_fresh<N>(r, t);
    } while (!__all(ok) && ++spin < SPIN_CAP);
  }
  asm volatile("" ::: "memory");
}
__device__ __forceinline__ unsigned lds_word(LAS unsigned* c) { return uni(__hip_atomic_load(c, __ATOMIC_RELAXED, __HIP_MEMORY_SCOPE_WORKGROUP)); }
__device__ __forceinline__ void lds_arrive(LAS unsigned* c, int lane) {
  asm volatile("s_waitcnt lgkmcnt(0)" ::: "memory");
  if (lane == 0) __hip_atomic_fetch_add(c, 1u, __ATOMIC_RELAXED, __HIP_MEMORY_SCOPE_WORKGROUP);
}
template <int SLEEP = 1> __device__ __forceinline__ void lds_wait(LAS unsigned* c, unsigned n) {
#pragma nounroll
  for (unsigned spin = 0; spin < (SPIN_CAP << 4); ++spin) {
    if (lds_word(c) >= n) break;
    if (SLEEP) __builtin_amdgcn_s_sleep(SLEEP);
  }
  asm volatile("" ::: "memory");
}

__device__ __forceinline__ float half_sum(float a, float b) {
  float x = sw32_add(a, b);
  x += dppf<XOR1>(x); x += dppf<XOR2>(x); x += dppf<HMIR>(x); x += dppf<ROR8>(x); return sw16_add(x, x); }
__device__ __forceinline__ void layer_norm(float (&u)[8], float (&v)[8]) {
  const float su = ((u[0] + u[1]) + (u[2] + u[3])) + ((u[4] + u[5]) + (u[6] + u[7]));
  const float sv = ((v[0] + v[1]) + (v[2] + v[3])) + ((v[4] + v[5]) + (v[6] + v[7]));
  const float mf = half_sum(su, sv) * (1.f / D);
  const float mu = asf(__builtin_amdgcn_readlane(asu(mf), 0)), mv = asf(__builtin_amdgcn_readlane(asu(mf), 32));
  float qu = 0.f, qv = 0.f;
#pragma unroll
  for (int j = 0; j < 8; ++j) { const float a = u[j] - mu, b = v[j] - mv; qu = fmaf(a, a, qu); qv = fmaf(b, b, qv); }
  const float var = rbf(half_sum(qu, qv) * (1.f / D));
  const float rs = rcpf_(rbf(__builtin_amdgcn_sqrtf(rbf(var + 1e-5f))));
  const float ru = asf(__builtin_amdgcn_readlane(asu(rs), 0)), rv = asf(__builtin_amdgcn_readlane(asu(rs), 32)), m0 = rbf(mu), m1 = rbf(mv);
#pragma unroll
  for (int j = 0; j < 8; ++j) { u[j] = rbf(rbf(u[j] - m0) * ru); v[j] = rbf(rbf(v[j] - m1) * rv); }
}

__device__ __forceinline__ v4u column_frag(const v2u (&w)[8], int j) {
  v4u a;
#pragma unroll
  for (int t = 0; t < 4; ++t) a[t] = (j & 1) ? perm_hi(w[2 * t][j >> 1], w[2 * t + 1][j >> 1]) : perm_lo(w[2 * t][j >> 1], w[2 * t + 1][j >> 1]);
  return a;
}
__device__ __forceinline__ v4u masked(v4u v, unsigned m) { return v4u{v[0] & m, v[1] & m, v[2] & m, v[3] & m}; }
__device__ __forceinline__ bool sum_products(v4f p, float* buf, int a, LAS unsigned* c, int lane, v2f& s) {
  if ((lane & 15) < 8) *(v4f*)(buf + a * PW + ((lane >> 2) & 1) * 96 + (lane & 3) * 24 + (lane >> 4) * 4) = p;
  asm volatile("s_waitcnt lgkmcnt(0)" ::: "memory");
  unsigned old = 0;
  if (lane == 0) old = __hip_atomic_fetch_add(c, 1u, __ATOMIC_RELAXED, __HIP_MEMORY_SCOPE_WORKGROUP);
  if (uni(old) != 3u) return false;
  asm volatile("" ::: "memory");
  const float* q = buf + (lane >> 5) * 96 + (lane & 1) * 48 + ((lane & 31) >> 1);
  s = (v2f{q[0], q[24]} + v2f{q[PW], q[PW + 24]}) + (v2f{q[2 * PW], q[2 * PW + 24]} + v2f{q[3 * PW], q[3 * PW + 24]});
  return true;
}

__global__ void __launch_bounds__(768, 3)
k_fused(const void*, const void*, const void*, const void*, const void*, const void*, const void*, const void*, const void*, void*, void*) {
  v16u ka; v4u kb; v2u kc2; u64 t_wave;
  asm volatile("s_memrealtime %3\n\ts_load_dwordx16 %0, %4, 0x0\n\ts_load_dwordx4 %1, %4, 0x40\n\ts_load_dwordx2 %2, %4, 0x50"
               : "=&s"(ka), "=&s"(kb), "=&s"(kc2), "=&s"(t_wave) : "s"(__builtin_amdgcn_kernarg_segment_ptr()));
  SB();
  const int tid = threadIdx.x, lane = tid & 63, wv = uni(tid >> 6);
  const int blk = blockIdx.x, x8 = blk & 7, i32 = blk >> 3, rg = i32 >> 3, k8 = i32 & 7, bat = i32 >> 4;
  const int g = lane >> 4, c16 = lane & 15, hg = x8 * 4 + rg, w = wv - 4;
  int kvbase = ((bat * HKV + x8) * SK + (i32 & 15) * 512) * 256 + w * 1024;
  int koff = ((lane >> 2) & 3) * 256 + g * 64 + (lane & 3) * 16;
  asm volatile("s_waitcnt lgkmcnt(0)" : "+s"(ka), "+s"(kb), "+s"(kc2), "+s"(t_wave), "+s"(kvbase), "+v"(koff));
#define ARG(v, i) ((void*)(((u64)uni((v)[2 * (i) + 1]) << 32) | uni((v)[2 * (i)])))
  const void *x = ARG(ka, 0), *kc = ARG(ka, 1), *vc = ARG(ka, 2), *wq = ARG(ka, 3), *wo = ARG(ka, 4), *w1 = ARG(ka, 5), *b1 = ARG(ka, 6), *w2 = ARG(ka, 7),
             *b2 = ARG(kb, 0);
  void *out = ARG(kb, 1), *wsp = ARG(kc2, 0);
  __shared__ __attribute__((aligned(16))) float lds[L_END];
  LAS unsigned* cnt = (LAS unsigned*)(lds + L_CNT);
  const int id2 = x8 * 14 + i32 - 18, rb = id2 >> 3, c2 = id2 & 7;
  const bool isq = i32 < 16, big = i32 < 23, is2 = i32 >= 18;
  const rsrc_t rk = mk(kc, KV_BYTES);
  v4u K[16];
  int wv2 = wv;
  asm volatile("" : "+s"(wv2));
#pragma unroll
  for (int n = 0; n < KFRONT; ++n) asm volatile("" : "=v"(K[n]));
  if (wv2 >= 4) {
    K[0] = ld16<AUX_NT>(rk, koff, kvbase);
    SB();
    if (!big) {
#pragma unroll
      for (int n = 1; n < KFRONT; ++n) { K[n] = ld16<AUX_NT>(rk, koff, kvbase + n * 8192); SB(); }
    }
  } else if (tid < 12) {
    cnt[tid] = tid == W_T0 ? (unsigned)t_wave : tid == W_SHIFT ? NO_SHIFT : 0u;
  }
  SB();
  u64 pc;
  int code0, code1;
  asm volatile("s_getpc_b64 %0\n.Lk_fused_pc:\n\ts_mov_b32 %1, .Lk_fused_end0-.Lk_fused_pc\n\ts_mov_b32 %2, .Lk_fused_end1-.Lk_fused_pc"
               : "=s"(pc), "=s"(code0), "=s"(code1));
  LDS_BARRIER();
  const unsigned t_in = uni(cnt[W_T0]);
  const int lb = (lane >> 2) & 1;

  if (wv >= 4) {
    const rsrc_t rv = mk(vc, KV_BYTES);
    const int voff = lane * 16, stag = (7 - w) * STAG;
    v4u V[16];
    int tau = (TLAST - (big ? TBIG : TBASE)) * 16 / (32 - KFRONT), base = TLAST * 16 - 31 * tau;
#define PACE(n) do { UNTIL(t_in, ((base + (n) * tau) >> 4) - stag); VMWAIT(WINB - 1); if (!big) VMWAIT(WIN - 1); SB(); } while (0)
#define REPLAN(n, sh) do { const int end_ = (TLAST + (sh)) * 16; tau = (end_ - base - ((n) - 1) * tau) / (32 - (n)); base = end_ - 31 * tau; } while (0)
    if (big) VMWAIT(0); else VMWAIT(KFRONT - 1);
    SB();
    int late = (int)(now() - t_in);
    late = late > T1THR ? min(late - (late < 180 ? T1NOM : T1NOM + 15), 400) : 0;
    base += late * 16;
    if (tid == 256) cnt[W_LATE] = (unsigned)(late + 1);
    if (big) {
      UNTIL(t_in, TBIG + late);
#pragma unroll
      for (int n = 1; n < KFRONT; ++n) { K[n] = ld16<AUX_NT>(rk, koff, kvbase + n * 8192); SB(); }
    }
#pragma unroll
    for (int n = KFRONT; n < QPOS; ++n) {
      if (n == NSH) { const unsigned sh = lds_word(cnt + W_SHIFT); if (sh != NO_SHIFT) REPLAN(n, (int)sh); }
      if (n < QPOS - BURST) PACE(n);
      if (n < 16) K[n] = ld16<AUX_NT>(rk, koff, kvbase + n * 8192);
      else V[n - 16] = ld16<AUX_NT>(rv, voff, kvbase + (n - 16) * 8192);
      SB();
    }
    lds_wait(cnt + C_Q, 3u);
    const int bank = (lane >> 2) & 3, hh = lane & 3;
    const v4u qf = *(const v4u*)(lds + L_Q + hh * 64 + g * 16 + bank * 4);
    const v4u lt = *(const v4u*)(lds + L_CNT + W_TAGA);
    v4f d[16];
#pragma unroll
    for (int n = 0; n < 16; ++n) d[n] = mfma32(K[n], qf, v4f{0.f, 0.f, 0.f, 0.f});
    SB();
    float S[4], sn[16];
#pragma unroll
    for (int n = 0; n < 16; ++n) { d[n][0] += dppf<SHL4>(d[n][1]); d[n][2] += dppf<SHL4>(d[n][3]); }
    SB();
#pragma unroll
    for (int n = 0; n < 16; ++n) sn[n] = d[n][0] + dppf<SHL8>(d[n][2]);
    SB();
#pragma unroll
    for (int z = 0; z < 4; ++z) S[z] = to_bank<1>(sn[4 * z], sn[4 * z + 1]);
#pragma unroll
    for (int z = 0; z < 4; ++z) S[z] = to_bank<2>(S[z], sn[4 * z + 2]);
#pragma unroll
    for (int z = 0; z < 4; ++z) S[z] = to_bank<3>(S[z], sn[4 * z + 3]);
    SB();
    float mw = fmaxf(fmaxf(S[0], S[1]), fmaxf(S[2], S[3]));
    mw = fmaxf(mw, dppf<ROR4>(mw)); mw = fmaxf(mw, dppf<ROR8>(mw)); mw = sw32_max(sw16_max(mw));
    mw *= SC2;
    float lw = 0.f;
#pragma unroll
    for (int z = 0; z < 4; ++z) { S[z] = exp2f_(fmaf(S[z], SC2, -mw)); lw += S[z]; }
    lw += dppf<ROR4>(lw); lw += dppf<ROR8>(lw); lw = sw16_add(lw, lw); lw = sw32_add(lw, lw);
    if (lane < 4) *(v2f*)(lds + L_ML + lane * 16 + w * 2) = v2f{mw, lw};
    lds_arrive(cnt + C_ML, lane);
    REPLAN(QPOS, (int)uni(lt.z));
    unsigned P[4], bm[4];
#pragma unroll
    for (int z = 0; z < 4; ++z) { P[z] = pk(S[z], dppf<SHL4>(S[z])); bm[z] = bank == z ? 0xffffffffu : 0u; }
    v4u Bf[2][4];
#pragma unroll
    for (int be = 0; be < 2; ++be) {
      const unsigned p0 = P[2 * be], p1 = P[2 * be + 1];
      Bf[be][0] = masked(v4u{p0, dppu<ROR8>(p0), p1, dppu<ROR8>(p1)}, bm[0]);
      Bf[be][1] = masked(v4u{dppu<ROR4>(p0), dppu<ROR12>(p0), dppu<ROR4>(p1), dppu<ROR12>(p1)}, bm[1]);
      Bf[be][2] = masked(v4u{dppu<ROR8>(p0), p0, dppu<ROR8>(p1), p1}, bm[2]);
      Bf[be][3] = masked(v4u{dppu<ROR12>(p0), dppu<ROR4>(p0), dppu<ROR12>(p1), dppu<ROR4>(p1)}, bm[3]);
    }
    SB();
    v4f acc[2] = {v4f{0.f, 0.f, 0.f, 0.f}, v4f{0.f, 0.f, 0.f, 0.f}};
#pragma unroll
    for (int be = 0; be < 2; ++be) {
#pragma unroll
      for (int n = be ? PV0 : QPOS; n < (be ? 32 : PV0); ++n) { PACE(n); V[n - 16] = ld16<AUX_NT>(rv, voff, kvbase + (n - 16) * 8192); SB(); }
      if (be) lds_arrive(cnt + C_ISSUED, lane);
      v4u A[8];
#pragma unroll
      for (int t = 0; t < 4; ++t) {
#pragma unroll
        for (int u = 0; u < 4; ++u) {
          A[2 * u][t] = perm_lo(V[8 * be + 2 * t][u], V[8 * be + 2 * t + 1][u]);
          A[2 * u + 1][t] = perm_hi(V[8 * be + 2 * t][u], V[8 * be + 2 * t + 1][u]);
        }
        SB();
      }
#pragma unroll
      for (int u = 0; u < 4; ++u) { acc[0] = mfma32(A[2 * u], Bf[be][u], acc[0]); acc[1] = mfma32(A[2 * u + 1], Bf[be][u], acc[1]); }
      SB();
    }
    *(v4f*)(lds + L_O + w * 512 + lane * 4) = acc[0];
    *(v4f*)(lds + L_O + w * 512 + 256 + lane * 4) = acc[1];
    LDS_BARRIER();
    if (w == 7) {
      const unsigned t_ba = now();
      const Tag tag = {uni(lt.x), uni(lt.y)};
      const rsrc_t rws = mk(wsp, WS_END);
      const int gq = (i32 >> 2) & 3, jq = i32 & 3, fcol = x8 * 64 + gq * 16 + g * 4 + jq;
      const unsigned xo = __builtin_amdgcn_raw_buffer_load_b16(mk(x, 2 * D * 2), fcol * 2, bat * D * 2, 0);
      const unsigned bias2 = __builtin_amdgcn_raw_buffer_load_b16(mk(b2, D * 2), fcol * 2, 0, 0);
      SB();
      v4u fg[1], fs[2];
      const int go[1] = {blk_at<1024>(R_GP, (c16 < 14 ? c16 : 0) * 8 + x8) + (bat * 32 + gq * 8 + g * 2 + (jq >> 1)) * 16};
      int so[2] = {blk_at<512>(R_SP, lane * 8 + x8) + (bat * 16 + gq * 4 + jq) * 16, blk_at<512>(R_SP, (lane + 64) * 8 + x8) + (bat * 16 + gq * 4 + jq) * 16};
      UNTIL(t_ba, GDELTA);
      request<1>(fg, go, rws);
      await<1, 6>(fg, go, rws, tag);
      float gs = c16 < 14 ? asf((jq & 1) ? fg[0].z : fg[0].x) : 0.f;
      gs += dppf<XOR1>(gs); gs += dppf<XOR2>(gs); gs += dppf<HMIR>(gs); gs += dppf<ROR8>(gs);
      const float mv = rbf(rbf(gs) + lo16(bias2));
      float gv = rbf(mv * rcpf_(1.f + exp2f_(-LOG2E * mv))), xv = lo16(xo);
      int ooff = c16 == 0 ? (bat * D + fcol) * 2 : 2 * D * 2;
      asm volatile("" : "+v"(gv), "+v"(xv), "+v"(ooff), "+v"(so[0]), "+v"(so[1]));
      lds_wait<0>(cnt + C_SP, 4u);
      const unsigned t_sp = now();
      UNTIL(t_sp, FDELTA);
      request<2>(fs, so, rws);
      await<2, 0>(fs, so, rws, tag);
      float sc = sw16_add(sw32_add(lo16(fs[0].x) + lo16(fs[1].x), lo16(fs[0].z) + lo16(fs[1].z)),
                          sw32_add(hi16(fs[0].x) + hi16(fs[1].x), hi16(fs[0].z) + hi16(fs[1].z)));
      sc += dppf<XOR1>(sc); sc += dppf<XOR2>(sc); sc += dppf<HMIR>(sc); sc += dppf<ROR8>(sc);
      const float ov = xv + rbf(sc * gv);
      __builtin_amdgcn_raw_buffer_store_b16((unsigned short)pk(ov, ov), mk(out, 2 * D * 2), ooff, 0, 0);
      if (blk == 0 && lane == 0) __builtin_amdgcn_raw_buffer_store_b64(v2u{tag.a, tag.b}, rws, 0, WS_E, AUX_SC1);
      asm volatile(".Lk_fused_end0:");
    }
    return;
  }

  const int a = wv;
  __builtin_amdgcn_s_setprio(2);
  const rsrc_t rws = mk(wsp, WS_END);
  unsigned mj[4];
#pragma unroll
  for (int j = 0; j < 4; ++j) mj[j] = (lane & 11) == j ? 0xffffffffu : 0u;
  const v2u ep = ld8<AUX_SC1>(rws, 0, WS_E);
  unsigned touch = 0;
  if (a == 3 && i32 == 31) {
    const char* tp = lane < 2 ? (const char*)vc + (size_t)(lane * HKV + x8) * (SK * 256) + 65536
                   : lane == 2 ? (const char*)wo + x8 * 512 * 1024 + 4096
                   : lane < 5 ? (const char*)w2 + ((x8 * 14 + (lane - 3) * 13) >> 3) * 131072 + x8 * 128
                   : lane == 5 ? (const char*)b1 : (const char*)b2;
    if (lane < 7) touch = *(const GAS unsigned*)tp;
  }
  Tag tag;
  if (big) {
    const rsrc_t rx = mk(x, 2 * D * 2);
    const v4u xa0 = ld16<0>(rx, lane * 16, 0), xa1 = ld16<0>(rx, lane * 16, D * 2);
    const int ld = isq ? HQ * DH * 2 : F * 2, idb = isq ? x8 * 16 + k8 * 2 + rg : x8 * 7 + i32 - 16, row0 = (idb & 1) * 256 + a * 64;
    const rsrc_t rw = mk(isq ? wq : w1, D * ld);
    v2u wA[2][8];
    const int woff = g * 8 * ld + c16 * 8;
#pragma unroll
    for (int t = 0; t < 2; ++t)
#pragma unroll
      for (int i = 0; i < 8; ++i) wA[t][i] = ld8<AUX_NT>(rw, woff, (row0 + t * 32 + i) * ld + (idb >> 1) * 128);
    SB();
    tag = Tag{uni(ep.x) + 1u, uni(ep.y) + 0x9E3779B9u};
    float u[8], v[8];
#pragma unroll
    for (int t = 0; t < 4; ++t) { u[2 * t] = lo16(xa0[t]); u[2 * t + 1] = hi16(xa0[t]); v[2 * t] = lo16(xa1[t]); v[2 * t + 1] = hi16(xa1[t]); }
#pragma nounroll
    for (int pass = isq ? 1 : 0; pass < 2; ++pass) layer_norm(u, v);
    float* xs = lds + L_X + a * 512;
    *(v4u*)(xs + lane * 4) = v4u{pk(u[0], u[1]), pk(u[2], u[3]), pk(u[4], u[5]), pk(u[6], u[7])};
    *(v4u*)(xs + 256 + lane * 4) = v4u{pk(v[0], v[1]), pk(v[2], v[3]), pk(v[4], v[5]), pk(v[6], v[7])};
    asm volatile("s_waitcnt lgkmcnt(0)" ::: "memory");
    v4f acc[4];
#pragma unroll
    for (int t = 0; t < 2; ++t) {
      const v4u in = *(const v4u*)(xs + lb * 256 + (row0 >> 1) + (t * 4 + g) * 4);
#pragma unroll
      for (int j = 0; j < 4; ++j) acc[j] = mfma32(column_frag(wA[t], j), in, t ? acc[j] : v4f{0.f, 0.f, 0.f, 0.f});
    }
    v2f sum;
    if (sum_products((lane & 2) ? ((lane & 1) ? acc[3] : acc[2]) : ((lane & 1) ? acc[1] : acc[0]), lds + L_R1, a, cnt + C_AUX, lane, sum)) {
      const unsigned pp = pk(sum.x, sum.y);
      st16(rec(pp, dppu<XOR1>(pp), tag), rws, (lane & 1) ? WS_END : lane * 8, blk_at<512>(isq ? R_QP : R_HP, idb));
    }
  } else {
    tag = Tag{uni(ep.x) + 1u, uni(ep.y) + 0x9E3779B9u};
  }
  if (a == 3 && blk >= 248) {
    lds_wait(cnt + W_LATE, 1u);
    const unsigned ts = t_in + lds_word(cnt + W_LATE);
    if (lane == 0) st16(rec(ts, ts, tag), rws, x8 * 16, WS_T0);
  }
  const unsigned code_pf = __builtin_amdgcn_raw_buffer_load_b32(mk((const void*)(pc & ~(u64)127), max(code0, code1) + (int)(pc & 127)), lane * 128, (a & 1) * 8192, 0);
  if (a < 2) {
    v4u qr[2];
    const int qo[2] = {blk_at<512>(R_QP, (x8 * 8 + a * 4 + g) * 2) + (bat * 16 + c16) * 16, blk_at<512>(R_QP, (x8 * 8 + a * 4 + g) * 2 + 1) + (bat * 16 + c16) * 16};
    UNTIL(t_in, QWAIT + lds_word(cnt + W_LATE));
    request<2>(qr, qo, rws);
    await<2, 16>(qr, qo, rws, tag);
    *(v2u*)(lds + L_Q + (a * 64 + lane) * 2) = v2u{pk(lo16(qr[0].x) + lo16(qr[1].x), hi16(qr[0].x) + hi16(qr[1].x)),
                                                   pk(lo16(qr[0].z) + lo16(qr[1].z), hi16(qr[0].z) + hi16(qr[1].z))};
    lds_arrive(cnt + C_Q, lane);
  }
  if (a == 2) {
    v4u tr[1];
    const int to[1] = {WS_T0 + (lane & 7) * 16};
    UNTIL(t_in, TWAIT + lds_word(cnt + W_LATE));
    request<1>(tr, to, rws);
    await<1, 16>(tr, to, rws, tag);
    int sh = min(max((int)(tr[0].x - t_in), -400), 400), mx = sh;
    sh += (int)dppu<XOR1>(sh); sh += (int)dppu<XOR2>(sh); sh += (int)dppu<HMIR>(sh);
    mx = max(mx, (int)dppu<XOR1>(mx)); mx = max(mx, (int)dppu<XOR2>(mx)); mx = max(mx, (int)dppu<HMIR>(mx));
    sh = max(sh >> 3, mx - GAIN);
    if (lane == 0) *(v4u*)(lds + L_CNT + W_TAGA) = v4u{tag.a, tag.b, (unsigned)sh, (unsigned)mx};
    lds_arrive(cnt + C_Q, lane);
  }
  asm volatile("" :: "v"(code_pf), "v"(touch));
  v2f gpart = {0.f, 0.f};
  bool gpub = false;
  if (is2) {
    const rsrc_t r2 = mk(w2, D * F * 2);
    v2u wB[8];
    v4u hr[2];
    const int hoff = (lb * 16 + (a & 1) * 8 + g * 2 + ((lane >> 3) & 1)) * 16;
    const int ho[2] = {blk_at<512>(R_HP, (2 * rb + (a >> 1)) * 2) + hoff, blk_at<512>(R_HP, (2 * rb + (a >> 1)) * 2 + 1) + hoff};
    lds_wait(cnt + C_Q, 3u);
    UNTIL(t_in, HWAIT + (int)lds_word(cnt + W_LATEST));
    request<2>(hr, ho, rws);
    const v2u bias1 = ld8<0>(mk(b1, F * 2), g * 16 + ((lane >> 3) & 1) * 8, (rb * 128 + a * 32) * 2);
#pragma unroll
    for (int i = 0; i < 8; ++i) wB[i] = ld8<AUX_NT>(r2, g * 8 * 1024 + c16 * 8 + (i & 3) * 1024, (rb * 128 + a * 32 + (i & 4)) * 1024 + c2 * 128);
    SB();
    await<2, 16>(hr, ho, rws, tag);
    float h[4] = {lo16(hr[0].x) + lo16(hr[1].x), hi16(hr[0].x) + hi16(hr[1].x), lo16(hr[0].z) + lo16(hr[1].z), hi16(hr[0].z) + hi16(hr[1].z)};
    const float bb[4] = {lo16(bias1.x), hi16(bias1.x), lo16(bias1.y), hi16(bias1.y)};
    constexpr float G0 = -2.f * 0.7978845608028654f * LOG2E, G1 = G0 * 0.044715f;
#pragma unroll
    for (int j = 0; j < 4; ++j) {
      const float t = rbf(rbf(h[j]) + bb[j]);
      h[j] = t * rcpf_(1.f + exp2f_(t * fmaf(G1, t * t, G0)));
    }
    const unsigned h0 = pk(h[0], h[1]), h1 = pk(h[2], h[3]);
    const v4u in = {h0, h1, dppu<ROR8>(h0), dppu<ROR8>(h1)};
    v4f p = {0.f, 0.f, 0.f, 0.f};
#pragma unroll
    for (int j = 0; j < 4; ++j) p = mfma32(column_frag(wB, j), masked(in, mj[j]), p);
    gpub = sum_products(p, lds + L_R2, a, cnt + C_G, lane, gpart);
  }
  lds_wait(cnt + C_ML, 8u);
  if (a == 3 && lane < 32) ((unsigned*)lds)[L_ON + lane] = NOT_YET;
  const int mh = (lane >> 1) & 3;
  float fw[8];
  {
    v4f ml[4];
#pragma unroll
    for (int t = 0; t < 4; ++t) ml[t] = *(const v4f*)(lds + L_ML + mh * 16 + 4 * t);
    const float M = fmaxf(fmaxf(fmaxf(ml[0][0], ml[0][2]), fmaxf(ml[1][0], ml[1][2])), fmaxf(fmaxf(ml[2][0], ml[2][2]), fmaxf(ml[3][0], ml[3][2])));
    float L = 0.f;
#pragma unroll
    for (int t = 0; t < 4; ++t) { fw[2 * t] = exp2f_(ml[t][0] - M); fw[2 * t + 1] = exp2f_(ml[t][2] - M); L = fmaf(fw[2 * t], ml[t][1], fmaf(fw[2 * t + 1], ml[t][3], L)); }
    const float il = rcpf_(L);
#pragma unroll
    for (int t = 0; t < 8; ++t) fw[t] *= il;
    const float lse = M + __builtin_amdgcn_logf(L);
    asm volatile("" : "+v"(fw[0]), "+v"(fw[1]), "+v"(fw[2]), "+v"(fw[3]), "+v"(fw[4]), "+v"(fw[5]), "+v"(fw[6]), "+v"(fw[7]));
    if (a == 0 && lane < 8) st16(rec(asu(lse), asu(lse), tag), rws, lane * 16, blk_at<128>(R_LS, x8 * 32 + i32));
  }
  const int dq = k8 >> 1, ch = k8 & 1;
  v4u lr[1], pr[2];
  const int lo[1] = {blk_at<128>(R_LS, x8 * 32 + (a & 1) * 16 + (lane >> 3) + (lane & 1) * 8) + rg * 32};
  int po[2] = {blk_at<512>(R_R1, hg * 32 + (a & 1) * 16 + (lane >> 3)) + dq * 128 + (lane & 7) * 16,
               blk_at<512>(R_R1, hg * 32 + (a & 1) * 16 + (lane >> 3) + 8) + dq * 128 + (lane & 7) * 16};
  int on_w = (L_ON + a * 16 + (lane & 7) * 2 + (lane >> 5)) * 4, on_r = (L_ON + lb * 16 + g * 4) * 4;
  int soff = c16 < 8 ? blk_at<512>(R_SP, (hg * 4 + dq) * 8 + ch * 4 + a) + (lb * 16 + g * 4 + (lane & 3)) * 16 : WS_END;
  int mrd = (L_O + (a * 16 + ((lane >> 3) & 3) * 4 + mh) * 4 + (lane >> 5) * 2 + (lane & 1)) * 4;
  int moff = (lane & 8) ? WS_END : blk_at<512>(R_R1, (x8 * 4 + mh) * 32 + i32) + (a * 8 + ((lane >> 5) * 2 + (lane & 1)) * 2 + ((lane >> 4) & 1)) * 16;
  asm volatile("" : "+v"(moff), "+v"(mrd), "+v"(po[0]), "+v"(po[1]), "+v"(on_w), "+v"(on_r), "+v"(soff), "+v"(mj[0]), "+v"(mj[1]), "+v"(mj[2]), "+v"(mj[3]));
  SB();
  const rsrc_t ro = mk(wo, D * HQ * DH * 2), rgather = mk(wsp, a < 2 ? WS_END : 0);
  const int shift = (int)lds_word(cnt + W_SHIFT);
  v2u wO[8];
  lds_wait(cnt + C_ISSUED, 8u);
  UNTIL(t_in, TLAST + WOD0 + shift);
  request<1>(lr, lo, rgather);
#pragma unroll
  for (int i = 0; i < 8; ++i) {
    if (i == 4) UNTIL(t_in, TLAST + WOD1 + shift);
    wO[i] = ld8<AUX_NT>(ro, g * 8 * 1024 + c16 * 8 + (i & 3) * 1024, (hg * 128 + dq * 32 + (i & 4)) * 1024 + ch * 512 + a * 128);
    SB();
  }
  LDS_BARRIER();
  {
    const LAS float* mp = (const LAS float*)((LAS char*)lds + mrd);
    v2f t[8];
#pragma unroll
    for (int ww = 0; ww < 8; ++ww) t[ww] = v2f{mp[ww * 512], mp[ww * 512 + 256]};
    const v2f s = (fw[0] * t[0] + fw[1] * t[1] + fw[2] * t[2] + fw[3] * t[3]) + (fw[4] * t[4] + fw[5] * t[5] + fw[6] * t[6] + fw[7] * t[7]);
    const unsigned p = pk(s.x, s.y);
    st16(rec(p, dppu<ROR8>(p), tag), rws, moff, 0);
    if (gpub) st16(rec(asu(gpart.x), asu(gpart.y), tag), rws, lane * 16, blk_at<1024>(R_GP, rb * 8 + c2));
  }
  SB();
  const unsigned t_pub = now();
  float wt0 = 0.f, wt1 = 0.f;
  if (a < 2) {
    await<1, 6>(lr, lo, rws, tag);
    const float ls = asf(lr[0].x);
    float M = fmaxf(ls, dppf<XOR1>(ls));
    M = fmaxf(M, dppf<ROR8>(M)); M = sw32_max(sw16_max(M));
    const float e = exp2f_(ls - M), ex = dppf<XOR1>(e);
    float z = e + ex;
    z += dppf<ROR8>(z); z = sw16_add(z, z); z = sw32_add(z, z);
    const float iz = rcpf_(z);
    wt0 = ((lane & 1) ? ex : e) * iz; wt1 = ((lane & 1) ? e : ex) * iz;
    asm volatile("" : "+v"(wt0), "+v"(wt1));
    UNTIL(t_pub, DELTA);
  }
  asm volatile("" :: "v"(lr[0]));
  SB();
  request<2>(pr, po, rgather);
  SB();
  v4u wf[4];
#pragma unroll
  for (int j = 0; j < 4; ++j) wf[j] = column_frag(wO, j);
  asm volatile("" : "+v"(wf[0]), "+v"(wf[1]), "+v"(wf[2]), "+v"(wf[3]));
  if (a < 2) {
    await<2, 0>(pr, po, rws, tag);
    const float o0 = fmaf(wt0, lo16(pr[0].x), wt1 * lo16(pr[1].x)), o1 = fmaf(wt0, hi16(pr[0].x), wt1 * hi16(pr[1].x)),
                o2 = fmaf(wt0, lo16(pr[0].z), wt1 * lo16(pr[1].z)), o3 = fmaf(wt0, hi16(pr[0].z), wt1 * hi16(pr[1].z));
    float oo = sw16_add(sw32_add(o0, o2), sw32_add(o1, o3));
    oo += dppf<ROR8>(oo);
    const auto t = __builtin_amdgcn_permlane16_swap(asu(oo), asu(oo), false, false);
    const unsigned ob = pk(asf(t[0]), asf(t[1]));
    *(LAS unsigned*)((LAS char*)lds + on_w) = ob == NOT_YET ? 0x7fc07fc0u : ob;
  }
  asm volatile("" :: "v"(pr[0]), "v"(pr[1]));
  v4u in;
#pragma nounroll
  for (unsigned spin = 0; spin < (SPIN_CAP << 6); ++spin) {
    asm volatile("" ::: "memory");
    in = *(const LAS v4u*)((LAS char*)lds + on_r);
    if (__all(max(max(in[0], in[1]), max(in[2], in[3])) != NOT_YET)) break;
  }
  v4u bj[4];
#pragma unroll
  for (int j = 0; j < 4; ++j) bj[j] = masked(in, mj[j]);
  SB();
  v4f sp = {0.f, 0.f, 0.f, 0.f};
#pragma unroll
  for (int j = 0; j < 4; ++j) sp = mfma32(wf[j], bj[j], sp);
  st16(rec(pk(sp[0], sp[1]), pk(sp[2], sp[3]), tag), rws, soff, 0);
  lds_arrive(cnt + C_SP, lane);
  asm volatile(".Lk_fused_end1:");
}
}

extern "C" void kernel_launch(void* const* d_in, const int* in_sizes, int n_in, void* d_out, int out_size, void* d_ws, size_t ws_size,
                              hipStream_t stream) {
  fused::k_fused<<<256, 768, 0, stream>>>(d_in[0], d_in[1], d_in[2], d_in[3], d_in[4], d_in[6], d_in[7], d_in[8], d_in[9], d_out, d_ws);
}
```

```cpp
#define PROBE 1
#include <hip/hip_runtime.h>
#include <hip/hip_bf16.h>
#include <stdint.h>

namespace fused {
typedef unsigned v16u __attribute__((ext_vector_type(16)));
typedef unsigned v4u __attribute__((ext_vector_type(4)));
typedef unsigned v2u __attribute__((ext_vector_type(2)));
typedef float v4f __attribute__((ext_vector_type(4)));
typedef float v2f __attribute__((ext_vector_type(2)));
typedef __bf16 bf16x2 __attribute__((ext_vector_type(2)));
typedef __bf16 bf16x8 __attribute__((ext_vector_type(8)));
typedef __amdgpu_buffer_rsrc_t rsrc_t;
typedef unsigned long long u64;
#define LAS __attribute__((address_space(3)))
#define GAS __attribute__((address_space(1)))

constexpr int D = 512, HQ = 32, HKV = 8, SK = 8192, DH = 128, F = 1792;
constexpr int AUX_NT = 2, AUX_SC1 = 16;
constexpr int KV_BYTES = 2 * HKV * SK * DH * 2;
constexpr float LOG2E = 1.4426950408889634f;
constexpr float SC2 = 0.08838834764831845f * LOG2E;
constexpr unsigned SPIN_CAP = 1u << 18;

#define TLAST 1068
constexpr int TBASE = 30, TBIG = 125;
constexpr int STAG = 4;
constexpr int KFRONT = 2, NSH = 13, QPOS = 24, BURST = 2, PV0 = 28;
constexpr int WIN = 4, WINB = 5;
constexpr int T1THR = 125, T1NOM = 85;
constexpr int TWAIT = 300, QWAIT = 500, HWAIT = 650;
constexpr int GAIN = 80;
constexpr int WOD0 = 30, WOD1 = 60;
constexpr int DELTA = 36, GDELTA = 64, FDELTA = 18;
constexpr int TFINAL = 222;

constexpr int WS_E = 0, WS_T0 = 128, R_QP = 256, R_HP = R_QP + 8 * 512, R_GP = R_HP + 4 * 512, R_LS = R_GP + 7 * 1024, R_R1 = R_LS + 16 * 128,
              R_SP = R_R1 + 64 * 512, WS_END = 16 << 17;
static_assert(R_SP + 64 * 512 <= (1 << 17), "record kinds fit one chunk");
template <int S> __device__ __forceinline__ int blk_at(int region, int j) { return (((j + (j >> 4)) & 15) << 17) + region + (j >> 4) * S; }

constexpr int PW = 192;
constexpr int L_O = 0, L_X = 4096, L_R1 = L_X + 2048, L_R2 = L_R1 + 4 * PW, L_Q = L_R2 + 4 * PW, L_ML = L_Q + 256, L_ON = L_ML + 64, L_CNT = L_ON + 32,
              L_END = L_CNT + 12;
enum { C_AUX, C_SP, C_Q, C_ML, C_ISSUED, C_G, W_T0, W_LATE, W_TAGA, W_TAGB, W_SHIFT, W_LATEST };
constexpr unsigned NO_SHIFT = 0x80000000u, NOT_YET = 0xffffffffu;

__device__ __forceinline__ rsrc_t mk(const void* p, int bytes) { return __builtin_amdgcn_make_buffer_rsrc((void*)p, 0, bytes, 0x00020000); }
template <int AUX> __device__ __forceinline__ v4u ld16(rsrc_t r, int voff, int soff) { return __builtin_amdgcn_raw_buffer_load_b128(r, voff, soff, AUX); }
template <int AUX> __device__ __forceinline__ v2u ld8(rsrc_t r, int voff, int soff) { return __builtin_amdgcn_raw_buffer_load_b64(r, voff, soff, AUX); }
__device__ __forceinline__ void st16(v4u v, rsrc_t r, int voff, int soff) { __builtin_amdgcn_raw_buffer_store_b128(v, r, voff, soff, AUX_SC1); }
__device__ __forceinline__ float asf(unsigned u) { return __builtin_bit_cast(float, u); }
__device__ __forceinline__ unsigned asu(float f) { return __builtin_bit_cast(unsigned, f); }
__device__ __forceinline__ unsigned uni(unsigned u) { return (unsigned)__builtin_amdgcn_readfirstlane((int)u); }
__device__ __forceinline__ unsigned now() { return (unsigned)__builtin_amdgcn_s_memrealtime(); }
__device__ __forceinline__ float lo16(unsigned u) { return asf(u << 16); }
__device__ __forceinline__ float hi16(unsigned u) { return asf(u & 0xffff0000u); }
__device__ __forceinline__ unsigned pk(float a, float b) { return __builtin_bit_cast(unsigned, bf16x2{(__bf16)a, (__bf16)b}); }
__device__ __forceinline__ float rbf(float a) { return (float)(__bf16)a; }
template <int C> __device__ __forceinline__ unsigned dppu(unsigned x) { return (unsigned)__builtin_amdgcn_mov_dpp((int)x, C, 0xf, 0xf, true); }
template <int C> __device__ __forceinline__ float dppf(float x) { return asf(dppu<C>(asu(x))); }
template <int I> __device__ __forceinline__ float to_bank(float old, float src) {
  return asf((unsigned)__builtin_amdgcn_update_dpp((int)asu(old), (int)asu(src), 0x110 + 4 * I, 0xf, 1 << I, false)); }
constexpr int XOR1 = 0xB1, XOR2 = 0x4E, HMIR = 0x141, SHL4 = 0x104, SHL8 = 0x108, ROR4 = 0x124, ROR8 = 0x128, ROR12 = 0x12C;
__device__ __forceinline__ float sw32_add(float a, float b) {
  auto t = __builtin_amdgcn_permlane32_swap(asu(a), asu(b), false, false); return asf(t[0]) + asf(t[1]); }
__device__ __forceinline__ float sw16_add(float a, float b) {
  auto t = __builtin_amdgcn_permlane16_swap(asu(a), asu(b), false, false); return asf(t[0]) + asf(t[1]); }
__device__ __forceinline__ float sw32_max(float a) {
  auto t = __builtin_amdgcn_permlane32_swap(asu(a), asu(a), false, false); return fmaxf(asf(t[0]), asf(t[1])); }
__device__ __forceinline__ float sw16_max(float a) {
  auto t = __builtin_amdgcn_permlane16_swap(asu(a), asu(a), false, false); return fmaxf(asf(t[0]), asf(t[1])); }
__device__ __forceinline__ v4f mfma32(v4u a, v4u b, v4f c) {
  return __builtin_amdgcn_mfma_f32_16x16x32_bf16(__builtin_bit_cast(bf16x8, a), __builtin_bit_cast(bf16x8, b), c, 0, 0, 0); }
__device__ __forceinline__ unsigned perm_lo(unsigned even, unsigned odd) { return __builtin_amdgcn_perm(odd, even, 0x05040100u); }
__device__ __forceinline__ unsigned perm_hi(unsigned even, unsigned odd) { return __builtin_amdgcn_perm(odd, even, 0x07060302u); }
__device__ __forceinline__ float exp2f_(float x) { return __builtin_amdgcn_exp2f(x); }
__device__ __forceinline__ float rcpf_(float x) { return __builtin_amdgcn_rcpf(x); }
#define SB() __builtin_amdgcn_sched_barrier(0)
#define VMWAIT(n) __builtin_amdgcn_s_waitcnt(0x0F70 | ((n) & 15) | (((n) >> 4) << 14))
#define LDS_BARRIER() do { SB(); asm volatile("s_waitcnt lgkmcnt(0)\n\ts_barrier" ::: "memory"); SB(); } while (0)
#define UNTIL(t0, t) do { while ((int)(now() - (t0)) < (int)(t)) {} SB(); } while (0)
#define JITTER(i)
#define STAMP_END()

struct Tag { unsigned a, b; };
__device__ __forceinline__ v4u rec(unsigned p0, unsigned p1, Tag t) { return v4u{p0, t.a, p1, t.b}; }
template <int N> __device__ __forceinline__ bool all_fresh(const v4u (&r)[N], Tag t) {
  bool ok = true;
#pragma unroll
  for (int k = 0; k < N; ++k) ok = ok & (r[k].y == t.a) & (r[k].w == t.b);
  return ok;
}
template <int N> __device__ __forceinline__ void request(v4u (&r)[N], const int (&off)[N], rsrc_t ws) {
#pragma unroll
  for (int k = 0; k < N; ++k) r[k] = ld16<AUX_SC1>(ws, off[k], 0);
}
template <int N, int SLEEP> __device__ __forceinline__ void await(v4u (&r)[N], const int (&off)[N], rsrc_t ws, Tag t) {
  bool ok = all_fresh<N>(r, t);
  if (__builtin_expect(!__all(ok), 0)) {
    unsigned spin = 0;
#pragma nounroll
    do {
      if (SLEEP) __builtin_amdgcn_s_sleep(SLEEP);
      asm volatile("" ::: "memory");
      if (!ok) request<N>(r, off, ws);
      ok = all_fresh<N>(r, t);
    } while (!__all(ok) && ++spin < SPIN_CAP);
  }
  asm volatile("" ::: "memory");
}
__device__ __forceinline__ unsigned lds_word(LAS unsigned* c) { return uni(__hip_atomic_load(c, __ATOMIC_RELAXED, __HIP_MEMORY_SCOPE_WORKGROUP)); }
__device__ __forceinline__ void lds_arrive(LAS unsigned* c, int lane) {
  asm volatile("s_waitcnt lgkmcnt(0)" ::: "memory");
  if (lane == 0) __hip_atomic_fetch_add(c, 1u, __ATOMIC_RELAXED, __HIP_MEMORY_SCOPE_WORKGROUP);
}
template <int SLEEP = 1> __device__ __forceinline__ void lds_wait(LAS unsigned* c, unsigned n) {
#pragma nounroll
  for (unsigned spin = 0; spin < (SPIN_CAP << 4); ++spin) {
    if (lds_word(c) >= n) break;
    if (SLEEP) __builtin_amdgcn_s_sleep(SLEEP);
  }
  asm volatile("" ::: "memory");
}

__device__ __forceinline__ float half_sum(float a, float b) {
  float x = sw32_add(a, b);
  x += dppf<XOR1>(x); x += dppf<XOR2>(x); x += dppf<HMIR>(x); x += dppf<ROR8>(x); return sw16_add(x, x); }
__device__ __forceinline__ void layer_norm(float (&u)[8], float (&v)[8]) {
  const float su = ((u[0] + u[1]) + (u[2] + u[3])) + ((u[4] + u[5]) + (u[6] + u[7]));
  const float sv = ((v[0] + v[1]) + (v[2] + v[3])) + ((v[4] + v[5]) + (v[6] + v[7]));
  const float mf = half_sum(su, sv) * (1.f / D);
  const float mu = asf(__builtin_amdgcn_readlane(asu(mf), 0)), mv = asf(__builtin_amdgcn_readlane(asu(mf), 32));
  float qu = 0.f, qv = 0.f;
#pragma unroll
  for (int j = 0; j < 8; ++j) { const float a = u[j] - mu, b = v[j] - mv; qu = fmaf(a, a, qu); qv = fmaf(b, b, qv); }
  const float var = rbf(half_sum(qu, qv) * (1.f / D));
  const float rs = rcpf_(rbf(__builtin_amdgcn_sqrtf(rbf(var + 1e-5f))));
  const float ru = asf(__builtin_amdgcn_readlane(asu(rs), 0)), rv = asf(__builtin_amdgcn_readlane(asu(rs), 32)), m0 = rbf(mu), m1 = rbf(mv);
#pragma unroll
  for (int j = 0; j < 8; ++j) { u[j] = rbf(rbf(u[j] - m0) * ru); v[j] = rbf(rbf(v[j] - m1) * rv); }
}

__device__ __forceinline__ v4u column_frag(const v2u (&w)[8], int j) {
  v4u a;
#pragma unroll
  for (int t = 0; t < 4; ++t) a[t] = (j & 1) ? perm_hi(w[2 * t][j >> 1], w[2 * t + 1][j >> 1]) : perm_lo(w[2 * t][j >> 1], w[2 * t + 1][j >> 1]);
  return a;
}
__device__ __forceinline__ v4u masked(v4u v, unsigned m) { return v4u{v[0] & m, v[1] & m, v[2] & m, v[3] & m}; }
__device__ __forceinline__ bool sum_products(v4f p, float* buf, int a, LAS unsigned* c, int lane, v2f& s) {
  if ((lane & 15) < 8) *(v4f*)(buf + a * PW + ((lane >> 2) & 1) * 96 + (lane & 3) * 24 + (lane >> 4) * 4) = p;
  asm volatile("s_waitcnt lgkmcnt(0)" ::: "memory");
  unsigned old = 0;
  if (lane == 0) old = __hip_atomic_fetch_add(c, 1u, __ATOMIC_RELAXED, __HIP_MEMORY_SCOPE_WORKGROUP);
  if (uni(old) != 3u) return false;
  asm volatile("" ::: "memory");
  const float* q = buf + (lane >> 5) * 96 + (lane & 1) * 48 + ((lane & 31) >> 1);
  s = (v2f{q[0], q[24]} + v2f{q[PW], q[PW + 24]}) + (v2f{q[2 * PW], q[2 * PW + 24]} + v2f{q[3 * PW], q[3 * PW + 24]});
  return true;
}

__global__ void __launch_bounds__(768, 3)
k_fused(const void*, const void*, const void*, const void*, const void*, const void*, const void*, const void*, const void*, void*, void*) {
  v16u ka; v4u kb; v2u kc2; u64 t_wave;
  asm volatile("s_memrealtime %3\n\ts_load_dwordx16 %0, %4, 0x0\n\ts_load_dwordx4 %1, %4, 0x40\n\ts_load_dwordx2 %2, %4, 0x50"
               : "=&s"(ka), "=&s"(kb), "=&s"(kc2), "=&s"(t_wave) : "s"(__builtin_amdgcn_kernarg_segment_ptr()));
  SB();
  const int tid = threadIdx.x, lane = tid & 63, wv = uni(tid >> 6);
  const int blk = blockIdx.x, x8 = blk & 7, i32 = blk >> 3, rg = i32 >> 3, k8 = i32 & 7, bat = i32 >> 4;
  const int g = lane >> 4, c16 = lane & 15, hg = x8 * 4 + rg, w = wv - 4;
  int kvbase = ((bat * HKV + x8) * SK + (i32 & 15) * 512) * 256 + w * 1024;
  int koff = ((lane >> 2) & 3) * 256 + g * 64 + (lane & 3) * 16;
  asm volatile("s_waitcnt lgkmcnt(0)" : "+s"(ka), "+s"(kb), "+s"(kc2), "+s"(t_wave), "+s"(kvbase), "+v"(koff));
#define ARG(v, i) ((void*)(((u64)uni((v)[2 * (i) + 1]) << 32) | uni((v)[2 * (i)])))
  const void *x = ARG(ka, 0), *kc = ARG(ka, 1), *vc = ARG(ka, 2), *wq = ARG(ka, 3), *wo = ARG(ka, 4), *w1 = ARG(ka, 5), *b1 = ARG(ka, 6), *w2 = ARG(ka, 7),
             *b2 = ARG(kb, 0);
  void *out = ARG(kb, 1), *wsp = ARG(kc2, 0);
  __shared__ __attribute__((aligned(16))) float lds[L_END];
  LAS unsigned* cnt = (LAS unsigned*)(lds + L_CNT);
  const int id2 = x8 * 14 + i32 - 18, rb = id2 >> 3, c2 = id2 & 7;
  const bool isq = i32 < 16, big = i32 < 23, is2 = i32 >= 18;
  unsigned t_ev;
  if (PROBE == 1) {
    unsigned sv; const char* sp_ = (const char*)vc + (kvbase + 4096 + 65536);
    asm volatile("s_load_dword %0, %1, 0x0\n\ts_waitcnt lgkmcnt(0)" : "=s"(sv) : "s"(sp_) : "memory");
    SB(); t_ev = now(); SB();
  } else {
    UNTIL((unsigned)t_wave, 200);
    const unsigned pv = __builtin_amdgcn_raw_buffer_load_b32(mk(vc, KV_BYTES), 0, kvbase + 4096 + 65536, 0);
    asm volatile("" :: "v"(pv)); SB(); t_ev = now(); SB();
  }
  const rsrc_t rk = mk(kc, KV_BYTES);
  v4u K[16];
  int wv2 = wv;
  asm volatile("" : "+s"(wv2));
#pragma unroll
  for (int n = 0; n < KFRONT; ++n) asm volatile("" : "=v"(K[n]));
  if (wv2 >= 4) {
    K[0] = ld16<AUX_NT>(rk, koff, kvbase);
    SB();
    if (!big) {
#pragma unroll
      for (int n = 1; n < KFRONT; ++n) { K[n] = ld16<AUX_NT>(rk, koff, kvbase + n * 8192); SB(); }
    }
  } else if (tid < 12) {
    cnt[tid] = tid == W_T0 ? (unsigned)t_wave : tid == W_SHIFT ? NO_SHIFT : 0u;
  }
  SB();
  u64 pc;
  int code0, code1;
  asm volatile("s_getpc_b64 %0\n.Lk_fused_pc:\n\ts_mov_b32 %1, .Lk_fused_end0-.Lk_fused_pc\n\ts_mov_b32 %2, .Lk_fused_end1-.Lk_fused_pc"
               : "=s"(pc), "=s"(code0), "=s"(code1));
  LDS_BARRIER();
  const unsigned t_in = uni(cnt[W_T0]);
  const int lb = (lane >> 2) & 1;
  JITTER(0);

  if (wv >= 4) {
    const rsrc_t rv = mk(vc, KV_BYTES);
    const int voff = lane * 16, stag = (7 - w) * STAG;
    v4u V[16];
    int tau = (TLAST - (big ? TBIG : TBASE)) * 16 / (32 - KFRONT), base = TLAST * 16 - 31 * tau;
#define PACE(n) do { UNTIL(t_in, ((base + (n) * tau) >> 4) - stag); VMWAIT(WINB - 1); if (!big) VMWAIT(WIN - 1); SB(); } while (0)
#define REPLAN(n, sh) do { const int end_ = (TLAST + min(max(sh, -400), 400)) * 16; tau = (end_ - base - ((n) - 1) * tau) / (32 - (n)); base = end_ - 31 * tau; } while (0)
    if (big) VMWAIT(0); else VMWAIT(KFRONT - 1);
    SB();
    int late = (int)(now() - t_in);
    late = late > T1THR ? min(late - (late < 180 ? T1NOM : T1NOM + 15), 400) : 0;
    base += late * 16;
    if (tid == 256) cnt[W_LATE] = (unsigned)(late + 1);
    if (big) {
      UNTIL(t_in, TBIG + late);
#pragma unroll
      for (int n = 1; n < KFRONT; ++n) { K[n] = ld16<AUX_NT>(rk, koff, kvbase + n * 8192); SB(); }
    }
#pragma unroll
    for (int n = KFRONT; n < QPOS; ++n) {
      if (n == NSH) { const unsigned sh = lds_word(cnt + W_SHIFT); if (sh != NO_SHIFT) REPLAN(n, (int)sh); }
      if (n < QPOS - BURST) PACE(n);
      if (n < 16) K[n] = ld16<AUX_NT>(rk, koff, kvbase + n * 8192);
      else V[n - 16] = ld16<AUX_NT>(rv, voff, kvbase + (n - 16) * 8192);
      SB();
    }
    JITTER(1);
    lds_wait(cnt + C_Q, 3u);
    const int bank = (lane >> 2) & 3, hh = lane & 3;
    const v4u qf = *(const v4u*)(lds + L_Q + hh * 64 + g * 16 + bank * 4);
    const v4u lt = *(const v4u*)(lds + L_CNT + W_TAGA);
    v4f d[16];
#pragma unroll
    for (int n = 0; n < 16; ++n) d[n] = mfma32(K[n], qf, v4f{0.f, 0.f, 0.f, 0.f});
    SB();
    float S[4], sn[16];
#pragma unroll
    for (int n = 0; n < 16; ++n) { d[n][0] += dppf<SHL4>(d[n][1]); d[n][2] += dppf<SHL4>(d[n][3]); }
    SB();
#pragma unroll
    for (int n = 0; n < 16; ++n) sn[n] = d[n][0] + dppf<SHL8>(d[n][2]);
    SB();
#pragma unroll
    for (int z = 0; z < 4; ++z) S[z] = to_bank<1>(sn[4 * z], sn[4 * z + 1]);
#pragma unroll
    for (int z = 0; z < 4; ++z) S[z] = to_bank<2>(S[z], sn[4 * z + 2]);
#pragma unroll
    for (int z = 0; z < 4; ++z) S[z] = to_bank<3>(S[z], sn[4 * z + 3]);
    SB();
    float mw = fmaxf(fmaxf(S[0], S[1]), fmaxf(S[2], S[3]));
    mw = fmaxf(mw, dppf<ROR4>(mw)); mw = fmaxf(mw, dppf<ROR8>(mw)); mw = sw32_max(sw16_max(mw));
    mw *= SC2;
    float lw = 0.f;
#pragma unroll
    for (int z = 0; z < 4; ++z) { S[z] = exp2f_(fmaf(S[z], SC2, -mw)); lw += S[z]; }
    lw += dppf<ROR4>(lw); lw += dppf<ROR8>(lw); lw = sw16_add(lw, lw); lw = sw32_add(lw, lw);
    if (lane < 4) *(v2f*)(lds + L_ML + lane * 16 + w * 2) = v2f{mw, lw};
    lds_arrive(cnt + C_ML, lane);
    REPLAN(QPOS, (int)uni(lt.z));
    unsigned P[4], bm[4];
#pragma unroll
    for (int z = 0; z < 4; ++z) { P[z] = pk(S[z], dppf<SHL4>(S[z])); bm[z] = bank == z ? 0xffffffffu : 0u; }
    v4u Bf[2][4];
#pragma unroll
    for (int be = 0; be < 2; ++be) {
      const unsigned p0 = P[2 * be], p1 = P[2 * be + 1];
      Bf[be][0] = masked(v4u{p0, dppu<ROR8>(p0), p1, dppu<ROR8>(p1)}, bm[0]);
      Bf[be][1] = masked(v4u{dppu<ROR4>(p0), dppu<ROR12>(p0), dppu<ROR4>(p1), dppu<ROR12>(p1)}, bm[1]);
      Bf[be][2] = masked(v4u{dppu<ROR8>(p0), p0, dppu<ROR8>(p1), p1}, bm[2]);
      Bf[be][3] = masked(v4u{dppu<ROR12>(p0), dppu<ROR4>(p0), dppu<ROR12>(p1), dppu<ROR4>(p1)}, bm[3]);
    }
    SB();
    v4f acc[2] = {v4f{0.f, 0.f, 0.f, 0.f}, v4f{0.f, 0.f, 0.f, 0.f}};
#pragma unroll
    for (int be = 0; be < 2; ++be) {
#pragma unroll
      for (int n = be ? PV0 : QPOS; n < (be ? 32 : PV0); ++n) { PACE(n); V[n - 16] = ld16<AUX_NT>(rv, voff, kvbase + (n - 16) * 8192); SB(); }
      if (be) lds_arrive(cnt + C_ISSUED, lane);
      v4u A[8];
#pragma unroll
      for (int t = 0; t < 4; ++t) {
#pragma unroll
        for (int u = 0; u < 4; ++u) {
          A[2 * u][t] = perm_lo(V[8 * be + 2 * t][u], V[8 * be + 2 * t + 1][u]);
          A[2 * u + 1][t] = perm_hi(V[8 * be + 2 * t][u], V[8 * be + 2 * t + 1][u]);
        }
        SB();
      }
#pragma unroll
      for (int u = 0; u < 4; ++u) { acc[0] = mfma32(A[2 * u], Bf[be][u], acc[0]); acc[1] = mfma32(A[2 * u + 1], Bf[be][u], acc[1]); }
      SB();
    }
    JITTER(2);
    *(v4f*)(lds + L_O + w * 512 + lane * 4) = acc[0];
    *(v4f*)(lds + L_O + w * 512 + 256 + lane * 4) = acc[1];
    LDS_BARRIER();
    if (w == 7) {
      const unsigned t_ba = now();
      const Tag tag = {uni(lt.x), uni(lt.y)};
      const rsrc_t rws = mk(wsp, WS_END);
      const int gq = (i32 >> 2) & 3, jq = i32 & 3, fcol = x8 * 64 + gq * 16 + g * 4 + jq;
      const unsigned xo = __builtin_amdgcn_raw_buffer_load_b16(mk(x, 2 * D * 2), fcol * 2, bat * D * 2, 0);
      const unsigned bias2 = __builtin_amdgcn_raw_buffer_load_b16(mk(b2, D * 2), fcol * 2, 0, 0);
      SB();
      v4u fg[1], fs[2];
      const int go[1] = {blk_at<1024>(R_GP, (c16 < 14 ? c16 : 0) * 8 + x8) + (bat * 32 + gq * 8 + g * 2 + (jq >> 1)) * 16};
      int so[2] = {blk_at<512>(R_SP, lane * 8 + x8) + (bat * 16 + gq * 4 + jq) * 16, blk_at<512>(R_SP, (lane + 64) * 8 + x8) + (bat * 16 + gq * 4 + jq) * 16};
      UNTIL(t_ba, GDELTA);
      request<1>(fg, go, rws);
      await<1, 6>(fg, go, rws, tag);
      float gs = c16 < 14 ? asf((jq & 1) ? fg[0].z : fg[0].x) : 0.f;
      gs += dppf<XOR1>(gs); gs += dppf<XOR2>(gs); gs += dppf<HMIR>(gs); gs += dppf<ROR8>(gs);
      const float mv = rbf(rbf(gs) + lo16(bias2));
      float gv = rbf(mv * rcpf_(1.f + exp2f_(-LOG2E * mv))), xv = lo16(xo);
      int ooff = c16 == 0 ? (bat * D + fcol) * 2 : 2 * D * 2;
      asm volatile("" : "+v"(gv), "+v"(xv), "+v"(ooff), "+v"(so[0]), "+v"(so[1]));
      lds_wait<0>(cnt + C_SP, 4u);
      const unsigned t_sp = now();
      UNTIL(t_sp, FDELTA);
      UNTIL(t_in, TLAST + TFINAL + (int)uni(lt.z));
      request<2>(fs, so, rws);
      await<2, 0>(fs, so, rws, tag);
      float sc = sw16_add(sw32_add(lo16(fs[0].x) + lo16(fs[1].x), lo16(fs[0].z) + lo16(fs[1].z)),
                          sw32_add(hi16(fs[0].x) + hi16(fs[1].x), hi16(fs[0].z) + hi16(fs[1].z)));
      sc += dppf<XOR1>(sc); sc += dppf<XOR2>(sc); sc += dppf<HMIR>(sc); sc += dppf<ROR8>(sc);
      const float ov = xv + rbf(sc * gv);
      __builtin_amdgcn_raw_buffer_store_b16((unsigned short)pk(ov, ov), mk(out, 2 * D * 2), ooff, 0, 0);
      if (blk == 0 && lane == 0) __builtin_amdgcn_raw_buffer_store_b64(v2u{tag.a, tag.b}, rws, 0, WS_E, AUX_SC1);
      UNTIL(t_in, min(3000 + (int)(t_ev - t_in), 8000));
      asm volatile(".Lk_fused_end0:");
      STAMP_END();
    }
    return;
  }

  const int a = wv;
  __builtin_amdgcn_s_setprio(2);
  const rsrc_t rws = mk(wsp, WS_END);
  unsigned mj[4];
#pragma unroll
  for (int j = 0; j < 4; ++j) mj[j] = (lane & 11) == j ? 0xffffffffu : 0u;
  const v2u ep = ld8<AUX_SC1>(rws, 0, WS_E);
  unsigned touch = 0;
  if (a == 3 && i32 == 31) {
    const char* tp = lane < 2 ? (const char*)vc + (size_t)(lane * HKV + x8) * (SK * 256) + 65536
                   : lane == 2 ? (const char*)wo + x8 * 512 * 1024 + 4096
                   : lane < 5 ? (const char*)w2 + ((x8 * 14 + (lane - 3) * 13) >> 3) * 131072 + x8 * 128
                   : lane == 5 ? (const char*)b1 : (const char*)b2;
    if (lane < 7) touch = *(const GAS unsigned*)tp;
  }
  Tag tag;
  if (big) {
    const rsrc_t rx = mk(x, 2 * D * 2);
    const v4u xa0 = ld16<0>(rx, lane * 16, 0), xa1 = ld16<0>(rx, lane * 16, D * 2);
    const int ld = isq ? HQ * DH * 2 : F * 2, idb = isq ? x8 * 16 + k8 * 2 + rg : x8 * 7 + i32 - 16, row0 = (idb & 1) * 256 + a * 64;
    const rsrc_t rw = mk(isq ? wq : w1, D * ld);
    v2u wA[2][8];
    const int woff = g * 8 * ld + c16 * 8;
#pragma unroll
    for (int t = 0; t < 2; ++t)
#pragma unroll
      for (int i = 0; i < 8; ++i) wA[t][i] = ld8<AUX_NT>(rw, woff, (row0 + t * 32 + i) * ld + (idb >> 1) * 128);
    SB();
    tag = Tag{uni(ep.x) + 1u, uni(ep.y) + 0x9E3779B9u};
    float u[8], v[8];
#pragma unroll
    for (int t = 0; t < 4; ++t) { u[2 * t] = lo16(xa0[t]); u[2 * t + 1] = hi16(xa0[t]); v[2 * t] = lo16(xa1[t]); v[2 * t + 1] = hi16(xa1[t]); }
#pragma nounroll
    for (int pass = isq ? 1 : 0; pass < 2; ++pass) layer_norm(u, v);
    float* xs = lds + L_X + a * 512;
    *(v4u*)(xs + lane * 4) = v4u{pk(u[0], u[1]), pk(u[2], u[3]), pk(u[4], u[5]), pk(u[6], u[7])};
    *(v4u*)(xs + 256 + lane * 4) = v4u{pk(v[0], v[1]), pk(v[2], v[3]), pk(v[4], v[5]), pk(v[6], v[7])};
    asm volatile("s_waitcnt lgkmcnt(0)" ::: "memory");
    v4f acc[4];
#pragma unroll
    for (int t = 0; t < 2; ++t) {
      const v4u in = *(const v4u*)(xs + lb * 256 + (row0 >> 1) + (t * 4 + g) * 4);
#pragma unroll
      for (int j = 0; j < 4; ++j) acc[j] = mfma32(column_frag(wA[t], j), in, t ? acc[j] : v4f{0.f, 0.f, 0.f, 0.f});
    }
    JITTER(3);
    v2f sum;
    if (sum_products((lane & 2) ? ((lane & 1) ? acc[3] : acc[2]) : ((lane & 1) ? acc[1] : acc[0]), lds + L_R1, a, cnt + C_AUX, lane, sum)) {
      const unsigned pp = pk(sum.x, sum.y);
      st16(rec(pp, dppu<XOR1>(pp), tag), rws, (lane & 1) ? WS_END : lane * 8, blk_at<512>(isq ? R_QP : R_HP, idb));
    }
  } else {
    tag = Tag{uni(ep.x) + 1u, uni(ep.y) + 0x9E3779B9u};
  }
  if (a == 3 && blk >= 248) {
    lds_wait(cnt + W_LATE, 1u);
    const unsigned ts = t_in + lds_word(cnt + W_LATE);
    if (lane == 0) st16(rec(ts, ts, tag), rws, x8 * 16, WS_T0);
  }
  const unsigned code_pf = __builtin_amdgcn_raw_buffer_load_b32(mk((const void*)(pc & ~(u64)127), max(code0, code1) + (int)(pc & 127)), lane * 128, (a & 1) * 8192, 0);
  if (a < 2) {
    v4u qr[2];
    const int qo[2] = {blk_at<512>(R_QP, (x8 * 8 + a * 4 + g) * 2) + (bat * 16 + c16) * 16, blk_at<512>(R_QP, (x8 * 8 + a * 4 + g) * 2 + 1) + (bat * 16 + c16) * 16};
    UNTIL(t_in, QWAIT + lds_word(cnt + W_LATE));
    request<2>(qr, qo, rws);
    await<2, 16>(qr, qo, rws, tag);
    *(v2u*)(lds + L_Q + (a * 64 + lane) * 2) = v2u{pk(lo16(qr[0].x) + lo16(qr[1].x), hi16(qr[0].x) + hi16(qr[1].x)),
                                                   pk(lo16(qr[0].z) + lo16(qr[1].z), hi16(qr[0].z) + hi16(qr[1].z))};
    lds_arrive(cnt + C_Q, lane);
  }
  if (a == 2) {
    v4u tr[1];
    const int to[1] = {WS_T0 + (lane & 7) * 16};
    UNTIL(t_in, TWAIT + lds_word(cnt + W_LATE));
    request<1>(tr, to, rws);
    await<1, 16>(tr, to, rws, tag);
    int sh = min(max((int)(tr[0].x - t_in), -400), 400), mx = sh;
    sh += (int)dppu<XOR1>(sh); sh += (int)dppu<XOR2>(sh); sh += (int)dppu<HMIR>(sh);
    mx = max(mx, (int)dppu<XOR1>(mx)); mx = max(mx, (int)dppu<XOR2>(mx)); mx = max(mx, (int)dppu<HMIR>(mx));
    sh = max(sh >> 3, mx - GAIN);
    if (lane == 0) *(v4u*)(lds + L_CNT + W_TAGA) = v4u{tag.a, tag.b, (unsigned)sh, (unsigned)mx};
    lds_arrive(cnt + C_Q, lane);
  }
  asm volatile("" :: "v"(code_pf), "v"(touch));
  v2f gpart = {0.f, 0.f};
  bool gpub = false;
  if (is2) {
    const rsrc_t r2 = mk(w2, D * F * 2);
    v2u wB[8];
    v4u hr[2];
    const int hoff = (lb * 16 + (a & 1) * 8 + g * 2 + ((lane >> 3) & 1)) * 16;
    const int ho[2] = {blk_at<512>(R_HP, (2 * rb + (a >> 1)) * 2) + hoff, blk_at<512>(R_HP, (2 * rb + (a >> 1)) * 2 + 1) + hoff};
    lds_wait(cnt + C_Q, 3u);
    UNTIL(t_in, HWAIT + (int)lds_word(cnt + W_LATEST));
    request<2>(hr, ho, rws);
    const v2u bias1 = ld8<0>(mk(b1, F * 2), g * 16 + ((lane >> 3) & 1) * 8, (rb * 128 + a * 32) * 2);
#pragma unroll
    for (int i = 0; i < 8; ++i) wB[i] = ld8<AUX_NT>(r2, g * 8 * 1024 + c16 * 8 + (i & 3) * 1024, (rb * 128 + a * 32 + (i & 4)) * 1024 + c2 * 128);
    SB();
    await<2, 16>(hr, ho, rws, tag);
    float h[4] = {lo16(hr[0].x) + lo16(hr[1].x), hi16(hr[0].x) + hi16(hr[1].x), lo16(hr[0].z) + lo16(hr[1].z), hi16(hr[0].z) + hi16(hr[1].z)};
    const float bb[4] = {lo16(bias1.x), hi16(bias1.x), lo16(bias1.y), hi16(bias1.y)};
    constexpr float G0 = -2.f * 0.7978845608028654f * LOG2E, G1 = G0 * 0.044715f;
#pragma unroll
    for (int j = 0; j < 4; ++j) {
      const float t = rbf(rbf(h[j]) + bb[j]);
      h[j] = t * rcpf_(1.f + exp2f_(t * fmaf(G1, t * t, G0)));
    }
    const unsigned h0 = pk(h[0], h[1]), h1 = pk(h[2], h[3]);
    const v4u in = {h0, h1, dppu<ROR8>(h0), dppu<ROR8>(h1)};
    v4f p = {0.f, 0.f, 0.f, 0.f};
#pragma unroll
    for (int j = 0; j < 4; ++j) p = mfma32(column_frag(wB, j), masked(in, mj[j]), p);
    gpub = sum_products(p, lds + L_R2, a, cnt + C_G, lane, gpart);
  }
  lds_wait(cnt + C_ML, 8u);
  JITTER(4);
  if (a == 3 && lane < 32) ((unsigned*)lds)[L_ON + lane] = NOT_YET;
  const int mh = (lane >> 1) & 3;
  float fw[8];
  {
    v4f ml[4];
#pragma unroll
    for (int t = 0; t < 4; ++t) ml[t] = *(const v4f*)(lds + L_ML + mh * 16 + 4 * t);
    const float M = fmaxf(fmaxf(fmaxf(ml[0][0], ml[0][2]), fmaxf(ml[1][0], ml[1][2])), fmaxf(fmaxf(ml[2][0], ml[2][2]), fmaxf(ml[3][0], ml[3][2])));
    float L = 0.f;
#pragma unroll
    for (int t = 0; t < 4; ++t) { fw[2 * t] = exp2f_(ml[t][0] - M); fw[2 * t + 1] = exp2f_(ml[t][2] - M); L = fmaf(fw[2 * t], ml[t][1], fmaf(fw[2 * t + 1], ml[t][3], L)); }
    const float il = rcpf_(L);
#pragma unroll
    for (int t = 0; t < 8; ++t) fw[t] *= il;
    const float lse = M + __builtin_amdgcn_logf(L);
    asm volatile("" : "+v"(fw[0]), "+v"(fw[1]), "+v"(fw[2]), "+v"(fw[3]), "+v"(fw[4]), "+v"(fw[5]), "+v"(fw[6]), "+v"(fw[7]));
    if (a == 0 && lane < 8) st16(rec(asu(lse), asu(lse), tag), rws, lane * 16, blk_at<128>(R_LS, x8 * 32 + i32));
  }
  const int dq = k8 >> 1, ch = k8 & 1;
  v4u lr[1], pr[2];
  const int lo[1] = {blk_at<128>(R_LS, x8 * 32 + (a & 1) * 16 + (lane >> 3) + (lane & 1) * 8) + rg * 32};
  int po[2] = {blk_at<512>(R_R1, hg * 32 + (a & 1) * 16 + (lane >> 3)) + dq * 128 + (lane & 7) * 16,
               blk_at<512>(R_R1, hg * 32 + (a & 1) * 16 + (lane >> 3) + 8) + dq * 128 + (lane & 7) * 16};
  int on_w = (L_ON + a * 16 + (lane & 7) * 2 + (lane >> 5)) * 4, on_r = (L_ON + lb * 16 + g * 4) * 4;
  int soff = c16 < 8 ? blk_at<512>(R_SP, (hg * 4 + dq) * 8 + ch * 4 + a) + (lb * 16 + g * 4 + (lane & 3)) * 16 : WS_END;
  int mrd = (L_O + (a * 16 + ((lane >> 3) & 3) * 4 + mh) * 4 + (lane >> 5) * 2 + (lane & 1)) * 4;
  int moff = (lane & 8) ? WS_END : blk_at<512>(R_R1, (x8 * 4 + mh) * 32 + i32) + (a * 8 + ((lane >> 5) * 2 + (lane & 1)) * 2 + ((lane >> 4) & 1)) * 16;
  asm volatile("" : "+v"(moff), "+v"(mrd), "+v"(po[0]), "+v"(po[1]), "+v"(on_w), "+v"(on_r), "+v"(soff), "+v"(mj[0]), "+v"(mj[1]), "+v"(mj[2]), "+v"(mj[3]));
  SB();
  const rsrc_t ro = mk(wo, D * HQ * DH * 2), rgather = mk(wsp, a < 2 ? WS_END : 0);
  const int shift = (int)lds_word(cnt + W_SHIFT);
  v2u wO[8];
  lds_wait(cnt + C_ISSUED, 8u);
  UNTIL(t_in, TLAST + WOD0 + shift);
  request<1>(lr, lo, rgather);
#pragma unroll
  for (int i = 0; i < 8; ++i) {
    if (i == 4) UNTIL(t_in, TLAST + WOD1 + shift);
    wO[i] = ld8<AUX_NT>(ro, g * 8 * 1024 + c16 * 8 + (i & 3) * 1024, (hg * 128 + dq * 32 + (i & 4)) * 1024 + ch * 512 + a * 128);
    SB();
  }
  LDS_BARRIER();
  JITTER(5);
  {
    const LAS float* mp = (const LAS float*)((LAS char*)lds + mrd);
    v2f t[8];
#pragma unroll
    for (int ww = 0; ww < 8; ++ww) t[ww] = v2f{mp[ww * 512], mp[ww * 512 + 256]};
    const v2f s = (fw[0] * t[0] + fw[1] * t[1] + fw[2] * t[2] + fw[3] * t[3]) + (fw[4] * t[4] + fw[5] * t[5] + fw[6] * t[6] + fw[7] * t[7]);
    const unsigned p = pk(s.x, s.y);
    st16(rec(p, dppu<ROR8>(p), tag), rws, moff, 0);
    if (gpub) st16(rec(asu(gpart.x), asu(gpart.y), tag), rws, lane * 16, blk_at<1024>(R_GP, rb * 8 + c2));
  }
  SB();
  const unsigned t_pub = now();
  float wt0 = 0.f, wt1 = 0.f;
  if (a < 2) {
    await<1, 6>(lr, lo, rws, tag);
    const float ls = asf(lr[0].x);
    float M = fmaxf(ls, dppf<XOR1>(ls));
    M = fmaxf(M, dppf<ROR8>(M)); M = sw32_max(sw16_max(M));
    const float e = exp2f_(ls - M), ex = dppf<XOR1>(e);
    float z = e + ex;
    z += dppf<ROR8>(z); z = sw16_add(z, z); z = sw32_add(z, z);
    const float iz = rcpf_(z);
    wt0 = ((lane & 1) ? ex : e) * iz; wt1 = ((lane & 1) ? e : ex) * iz;
    asm volatile("" : "+v"(wt0), "+v"(wt1));
    UNTIL(t_pub, DELTA);
  }
  asm volatile("" :: "v"(lr[0]));
  SB();
  request<2>(pr, po, rgather);
  SB();
  v4u wf[4];
#pragma unroll
  for (int j = 0; j < 4; ++j) wf[j] = column_frag(wO, j);
  asm volatile("" : "+v"(wf[0]), "+v"(wf[1]), "+v"(wf[2]), "+v"(wf[3]));
  if (a < 2) {
    await<2, 0>(pr, po, rws, tag);
    const float o0 = fmaf(wt0, lo16(pr[0].x), wt1 * lo16(pr[1].x)), o1 = fmaf(wt0, hi16(pr[0].x), wt1 * hi16(pr[1].x)),
                o2 = fmaf(wt0, lo16(pr[0].z), wt1 * lo16(pr[1].z)), o3 = fmaf(wt0, hi16(pr[0].z), wt1 * hi16(pr[1].z));
    float oo = sw16_add(sw32_add(o0, o2), sw32_add(o1, o3));
    oo += dppf<ROR8>(oo);
    const auto t = __builtin_amdgcn_permlane16_swap(asu(oo), asu(oo), false, false);
    const unsigned ob = pk(asf(t[0]), asf(t[1]));
    *(LAS unsigned*)((LAS char*)lds + on_w) = ob == NOT_YET ? 0x7fc07fc0u : ob;
  }
  asm volatile("" :: "v"(pr[0]), "v"(pr[1]));
  v4u in;
#pragma nounroll
  for (unsigned spin = 0; spin < (SPIN_CAP << 6); ++spin) {
    asm volatile("" ::: "memory");
    in = *(const LAS v4u*)((LAS char*)lds + on_r);
    if (__all(max(max(in[0], in[1]), max(in[2], in[3])) != NOT_YET)) break;
  }
  v4u bj[4];
#pragma unroll
  for (int j = 0; j < 4; ++j) bj[j] = masked(in, mj[j]);
  SB();
  v4f sp = {0.f, 0.f, 0.f, 0.f};
#pragma unroll
  for (int j = 0; j < 4; ++j) sp = mfma32(wf[j], bj[j], sp);
  JITTER(6);
  st16(rec(pk(sp[0], sp[1]), pk(sp[2], sp[3]), tag), rws, soff, 0);
  lds_arrive(cnt + C_SP, lane);
  asm volatile(".Lk_fused_end1:");
}
}

extern "C" void kernel_launch(void* const* d_in, const int* in_sizes, int n_in, void* d_out, int out_size, void* d_ws, size_t ws_size,
                              hipStream_t stream) {
  fused::k_fused<<<256, 768, 0, stream>>>(d_in[0], d_in[1], d_in[2], d_in[3], d_in[4], d_in[6], d_in[7], d_in[8], d_in[9], d_out, d_ws);
}
```

```cpp
#include <hip/hip_runtime.h>
#include <hip/hip_bf16.h>
#include <stdint.h>

namespace fused {
typedef unsigned v16u __attribute__((ext_vector_type(16)));
typedef unsigned v4u __attribute__((ext_vector_type(4)));
typedef unsigned v2u __attribute__((ext_vector_type(2)));
typedef float v4f __attribute__((ext_vector_type(4)));
typedef float v2f __attribute__((ext_vector_type(2)));
typedef __bf16 bf16x2 __attribute__((ext_vector_type(2)));
typedef __bf16 bf16x8 __attribute__((ext_vector_type(8)));
typedef __amdgpu_buffer_rsrc_t rsrc_t;
typedef unsigned long long u64;
#define LAS __attribute__((address_space(3)))
#define GAS __attribute__((address_space(1)))

constexpr int D = 512, HQ = 32, HKV = 8, SK = 8192, DH = 128, F = 1792;
constexpr int AUX_NT = 2, AUX_SC1 = 16;
constexpr int KV_BYTES = 2 * HKV * SK * DH * 2;
constexpr float LOG2E = 1.4426950408889634f;
constexpr float SC2 = 0.08838834764831845f * LOG2E;
constexpr unsigned SPIN_CAP = 1u << 18;

constexpr int TLAST = 1068;
constexpr int TBASE = 30, TBIG = 125;
constexpr int STAG = 4;
constexpr int KFRONT = 2, NSH = 13, QPOS = 24, BURST = 2, PV0 = 28;
constexpr int WIN = 4, WINB = 5;
constexpr int T1THR = 125, T1NOM = 85;
constexpr int TWAIT = 300, QWAIT = 500, HWAIT = 650;
constexpr int GAIN = 80;
constexpr int WOD0 = 30, WOD1 = 60;
constexpr int DELTA = 36, GDELTA = 64, FDELTA = 18;
constexpr int TFINAL = 222;

constexpr int WS_E = 0, WS_T0 = 128, R_QP = 256, R_HP = R_QP + 8 * 512, R_GP = R_HP + 4 * 512, R_LS = R_GP + 7 * 1024, R_R1 = R_LS + 16 * 128,
              R_SP = R_R1 + 64 * 512, WS_END = 16 << 17;
static_assert(R_SP + 64 * 512 <= (1 << 17), "record kinds fit one chunk");
template <int S> __device__ __forceinline__ int blk_at(int region, int j) { return (((j + (j >> 4)) & 15) << 17) + region + (j >> 4) * S; }

constexpr int PW = 192;
constexpr int L_O = 0, L_X = 4096, L_R1 = L_X + 2048, L_R2 = L_R1 + 4 * PW, L_Q = L_R2 + 4 * PW, L_ML = L_Q + 256, L_ON = L_ML + 64, L_CNT = L_ON + 32,
              L_END = L_CNT + 12;
enum { C_AUX, C_SP, C_Q, C_ML, C_ISSUED, C_G, W_T0, W_LATE, W_TAGA, W_TAGB, W_SHIFT, W_LATEST };
constexpr unsigned NO_SHIFT = 0x80000000u, NOT_YET = 0xffffffffu;

__device__ __forceinline__ rsrc_t mk(const void* p, int bytes) { return __builtin_amdgcn_make_buffer_rsrc((void*)p, 0, bytes, 0x00020000); }
template <int AUX> __device__ __forceinline__ v4u ld16(rsrc_t r, int voff, int soff) { return __builtin_amdgcn_raw_buffer_load_b128(r, voff, soff, AUX); }
template <int AUX> __device__ __forceinline__ v2u ld8(rsrc_t r, int voff, int soff) { return __builtin_amdgcn_raw_buffer_load_b64(r, voff, soff, AUX); }
__device__ __forceinline__ void st16(v4u v, rsrc_t r, int voff, int soff) { __builtin_amdgcn_raw_buffer_store_b128(v, r, voff, soff, AUX_SC1); }
__device__ __forceinline__ float asf(unsigned u) { return __builtin_bit_cast(float, u); }
__device__ __forceinline__ unsigned asu(float f) { return __builtin_bit_cast(unsigned, f); }
__device__ __forceinline__ unsigned uni(unsigned u) { return (unsigned)__builtin_amdgcn_readfirstlane((int)u); }
__device__ __forceinline__ unsigned now() { return (unsigned)__builtin_amdgcn_s_memrealtime(); }
__device__ __forceinline__ float lo16(unsigned u) { return asf(u << 16); }
__device__ __forceinline__ float hi16(unsigned u) { return asf(u & 0xffff0000u); }
__device__ __forceinline__ unsigned pk(float a, float b) { return __builtin_bit_cast(unsigned, bf16x2{(__bf16)a, (__bf16)b}); }
__device__ __forceinline__ float rbf(float a) { return (float)(__bf16)a; }
template <int C> __device__ __forceinline__ unsigned dppu(unsigned x) { return (unsigned)__builtin_amdgcn_mov_dpp((int)x, C, 0xf, 0xf, true); }
template <int C> __device__ __forceinline__ float dppf(float x) { return asf(dppu<C>(asu(x))); }
template <int I> __device__ __forceinline__ float to_bank(float old, float src) {
  return asf((unsigned)__builtin_amdgcn_update_dpp((int)asu(old), (int)asu(src), 0x110 + 4 * I, 0xf, 1 << I, false)); }
constexpr int XOR1 = 0xB1, XOR2 = 0x4E, HMIR = 0x141, SHL4 = 0x104, SHL8 = 0x108, ROR4 = 0x124, ROR8 = 0x128, ROR12 = 0x12C;
__device__ __forceinline__ float sw32_add(float a, float b) {
  auto t = __builtin_amdgcn_permlane32_swap(asu(a), asu(b), false, false); return asf(t[0]) + asf(t[1]); }
__device__ __forceinline__ float sw16_add(float a, float b) {
  auto t = __builtin_amdgcn_permlane16_swap(asu(a), asu(b), false, false); return asf(t[0]) + asf(t[1]); }
__device__ __forceinline__ float sw32_max(float a) {
  auto t = __builtin_amdgcn_permlane32_swap(asu(a), asu(a), false, false); return fmaxf(asf(t[0]), asf(t[1])); }
__device__ __forceinline__ float sw16_max(float a) {
  auto t = __builtin_amdgcn_permlane16_swap(asu(a), asu(a), false, false); return fmaxf(asf(t[0]), asf(t[1])); }
__device__ __forceinline__ v4f mfma32(v4u a, v4u b, v4f c) {
  return __builtin_amdgcn_mfma_f32_16x16x32_bf16(__builtin_bit_cast(bf16x8, a), __builtin_bit_cast(bf16x8, b), c, 0, 0, 0); }
__device__ __forceinline__ unsigned perm_lo(unsigned even, unsigned odd) { return __builtin_amdgcn_perm(odd, even, 0x05040100u); }
__device__ __forceinline__ unsigned perm_hi(unsigned even, unsigned odd) { return __builtin_amdgcn_perm(odd, even, 0x07060302u); }
__device__ __forceinline__ float exp2f_(float x) { return __builtin_amdgcn_exp2f(x); }
__device__ __forceinline__ float rcpf_(float x) { return __builtin_amdgcn_rcpf(x); }
#define SB() __builtin_amdgcn_sched_barrier(0)
#define VMWAIT(n) __builtin_amdgcn_s_waitcnt(0x0F70 | ((n) & 15) | (((n) >> 4) << 14))
#define LDS_BARRIER() do { SB(); asm volatile("s_waitcnt lgkmcnt(0)\n\ts_barrier" ::: "memory"); SB(); } while (0)
#define UNTIL(t0, t) do { while ((int)(now() - (t0)) < (int)(t)) {} SB(); } while (0)

struct Tag { unsigned a, b; };
__device__ __forceinline__ v4u rec(unsigned p0, unsigned p1, Tag t) { return v4u{p0, t.a, p1, t.b}; }
template <int N> __device__ __forceinline__ bool all_fresh(const v4u (&r)[N], Tag t) {
  bool ok = true;
#pragma unroll
  for (int k = 0; k < N; ++k) ok = ok & (r[k].y == t.a) & (r[k].w == t.b);
  return ok;
}
template <int N> __device__ __forceinline__ void request(v4u (&r)[N], const int (&off)[N], rsrc_t ws) {
#pragma unroll
  for (int k = 0; k < N; ++k) r[k] = ld16<AUX_SC1>(ws, off[k], 0);
}
template <int N, int SLEEP> __device__ __forceinline__ void await(v4u (&r)[N], const int (&off)[N], rsrc_t ws, Tag t) {
  bool ok = all_fresh<N>(r, t);
  if (__builtin_expect(!__all(ok), 0)) {
    unsigned spin = 0;
#pragma nounroll
    do {
      if (SLEEP) __builtin_amdgcn_s_sleep(SLEEP);
      asm volatile("" ::: "memory");
      if (!ok) request<N>(r, off, ws);
      ok = all_fresh<N>(r, t);
    } while (!__all(ok) && ++spin < SPIN_CAP);
  }
  asm volatile("" ::: "memory");
}
__device__ __forceinline__ unsigned lds_word(LAS unsigned* c) { return uni(__hip_atomic_load(c, __ATOMIC_RELAXED, __HIP_MEMORY_SCOPE_WORKGROUP)); }
__device__ __forceinline__ void lds_arrive(LAS unsigned* c, int lane) {
  asm volatile("s_waitcnt lgkmcnt(0)" ::: "memory");
  if (lane == 0) __hip_atomic_fetch_add(c, 1u, __ATOMIC_RELAXED, __HIP_MEMORY_SCOPE_WORKGROUP);
}
template <int SLEEP = 1> __device__ __forceinline__ void lds_wait(LAS unsigned* c, unsigned n) {
#pragma nounroll
  for (unsigned spin = 0; spin < (SPIN_CAP << 4); ++spin) {
    if (lds_word(c) >= n) break;
    if (SLEEP) __builtin_amdgcn_s_sleep(SLEEP);
  }
  asm volatile("" ::: "memory");
}

__device__ __forceinline__ float half_sum(float a, float b) {
  float x = sw32_add(a, b);
  x += dppf<XOR1>(x); x += dppf<XOR2>(x); x += dppf<HMIR>(x); x += dppf<ROR8>(x); return sw16_add(x, x); }
__device__ __forceinline__ void layer_norm(float (&u)[8], float (&v)[8]) {
  const float su = ((u[0] + u[1]) + (u[2] + u[3])) + ((u[4] + u[5]) + (u[6] + u[7]));
  const float sv = ((v[0] + v[1]) + (v[2] + v[3])) + ((v[4] + v[5]) + (v[6] + v[7]));
  const float mf = half_sum(su, sv) * (1.f / D);
  const float mu = asf(__builtin_amdgcn_readlane(asu(mf), 0)), mv = asf(__builtin_amdgcn_readlane(asu(mf), 32));
  float qu = 0.f, qv = 0.f;
#pragma unroll
  for (int j = 0; j < 8; ++j) { const float a = u[j] - mu, b = v[j] - mv; qu = fmaf(a, a, qu); qv = fmaf(b, b, qv); }
  const float var = rbf(half_sum(qu, qv) * (1.f / D));
  const float rs = rcpf_(rbf(__builtin_amdgcn_sqrtf(rbf(var + 1e-5f))));
  const float ru = asf(__builtin_amdgcn_readlane(asu(rs), 0)), rv = asf(__builtin_amdgcn_readlane(asu(rs), 32)), m0 = rbf(mu), m1 = rbf(mv);
#pragma unroll
  for (int j = 0; j < 8; ++j) { u[j] = rbf(rbf(u[j] - m0) * ru); v[j] = rbf(rbf(v[j] - m1) * rv); }
}

__device__ __forceinline__ v4u column_frag(const v2u (&w)[8], int j) {
  v4u a;
#pragma unroll
  for (int t = 0; t < 4; ++t) a[t] = (j & 1) ? perm_hi(w[2 * t][j >> 1], w[2 * t + 1][j >> 1]) : perm_lo(w[2 * t][j >> 1], w[2 * t + 1][j >> 1]);
  return a;
}
__device__ __forceinline__ v4u masked(v4u v, unsigned m) { return v4u{v[0] & m, v[1] & m, v[2] & m, v[3] & m}; }
__device__ __forceinline__ bool sum_products(v4f p, float* buf, int a, LAS unsigned* c, int lane, v2f& s) {
  if ((lane & 15) < 8) *(v4f*)(buf + a * PW + ((lane >> 2) & 1) * 96 + (lane & 3) * 24 + (lane >> 4) * 4) = p;
  asm volatile("s_waitcnt lgkmcnt(0)" ::: "memory");
  unsigned old = 0;
  if (lane == 0) old = __hip_atomic_fetch_add(c, 1u, __ATOMIC_RELAXED, __HIP_MEMORY_SCOPE_WORKGROUP);
  if (uni(old) != 3u) return false;
  asm volatile("" ::: "memory");
  const float* q = buf + (lane >> 5) * 96 + (lane & 1) * 48 + ((lane & 31) >> 1);
  s = (v2f{q[0], q[24]} + v2f{q[PW], q[PW + 24]}) + (v2f{q[2 * PW], q[2 * PW + 24]} + v2f{q[3 * PW], q[3 * PW + 24]});
  return true;
}

__global__ void __launch_bounds__(768, 3)
k_fused(const void*, const void*, const void*, const void*, const void*, const void*, const void*, const void*, const void*, void*, void*) {
  v16u ka; v4u kb; v2u kc2; u64 t_wave;
  asm volatile("s_memrealtime %3\n\ts_load_dwordx16 %0, %4, 0x0\n\ts_load_dwordx4 %1, %4, 0x40\n\ts_load_dwordx2 %2, %4, 0x50"
               : "=&s"(ka), "=&s"(kb), "=&s"(kc2), "=&s"(t_wave) : "s"(__builtin_amdgcn_kernarg_segment_ptr()));
  SB();
  const int tid = threadIdx.x, lane = tid & 63, wv = uni(tid >> 6);
  const int blk = blockIdx.x, x8 = blk & 7, i32 = blk >> 3, rg = i32 >> 3, k8 = i32 & 7, bat = i32 >> 4;
  const int g = lane >> 4, c16 = lane & 15, hg = x8 * 4 + rg, w = wv - 4;
  int kvbase = ((bat * HKV + x8) * SK + (i32 & 15) * 512) * 256 + w * 1024;
  int koff = ((lane >> 2) & 3) * 256 + g * 64 + (lane & 3) * 16;
  asm volatile("s_waitcnt lgkmcnt(0)" : "+s"(ka), "+s"(kb), "+s"(kc2), "+s"(t_wave), "+s"(kvbase), "+v"(koff));
#define ARG(v, i) ((void*)(((u64)uni((v)[2 * (i) + 1]) << 32) | uni((v)[2 * (i)])))
  const void *x = ARG(ka, 0), *kc = ARG(ka, 1), *vc = ARG(ka, 2), *wq = ARG(ka, 3), *wo = ARG(ka, 4), *w1 = ARG(ka, 5), *b1 = ARG(ka, 6), *w2 = ARG(ka, 7),
             *b2 = ARG(kb, 0);
  void *out = ARG(kb, 1), *wsp = ARG(kc2, 0);
  __shared__ __attribute__((aligned(16))) float lds[L_END];
  LAS unsigned* cnt = (LAS unsigned*)(lds + L_CNT);
  const int id2 = x8 * 14 + i32 - 18, rb = id2 >> 3, c2 = id2 & 7;
  const bool isq = i32 < 16, big = i32 < 23, is2 = i32 >= 18;
  const rsrc_t rk = mk(kc, KV_BYTES);
  v4u K[16];
  int wv2 = wv;
  asm volatile("" : "+s"(wv2));
#pragma unroll
  for (int n = 0; n < KFRONT; ++n) asm volatile("" : "=v"(K[n]));
  if (wv2 >= 4) {
    K[0] = ld16<AUX_NT>(rk, koff, kvbase);
    SB();
    if (!big) {
#pragma unroll
      for (int n = 1; n < KFRONT; ++n) { K[n] = ld16<AUX_NT>(rk, koff, kvbase + n * 8192); SB(); }
    }
  } else if (tid < 12) {
    cnt[tid] = tid == W_T0 ? (unsigned)t_wave : tid == W_SHIFT ? NO_SHIFT : 0u;
  }
  SB();
  u64 pc;
  int code0, code1;
  asm volatile("s_getpc_b64 %0\n.Lk_fused_pc:\n\ts_mov_b32 %1, .Lk_fused_end0-.Lk_fused_pc\n\ts_mov_b32 %2, .Lk_fused_end1-.Lk_fused_pc"
               : "=s"(pc), "=s"(code0), "=s"(code1));
  LDS_BARRIER();
  const unsigned t_in = uni(cnt[W_T0]);
  const int lb = (lane >> 2) & 1;

  if (wv >= 4) {
    const rsrc_t rv = mk(vc, KV_BYTES);
    const int voff = lane * 16, stag = (7 - w) * STAG;
    v4u V[16];
    int tau = (TLAST - (big ? TBIG : TBASE)) * 16 / (32 - KFRONT), base = TLAST * 16 - 31 * tau;
#define PACE(n) do { UNTIL(t_in, ((base + (n) * tau) >> 4) - stag); VMWAIT(WINB - 1); if (!big) VMWAIT(WIN - 1); SB(); } while (0)
#define REPLAN(n, sh) do { const int end_ = (TLAST + min(max(sh, -400), 400)) * 16; tau = (end_ - base - ((n) - 1) * tau) / (32 - (n)); base = end_ - 31 * tau; } while (0)
    if (big) VMWAIT(0); else VMWAIT(KFRONT - 1);
    SB();
    int late = (int)(now() - t_in);
    late = late > T1THR ? min(late - (late < 180 ? T1NOM : T1NOM + 15), 400) : 0;
    base += late * 16;
    if (tid == 256) cnt[W_LATE] = (unsigned)(late + 1);
    if (big) {
      UNTIL(t_in, TBIG + late);
#pragma unroll
      for (int n = 1; n < KFRONT; ++n) { K[n] = ld16<AUX_NT>(rk, koff, kvbase + n * 8192); SB(); }
    }
#pragma unroll
    for (int n = KFRONT; n < QPOS; ++n) {
      if (n == NSH) { const unsigned sh = lds_word(cnt + W_SHIFT); if (sh != NO_SHIFT) REPLAN(n, (int)sh); }
      if (n < QPOS - BURST) PACE(n);
      if (n < 16) K[n] = ld16<AUX_NT>(rk, koff, kvbase + n * 8192);
      else V[n - 16] = ld16<AUX_NT>(rv, voff, kvbase + (n - 16) * 8192);
      SB();
    }
    lds_wait(cnt + C_Q, 3u);
    const int bank = (lane >> 2) & 3, hh = lane & 3;
    const v4u qf = *(const v4u*)(lds + L_Q + hh * 64 + g * 16 + bank * 4);
    const v4u lt = *(const v4u*)(lds + L_CNT + W_TAGA);
    v4f d[16];
#pragma unroll
    for (int n = 0; n < 16; ++n) d[n] = mfma32(K[n], qf, v4f{0.f, 0.f, 0.f, 0.f});
    SB();
    float S[4], sn[16];
#pragma unroll
    for (int n = 0; n < 16; ++n) { d[n][0] += dppf<SHL4>(d[n][1]); d[n][2] += dppf<SHL4>(d[n][3]); }
    SB();
#pragma unroll
    for (int n = 0; n < 16; ++n) sn[n] = d[n][0] + dppf<SHL8>(d[n][2]);
    SB();
#pragma unroll
    for (int z = 0; z < 4; ++z) S[z] = to_bank<1>(sn[4 * z], sn[4 * z + 1]);
#pragma unroll
    for (int z = 0; z < 4; ++z) S[z] = to_bank<2>(S[z], sn[4 * z + 2]);
#pragma unroll
    for (int z = 0; z < 4; ++z) S[z] = to_bank<3>(S[z], sn[4 * z + 3]);
    SB();
    float mw = fmaxf(fmaxf(S[0], S[1]), fmaxf(S[2], S[3]));
    mw = fmaxf(mw, dppf<ROR4>(mw)); mw = fmaxf(mw, dppf<ROR8>(mw)); mw = sw32_max(sw16_max(mw));
    mw *= SC2;
    float lw = 0.f;
#pragma unroll
    for (int z = 0; z < 4; ++z) { S[z] = exp2f_(fmaf(S[z], SC2, -mw)); lw += S[z]; }
    lw += dppf<ROR4>(lw); lw += dppf<ROR8>(lw); lw = sw16_add(lw, lw); lw = sw32_add(lw, lw);
    if (lane < 4) *(v2f*)(lds + L_ML + lane * 16 + w * 2) = v2f{mw, lw};
    lds_arrive(cnt + C_ML, lane);
    REPLAN(QPOS, (int)uni(lt.z));
    unsigned P[4], bm[4];
#pragma unroll
    for (int z = 0; z < 4; ++z) { P[z] = pk(S[z], dppf<SHL4>(S[z])); bm[z] = bank == z ? 0xffffffffu : 0u; }
    v4u Bf[2][4];
#pragma unroll
    for (int be = 0; be < 2; ++be) {
      const unsigned p0 = P[2 * be], p1 = P[2 * be + 1];
      Bf[be][0] = masked(v4u{p0, dppu<ROR8>(p0), p1, dppu<ROR8>(p1)}, bm[0]);
      Bf[be][1] = masked(v4u{dppu<ROR4>(p0), dppu<ROR12>(p0), dppu<ROR4>(p1), dppu<ROR12>(p1)}, bm[1]);
      Bf[be][2] = masked(v4u{dppu<ROR8>(p0), p0, dppu<ROR8>(p1), p1}, bm[2]);
      Bf[be][3] = masked(v4u{dppu<ROR12>(p0), dppu<ROR4>(p0), dppu<ROR12>(p1), dppu<ROR4>(p1)}, bm[3]);
    }
    SB();
    v4f acc[2] = {v4f{0.f, 0.f, 0.f, 0.f}, v4f{0.f, 0.f, 0.f, 0.f}};
#pragma unroll
    for (int be = 0; be < 2; ++be) {
#pragma unroll
      for (int n = be ? PV0 : QPOS; n < (be ? 32 : PV0); ++n) { PACE(n); V[n - 16] = ld16<AUX_NT>(rv, voff, kvbase + (n - 16) * 8192); SB(); }
      if (be) lds_arrive(cnt + C_ISSUED, lane);
      v4u A[8];
#pragma unroll
      for (int t = 0; t < 4; ++t) {
#pragma unroll
        for (int u = 0; u < 4; ++u) {
          A[2 * u][t] = perm_lo(V[8 * be + 2 * t][u], V[8 * be + 2 * t + 1][u]);
          A[2 * u + 1][t] = perm_hi(V[8 * be + 2 * t][u], V[8 * be + 2 * t + 1][u]);
        }
        SB();
      }
#pragma unroll
      for (int u = 0; u < 4; ++u) { acc[0] = mfma32(A[2 * u], Bf[be][u], acc[0]); acc[1] = mfma32(A[2 * u + 1], Bf[be][u], acc[1]); }
      SB();
    }
    *(v4f*)(lds + L_O + w * 512 + lane * 4) = acc[0];
    *(v4f*)(lds + L_O + w * 512 + 256 + lane * 4) = acc[1];
    LDS_BARRIER();
    if (w == 7) {
      const unsigned t_ba = now();
      const Tag tag = {uni(lt.x), uni(lt.y)};
      const rsrc_t rws = mk(wsp, WS_END);
      const int gq = (i32 >> 2) & 3, jq = i32 & 3, fcol = x8 * 64 + gq * 16 + g * 4 + jq;
      const unsigned xo = __builtin_amdgcn_raw_buffer_load_b16(mk(x, 2 * D * 2), fcol * 2, bat * D * 2, 0);
      const unsigned bias2 = __builtin_amdgcn_raw_buffer_load_b16(mk(b2, D * 2), fcol * 2, 0, 0);
      SB();
      v4u fg[1], fs[2];
      const int go[1] = {blk_at<1024>(R_GP, (c16 < 14 ? c16 : 0) * 8 + x8) + (bat * 32 + gq * 8 + g * 2 + (jq >> 1)) * 16};
      int so[2] = {blk_at<512>(R_SP, lane * 8 + x8) + (bat * 16 + gq * 4 + jq) * 16, blk_at<512>(R_SP, (lane + 64) * 8 + x8) + (bat * 16 + gq * 4 + jq) * 16};
      UNTIL(t_ba, GDELTA);
      request<1>(fg, go, rws);
      await<1, 6>(fg, go, rws, tag);
      float gs = c16 < 14 ? asf((jq & 1) ? fg[0].z : fg[0].x) : 0.f;
      gs += dppf<XOR1>(gs); gs += dppf<XOR2>(gs); gs += dppf<HMIR>(gs); gs += dppf<ROR8>(gs);
      const float mv = rbf(rbf(gs) + lo16(bias2));
      float gv = rbf(mv * rcpf_(1.f + exp2f_(-LOG2E * mv))), xv = lo16(xo);
      int ooff = c16 == 0 ? (bat * D + fcol) * 2 : 2 * D * 2;
      asm volatile("" : "+v"(gv), "+v"(xv), "+v"(ooff), "+v"(so[0]), "+v"(so[1]));
      lds_wait<0>(cnt + C_SP, 4u);
      const unsigned t_sp = now();
      UNTIL(t_sp, FDELTA);
      UNTIL(t_in, TLAST + TFINAL + (int)uni(lt.z));
      request<2>(fs, so, rws);
      await<2, 0>(fs, so, rws, tag);
      float sc = sw16_add(sw32_add(lo16(fs[0].x) + lo16(fs[1].x), lo16(fs[0].z) + lo16(fs[1].z)),
                          sw32_add(hi16(fs[0].x) + hi16(fs[1].x), hi16(fs[0].z) + hi16(fs[1].z)));
      sc += dppf<XOR1>(sc); sc += dppf<XOR2>(sc); sc += dppf<HMIR>(sc); sc += dppf<ROR8>(sc);
      const float ov = xv + rbf(sc * gv);
      __builtin_amdgcn_raw_buffer_store_b16((unsigned short)pk(ov, ov), mk(out, 2 * D * 2), ooff, 0, 0);
      if (blk == 0 && lane == 0) __builtin_amdgcn_raw_buffer_store_b64(v2u{tag.a, tag.b}, rws, 0, WS_E, AUX_SC1);
      asm volatile(".Lk_fused_end0:");
    }
    return;
  }

  const int a = wv;
  __builtin_amdgcn_s_setprio(2);
  const rsrc_t rws = mk(wsp, WS_END);
  unsigned mj[4];
#pragma unroll
  for (int j = 0; j < 4; ++j) mj[j] = (lane & 11) == j ? 0xffffffffu : 0u;
  const v2u ep = ld8<AUX_SC1>(rws, 0, WS_E);
  unsigned touch = 0;
  if (a == 3 && i32 == 31) {
    const char* tp = lane < 2 ? (const char*)vc + (size_t)(lane * HKV + x8) * (SK * 256) + 65536
                   : lane == 2 ? (const char*)wo + x8 * 512 * 1024 + 4096
                   : lane < 5 ? (const char*)w2 + ((x8 * 14 + (lane - 3) * 13) >> 3) * 131072 + x8 * 128
                   : lane == 5 ? (const char*)b1 : (const char*)b2;
    if (lane < 7) touch = *(const GAS unsigned*)tp;
  }
  Tag tag;
  if (big) {
    const rsrc_t rx = mk(x, 2 * D * 2);
    const v4u xa0 = ld16<0>(rx, lane * 16, 0), xa1 = ld16<0>(rx, lane * 16, D * 2);
    const int ld = isq ? HQ * DH * 2 : F * 2, idb = isq ? x8 * 16 + k8 * 2 + rg : x8 * 7 + i32 - 16, row0 = (idb & 1) * 256 + a * 64;
    const rsrc_t rw = mk(isq ? wq : w1, D * ld);
    v2u wA[2][8];
    const int woff = g * 8 * ld + c16 * 8;
#pragma unroll
    for (int t = 0; t < 2; ++t)
#pragma unroll
      for (int i = 0; i < 8; ++i) wA[t][i] = ld8<AUX_NT>(rw, woff, (row0 + t * 32 + i) * ld + (idb >> 1) * 128);
    SB();
    tag = Tag{uni(ep.x) + 1u, uni(ep.y) + 0x9E3779B9u};
    float u[8], v[8];
#pragma unroll
    for (int t = 0; t < 4; ++t) { u[2 * t] = lo16(xa0[t]); u[2 * t + 1] = hi16(xa0[t]); v[2 * t] = lo16(xa1[t]); v[2 * t + 1] = hi16(xa1[t]); }
#pragma nounroll
    for (int pass = isq ? 1 : 0; pass < 2; ++pass) layer_norm(u, v);
    float* xs = lds + L_X + a * 512;
    *(v4u*)(xs + lane * 4) = v4u{pk(u[0], u[1]), pk(u[2], u[3]), pk(u[4], u[5]), pk(u[6], u[7])};
    *(v4u*)(xs + 256 + lane * 4) = v4u{pk(v[0], v[1]), pk(v[2], v[3]), pk(v[4], v[5]), pk(v[6], v[7])};
    asm volatile("s_waitcnt lgkmcnt(0)" ::: "memory");
    v4f acc[4];
#pragma unroll
    for (int t = 0; t < 2; ++t) {
      const v4u in = *(const v4u*)(xs + lb * 256 + (row0 >> 1) + (t * 4 + g) * 4);
#pragma unroll
      for (int j = 0; j < 4; ++j) acc[j] = mfma32(column_frag(wA[t], j), in, t ? acc[j] : v4f{0.f, 0.f, 0.f, 0.f});
    }
    v2f sum;
    if (sum_products((lane & 2) ? ((lane & 1) ? acc[3] : acc[2]) : ((lane & 1) ? acc[1] : acc[0]), lds + L_R1, a, cnt + C_AUX, lane, sum)) {
      const unsigned pp = pk(sum.x, sum.y);
      st16(rec(pp, dppu<XOR1>(pp), tag), rws, (lane & 1) ? WS_END : lane * 8, blk_at<512>(isq ? R_QP : R_HP, idb));
    }
  } else {
    tag = Tag{uni(ep.x) + 1u, uni(ep.y) + 0x9E3779B9u};
  }
  if (a == 3 && blk >= 248) {
    lds_wait(cnt + W_LATE, 1u);
    const unsigned ts = t_in + lds_word(cnt + W_LATE);
    if (lane == 0) st16(rec(ts, ts, tag), rws, x8 * 16, WS_T0);
  }
  const unsigned code_pf = __builtin_amdgcn_raw_buffer_load_b32(mk((const void*)(pc & ~(u64)127), max(code0, code1) + (int)(pc & 127)), lane * 128, (a & 1) * 8192, 0);
  if (a < 2) {
    v4u qr[2];
    const int qo[2] = {blk_at<512>(R_QP, (x8 * 8 + a * 4 + g) * 2) + (bat * 16 + c16) * 16, blk_at<512>(R_QP, (x8 * 8 + a * 4 + g) * 2 + 1) + (bat * 16 + c16) * 16};
    UNTIL(t_in, QWAIT + lds_word(cnt + W_LATE));
    request<2>(qr, qo, rws);
    await<2, 16>(qr, qo, rws, tag);
    *(v2u*)(lds + L_Q + (a * 64 + lane) * 2) = v2u{pk(lo16(qr[0].x) + lo16(qr[1].x), hi16(qr[0].x) + hi16(qr[1].x)),
                                                   pk(lo16(qr[0].z) + lo16(qr[1].z), hi16(qr[0].z) + hi16(qr[1].z))};
    lds_arrive(cnt + C_Q, lane);
  }
  if (a == 2) {
    v4u tr[1];
    const int to[1] = {WS_T0 + (lane & 7) * 16};
    UNTIL(t_in, TWAIT + lds_word(cnt + W_LATE));
    request<1>(tr, to, rws);
    await<1, 16>(tr, to, rws, tag);
    int sh = min(max((int)(tr[0].x - t_in), -400), 400), mx = sh;
    sh += (int)dppu<XOR1>(sh); sh += (int)dppu<XOR2>(sh); sh += (int)dppu<HMIR>(sh);
    mx = max(mx, (int)dppu<XOR1>(mx)); mx = max(mx, (int)dppu<XOR2>(mx)); mx = max(mx, (int)dppu<HMIR>(mx));
    sh = max(sh >> 3, mx - GAIN);
    if (lane == 0) *(v4u*)(lds + L_CNT + W_TAGA) = v4u{tag.a, tag.b, (unsigned)sh, (unsigned)mx};
    lds_arrive(cnt + C_Q, lane);
  }
  asm volatile("" :: "v"(code_pf), "v"(touch));
  v2f gpart = {0.f, 0.f};
  bool gpub = false;
  if (is2) {
    const rsrc_t r2 = mk(w2, D * F * 2);
    v2u wB[8];
    v4u hr[2];
    const int hoff = (lb * 16 + (a & 1) * 8 + g * 2 + ((lane >> 3) & 1)) * 16;
    const int ho[2] = {blk_at<512>(R_HP, (2 * rb + (a >> 1)) * 2) + hoff, blk_at<512>(R_HP, (2 * rb + (a >> 1)) * 2 + 1) + hoff};
    lds_wait(cnt + C_Q, 3u);
    UNTIL(t_in, HWAIT + (int)lds_word(cnt + W_LATEST));
    request<2>(hr, ho, rws);
    const v2u bias1 = ld8<0>(mk(b1, F * 2), g * 16 + ((lane >> 3) & 1) * 8, (rb * 128 + a * 32) * 2);
#pragma unroll
    for (int i = 0; i < 8; ++i) wB[i] = ld8<AUX_NT>(r2, g * 8 * 1024 + c16 * 8 + (i & 3) * 1024, (rb * 128 + a * 32 + (i & 4)) * 1024 + c2 * 128);
    SB();
    await<2, 16>(hr, ho, rws, tag);
    float h[4] = {lo16(hr[0].x) + lo16(hr[1].x), hi16(hr[0].x) + hi16(hr[1].x), lo16(hr[0].z) + lo16(hr[1].z), hi16(hr[0].z) + hi16(hr[1].z)};
    const float bb[4] = {lo16(bias1.x), hi16(bias1.x), lo16(bias1.y), hi16(bias1.y)};
    constexpr float G0 = -2.f * 0.7978845608028654f * LOG2E, G1 = G0 * 0.044715f;
#pragma unroll
    for (int j = 0; j < 4; ++j) {
      const float t = rbf(rbf(h[j]) + bb[j]);
      h[j] = t * rcpf_(1.f + exp2f_(t * fmaf(G1, t * t, G0)));
    }
    const unsigned h0 = pk(h[0], h[1]), h1 = pk(h[2], h[3]);
    const v4u in = {h0, h1, dppu<ROR8>(h0), dppu<ROR8>(h1)};
    v4f p = {0.f, 0.f, 0.f, 0.f};
#pragma unroll
    for (int j = 0; j < 4; ++j) p = mfma32(column_frag(wB, j), masked(in, mj[j]), p);
    gpub = sum_products(p, lds + L_R2, a, cnt + C_G, lane, gpart);
  }
  lds_wait(cnt + C_ML, 8u);
  if (a == 3 && lane < 32) ((unsigned*)lds)[L_ON + lane] = NOT_YET;
  const int mh = (lane >> 1) & 3;
  float fw[8];
  {
    v4f ml[4];
#pragma unroll
    for (int t = 0; t < 4; ++t) ml[t] = *(const v4f*)(lds + L_ML + mh * 16 + 4 * t);
    const float M = fmaxf(fmaxf(fmaxf(ml[0][0], ml[0][2]), fmaxf(ml[1][0], ml[1][2])), fmaxf(fmaxf(ml[2][0], ml[2][2]), fmaxf(ml[3][0], ml[3][2])));
    float L = 0.f;
#pragma unroll
    for (int t = 0; t < 4; ++t) { fw[2 * t] = exp2f_(ml[t][0] - M); fw[2 * t + 1] = exp2f_(ml[t][2] - M); L = fmaf(fw[2 * t], ml[t][1], fmaf(fw[2 * t + 1], ml[t][3], L)); }
    const float il = rcpf_(L);
#pragma unroll
    for (int t = 0; t < 8; ++t) fw[t] *= il;
    const float lse = M + __builtin_amdgcn_logf(L);
    asm volatile("" : "+v"(fw[0]), "+v"(fw[1]), "+v"(fw[2]), "+v"(fw[3]), "+v"(fw[4]), "+v"(fw[5]), "+v"(fw[6]), "+v"(fw[7]));
    if (a == 0 && lane < 8) st16(rec(asu(lse), asu(lse), tag), rws, lane * 16, blk_at<128>(R_LS, x8 * 32 + i32));
  }
  const int dq = k8 >> 1, ch = k8 & 1;
  v4u lr[1], pr[2];
  const int lo[1] = {blk_at<128>(R_LS, x8 * 32 + (a & 1) * 16 + (lane >> 3) + (lane & 1) * 8) + rg * 32};
  int po[2] = {blk_at<512>(R_R1, hg * 32 + (a & 1) * 16 + (lane >> 3)) + dq * 128 + (lane & 7) * 16,
               blk_at<512>(R_R1, hg * 32 + (a & 1) * 16 + (lane >> 3) + 8) + dq * 128 + (lane & 7) * 16};
  int on_w = (L_ON + a * 16 + (lane & 7) * 2 + (lane >> 5)) * 4, on_r = (L_ON + lb * 16 + g * 4) * 4;
  int soff = c16 < 8 ? blk_at<512>(R_SP, (hg * 4 + dq) * 8 + ch * 4 + a) + (lb * 16 + g * 4 + (lane & 3)) * 16 : WS_END;
  int mrd = (L_O + (a * 16 + ((lane >> 3) & 3) * 4 + mh) * 4 + (lane >> 5) * 2 + (lane & 1)) * 4;
  int moff = (lane & 8) ? WS_END : blk_at<512>(R_R1, (x8 * 4 + mh) * 32 + i32) + (a * 8 + ((lane >> 5) * 2 + (lane & 1)) * 2 + ((lane >> 4) & 1)) * 16;
  asm volatile("" : "+v"(moff), "+v"(mrd), "+v"(po[0]), "+v"(po[1]), "+v"(on_w), "+v"(on_r), "+v"(soff), "+v"(mj[0]), "+v"(mj[1]), "+v"(mj[2]), "+v"(mj[3]));
  SB();
  const rsrc_t ro = mk(wo, D * HQ * DH * 2), rgather = mk(wsp, a < 2 ? WS_END : 0);
  const int shift = (int)lds_word(cnt + W_SHIFT);
  v2u wO[8];
  lds_wait(cnt + C_ISSUED, 8u);
  UNTIL(t_in, TLAST + WOD0 + shift);
  request<1>(lr, lo, rgather);
#pragma unroll
  for (int i = 0; i < 8; ++i) {
    if (i == 4) UNTIL(t_in, TLAST + WOD1 + shift);
    wO[i] = ld8<AUX_NT>(ro, g * 8 * 1024 + c16 * 8 + (i & 3) * 1024, (hg * 128 + dq * 32 + (i & 4)) * 1024 + ch * 512 + a * 128);
    SB();
  }
  LDS_BARRIER();
  {
    const LAS float* mp = (const LAS float*)((LAS char*)lds + mrd);
    v2f t[8];
#pragma unroll
    for (int ww = 0; ww < 8; ++ww) t[ww] = v2f{mp[ww * 512], mp[ww * 512 + 256]};
    const v2f s = (fw[0] * t[0] + fw[1] * t[1] + fw[2] * t[2] + fw[3] * t[3]) + (fw[4] * t[4] + fw[5] * t[5] + fw[6] * t[6] + fw[7] * t[7]);
    const unsigned p = pk(s.x, s.y);
    st16(rec(p, dppu<ROR8>(p), tag), rws, moff, 0);
    if (gpub) st16(rec(asu(gpart.x), asu(gpart.y), tag), rws, lane * 16, blk_at<1024>(R_GP, rb * 8 + c2));
  }
  SB();
  const unsigned t_pub = now();
  float wt0 = 0.f, wt1 = 0.f;
  if (a < 2) {
    await<1, 6>(lr, lo, rws, tag);
    const float ls = asf(lr[0].x);
    float M = fmaxf(ls, dppf<XOR1>(ls));
    M = fmaxf(M, dppf<ROR8>(M)); M = sw32_max(sw16_max(M));
    const float e = exp2f_(ls - M), ex = dppf<XOR1>(e);
    float z = e + ex;
    z += dppf<ROR8>(z); z = sw16_add(z, z); z = sw32_add(z, z);
    const float iz = rcpf_(z);
    wt0 = ((lane & 1) ? ex : e) * iz; wt1 = ((lane & 1) ? e : ex) * iz;
    asm volatile("" : "+v"(wt0), "+v"(wt1));
    UNTIL(t_pub, DELTA);
  }
  asm volatile("" :: "v"(lr[0]));
  SB();
  request<2>(pr, po, rgather);
  SB();
  v4u wf[4];
#pragma unroll
  for (int j = 0; j < 4; ++j) wf[j] = column_frag(wO, j);
  asm volatile("" : "+v"(wf[0]), "+v"(wf[1]), "+v"(wf[2]), "+v"(wf[3]));
  if (a < 2) {
    await<2, 0>(pr, po, rws, tag);
    const float o0 = fmaf(wt0, lo16(pr[0].x), wt1 * lo16(pr[1].x)), o1 = fmaf(wt0, hi16(pr[0].x), wt1 * hi16(pr[1].x)),
                o2 = fmaf(wt0, lo16(pr[0].z), wt1 * lo16(pr[1].z)), o3 = fmaf(wt0, hi16(pr[0].z), wt1 * hi16(pr[1].z));
    float oo = sw16_add(sw32_add(o0, o2), sw32_add(o1, o3));
    oo += dppf<ROR8>(oo);
    const auto t = __builtin_amdgcn_permlane16_swap(asu(oo), asu(oo), false, false);
    const unsigned ob = pk(asf(t[0]), asf(t[1]));
    *(LAS unsigned*)((LAS char*)lds + on_w) = ob == NOT_YET ? 0x7fc07fc0u : ob;
  }
  asm volatile("" :: "v"(pr[0]), "v"(pr[1]));
  v4u in;
#pragma nounroll
  for (unsigned spin = 0; spin < (SPIN_CAP << 6); ++spin) {
    asm volatile("" ::: "memory");
    in = *(const LAS v4u*)((LAS char*)lds + on_r);
    if (__all(max(max(in[0], in[1]), max(in[2], in[3])) != NOT_YET)) break;
  }
  v4u bj[4];
#pragma unroll
  for (int j = 0; j < 4; ++j) bj[j] = masked(in, mj[j]);
  SB();
  v4f sp = {0.f, 0.f, 0.f, 0.f};
#pragma unroll
  for (int j = 0; j < 4; ++j) sp = mfma32(wf[j], bj[j], sp);
  st16(rec(pk(sp[0], sp[1]), pk(sp[2], sp[3]), tag), rws, soff, 0);
  lds_arrive(cnt + C_SP, lane);
  asm volatile(".Lk_fused_end1:");
}
}

extern "C" void kernel_launch(void* const* d_in, const int* in_sizes, int n_in, void* d_out, int out_size, void* d_ws, size_t ws_size,
                              hipStream_t stream) {
  fused::k_fused<<<256, 768, 0, stream>>>(d_in[0], d_in[1], d_in[2], d_in[3], d_in[4], d_in[6], d_in[7], d_in[8], d_in[9], d_out, d_ws);
}
```
